```python
import torch

def _softmax(x, axis=-1):
    return torch.softmax(x, dim=axis)
def _gelu(x):
    return torch.nn.functional.gelu(x, approximate="tanh")
def _silu(x):
    return torch.nn.functional.silu(x)
def _sigmoid(x):
    return torch.sigmoid(x)
def _layernorm(x, axis=-1, eps=1e-5):
    m = x.mean(dim=axis, keepdim=True)
    v = x.var(dim=axis, keepdim=True, unbiased=False)
    return (x - m) / torch.sqrt(v + eps)
def _scatter_add(tgt, idx, src, axis=0):
    return tgt.clone().index_add_(axis, idx, src)
def _segment_sum(src, seg, n):
    out = torch.zeros((n,) + src.shape[1:], dtype=src.dtype)
    return out.index_add_(0, seg, src)
def _pad(x, pw):
    flat = tuple(v for pair in reversed(pw) for v in pair)
    return torch.nn.functional.pad(x, flat)

kernel_name = 'standalone__mha_block_cached__near_ridge'

def setup_inputs():

    _w = (lambda a: (lambda t: t.to(torch.bfloat16) if t.is_floating_point() else t)(torch.as_tensor(a)))
    B, Sq, D = 16, 192, 1024
    H, Sk, Dh = 32, 1024, 128
    return {
        "x": _w((lambda *s: torch.randn(*s, dtype=torch.bfloat16) * 0.8)(B, Sq, D)),
        "k_cache": _w((lambda *s: torch.randn(*s, dtype=torch.bfloat16) * 0.8)(B, H, Sk, Dh)),
        "v_cache": _w((lambda *s: torch.randn(*s, dtype=torch.bfloat16) * 0.05)(B, H, Sk, Dh)),
        "wq": _w((lambda *s: torch.randn(*s, dtype=torch.bfloat16) * 0.8)(D, H * Dh) * (D ** -0.5)),
        "wo": _w((lambda *s: torch.randn(*s, dtype=torch.bfloat16) * 0.05)(H * Dh, D) * ((H * Dh) ** -0.5)),
    }

def reference(x, k_cache, v_cache, wq, wo):
    B, Sq, D = x.shape
    H, Dh = 32, 128
    q = (lambda x, *d: x.permute(*d))((x @ wq).reshape(B, Sq, H, Dh), 0, 2, 1, 3)
    scores = torch.einsum("bhqd,bhkd->bhqk", q, k_cache) * (Dh ** -0.5)
    p = _softmax(scores, axis=-1)
    o = torch.einsum("bhqk,bhkd->bhqd", p, v_cache)
    return (lambda x, *d: x.permute(*d))(o, 0, 2, 1, 3).reshape(B, Sq, H * Dh) @ wo
```

```cpp
#include <hip/hip_runtime.h>
#include <hip/hip_bf16.h>
typedef __attribute__((ext_vector_type(8))) short bf16x8;
typedef __attribute__((ext_vector_type(16))) float f32x16;
typedef unsigned short u16;
__device__ __forceinline__ float bf2f(u16 x){ return __uint_as_float(((unsigned)x)<<16); }
__device__ __forceinline__ u16 f2bf(float x){ unsigned u=__float_as_uint(x); u += 0x7fffu + ((u>>16)&1u); return (u16)(u>>16); }
constexpr int B_=16, SQ=192, D_=1024, H_=32, SK=1024, DH=128;

__global__ void transp(const u16* __restrict__ in, u16* __restrict__ out, int R, int C){
  __shared__ u16 t[32][33];
  int c0=blockIdx.x*32, r0=blockIdx.y*32; int tx=threadIdx.x&31, ty=threadIdx.x>>5;
  for(int i=ty;i<32;i+=8) t[i][tx]=in[(long)(r0+i)*C+c0+tx];
  __syncthreads();
  for(int i=ty;i<32;i+=8) out[(long)(c0+i)*R+r0+tx]=t[tx][i];
}
__global__ __launch_bounds__(256) void gemm_simple(const u16* __restrict__ A, const u16* __restrict__ Bt, u16* __restrict__ C, int M, int N, int K){
  int wid=threadIdx.x>>6, lane=threadIdx.x&63, r=lane&31, h=lane>>5;
  int m0=blockIdx.y*128+(wid>>1)*64, n0=blockIdx.x*128+(wid&1)*64;
  f32x16 acc[2][2]={};
  for(int k=0;k<K;k+=16){
    bf16x8 a[2],b[2];
    for(int i=0;i<2;++i){ a[i]=*(const bf16x8*)(A+(long)(m0+i*32+r)*K+k+8*h); b[i]=*(const bf16x8*)(Bt+(long)(n0+i*32+r)*K+k+8*h);}
    for(int i=0;i<2;++i)for(int j=0;j<2;++j) acc[i][j]=__builtin_amdgcn_mfma_f32_32x32x16_bf16(a[i],b[j],acc[i][j],0,0,0);
  }
  for(int i=0;i<2;++i)for(int j=0;j<2;++j)for(int g=0;g<16;++g){int row=(g&3)+8*(g>>2)+4*h; C[(long)(m0+i*32+row)*N+n0+j*32+r]=f2bf(acc[i][j][g]);}
}
__global__ __launch_bounds__(64) void attn_simple(const u16* __restrict__ Q, const u16* __restrict__ Kc, const u16* __restrict__ Vc, u16* __restrict__ O){
  int qb=blockIdx.x, hh=blockIdx.y, b=blockIdx.z; int lane=threadIdx.x, r=lane&31, h=lane>>5;
  const u16* q=Q+((long)(b*SQ+qb*32+r))*(H_*DH)+hh*DH;
  const u16* K=Kc+((long)(b*H_+hh))*SK*DH; const u16* V=Vc+((long)(b*H_+hh))*SK*DH;
  bf16x8 qf[8];
  #pragma unroll
  for(int s=0;s<8;++s) qf[s]=*(const bf16x8*)(q+16*s+8*h);
  f32x16 o[4]={}; float m=-1e30f,l=0.f; const float c=0.12751743082f;
  for(int t=0;t<SK;t+=32){
    f32x16 s={};
    #pragma unroll
    for(int ks=0;ks<8;++ks){ bf16x8 kf=*(const bf16x8*)(K+(long)(t+r)*DH+16*ks+8*h); s=__builtin_amdgcn_mfma_f32_32x32x16_bf16(kf,qf[ks],s,0,0,0);}
    float mx=-1e30f;
    #pragma unroll
    for(int g=0;g<16;++g){ s[g]*=c; mx=fmaxf(mx,s[g]); }
    mx=fmaxf(mx,__shfl_xor(mx,32));
    float mn=fmaxf(m,mx), al=exp2f(m-mn); m=mn; float ps=0;
    #pragma unroll
    for(int g=0;g<16;++g){ s[g]=exp2f(s[g]-mn); ps+=s[g]; }
    ps+=__shfl_xor(ps,32); l=l*al+ps;
    #pragma unroll
    for(int d=0;d<4;++d)
      #pragma unroll
      for(int g=0;g<16;++g) o[d][g]*=al;
    #pragma unroll
    for(int ks=0;ks<2;++ks){
      bf16x8 pf;
      #pragma unroll
      for(int j=0;j<8;++j) pf[j]=(short)f2bf(s[8*ks+j]);
      #pragma unroll
      for(int d=0;d<4;++d){ bf16x8 vf;
        #pragma unroll
        for(int j=0;j<8;++j){ int key=16*ks+8*(j>>2)+4*h+(j&3); vf[j]=(short)V[(long)(t+key)*DH+32*d+r]; }
        o[d]=__builtin_amdgcn_mfma_f32_32x32x16_bf16(vf,pf,o[d],0,0,0);} }
  }
  float il=1.f/l; u16* op=O+((long)(b*SQ+qb*32+r))*(H_*DH)+hh*DH;
  #pragma unroll
  for(int d=0;d<4;++d)
    #pragma unroll
    for(int g=0;g<16;++g){int dd=32*d+(g&3)+8*(g>>2)+4*h; op[dd]=f2bf(o[d][g]*il);}
}
extern "C" void kernel_launch(void* const* d_in, const int* in_sizes, int n_in, void* d_out, int out_size, void* d_ws, size_t ws_size, hipStream_t stream){
  const u16* x=(const u16*)d_in[0]; const u16* Kc=(const u16*)d_in[1]; const u16* Vc=(const u16*)d_in[2]; const u16* wq=(const u16*)d_in[3]; const u16* wo=(const u16*)d_in[4];
  u16* ws=(u16*)d_ws; u16* wqT=ws; u16* woT=wqT+4096*1024; u16* Q=woT+4096*1024; u16* O=Q+(long)3072*4096;
  transp<<<dim3(4096/32,1024/32),256,0,stream>>>(wq,wqT,1024,4096);
  transp<<<dim3(1024/32,4096/32),256,0,stream>>>(wo,woT,4096,1024);
  gemm_simple<<<dim3(4096/128,3072/128),256,0,stream>>>(x,wqT,Q,3072,4096,1024);
  attn_simple<<<dim3(6,32,16),64,0,stream>>>(Q,Kc,Vc,O);
  gemm_simple<<<dim3(1024/128,3072/128),256,0,stream>>>(O,woT,(u16*)d_out,3072,1024,4096);
}
```

```cpp
#include <hip/hip_runtime.h>
#include <hip/hip_bf16.h>
typedef unsigned long long u64;
typedef unsigned u32x16 __attribute__((ext_vector_type(16)));
typedef unsigned u32x8 __attribute__((ext_vector_type(8)));
typedef unsigned u32x4 __attribute__((ext_vector_type(4)));

#define SRD(v, i, a, n) v[i] = (unsigned)(a); v[i + 1] = (unsigned)((a) >> 32) & 0xffffu; v[i + 2] = (n); v[i + 3] = 0x20000u
#define SEL(c, a, b) ((b) ^ (((a) ^ (b)) & (0u - (unsigned)(c))))
#define DECODE                                                                                                   \
  const unsigned l = tid & 63u, hh = w >> 1, qh = w & 1u, xcd = bid & 7u, idx = bid >> 3;                          \
  const unsigned b = 4u * (xcd & 3u) + (idx >> 3), hp = 8u * (xcd >> 2) + (idx & 7u), kh = hp >> 3;                                                              \
  const unsigned g = l >> 4, p4 = l & 3u, q4 = (l >> 2) & 3u

__global__ __launch_bounds__(256, 1) void fused_mha_kernel(const char* __restrict__ x, const char* __restrict__ kc, const char* __restrict__ vc,
                                                           const char* __restrict__ wq, const char* __restrict__ wo, char* ws, char* out ) {
  unsigned tid = threadIdx.x, bid = blockIdx.x, w = __builtin_amdgcn_readfirstlane(threadIdx.x >> 6);
  u32x8 sxw, sq, vd;
  u32x4 clk;
  {
    DECODE;
    const u64 ax = (u64)x + (u64)b * 393216u, aw = (u64)wq + hp * 512u;
    const unsigned k0 = (4u * (xcd & 3u) + 8u * (xcd >> 2)) & 15u;
    SRD(sxw, 0, ax, 393216u); SRD(sxw, 4, aw, 8388608u - hp * 512u);
    sq[0] = (w ^ kh) * 48u * 2048u + k0 * 128u; sq[1] = sq[0] + 65536u;
    sq[2] = (w >> 1) * 4u * 8192u + (w & 1u) * 256u + k0 * 524288u;
    sq[3] = (16u << 20) + ((b * 16u + (hp ^ 8u)) * 4u + w) * 16384u;
    sq[4] = 81920u + w * 6144u; sq[5] = (w & 1u) * 8192u + (w >> 1) * 4096u;
    sq[6] = 0u; sq[7] = hh * 16384u + qh * 4096u;
#pragma unroll
    for (unsigned j = 0; j < 4; ++j) {
      vd[j] = (l >> 3) * 2048u + 16u * ((l & 7u) ^ (4u * (j & 1u) + g)) + j * 15360u;
      vd[4 + j] = (8u * j + q4) * 8192u + g * 64u + 16u * (p4 ^ ((j & 1u) << 1)) - 1024u * j;
    }
    asm volatile(
    "s_nop 4\n\t"
    "s_memtime s[92:93]\n\t"
    "s_memrealtime s[94:95]\n\t"
    "s_add_u32 m0, s61, 0\n\t"
    "s_nop 0\n\t"
    "buffer_load_dwordx4 v196, s[40:43], s58 offen offset:0 lds\n\t"
    "buffer_load_dwordx4 v197, s[40:43], s58 offen offset:1024 lds\n\t"
    "buffer_load_dwordx4 v198, s[40:43], s58 offen offset:2048 lds\n\t"
    "buffer_load_dwordx4 v199, s[40:43], s58 offen offset:3072 lds\n\t"
    "s_add_u32 s58, s58, 262144\n\t"
    "s_add_u32 m0, s60, 49152\n\t"
    "s_nop 0\n\t"
    "buffer_load_dwordx4 v192, s[36:39], s56 offen offset:0 lds\n\t"
    "buffer_load_dwordx4 v193, s[36:39], s56 offen offset:1024 lds\n\t"
    "buffer_load_dwordx4 v194, s[36:39], s56 offen offset:2048 lds\n\t"
    "buffer_load_dwordx4 v195, s[36:39], s56 offen offset:3072 lds\n\t"
    "s_addk_i32 m0, 4096\n\t"
    "s_nop 0\n\t"
    "buffer_load_dwordx4 v192, s[36:39], s57 offen offset:0 lds\n\t"
    "buffer_load_dwordx4 v193, s[36:39], s57 offen offset:1024 lds\n\t"
    "s_addk_i32 s56, 128\n\t"
    "s_addk_i32 s57, 128\n\t"
    "s_add_u32 m0, s61, 16384\n\t"
    "s_nop 0\n\t"
    "buffer_load_dwordx4 v196, s[40:43], s58 offen offset:0 lds\n\t"
    "buffer_load_dwordx4 v197, s[40:43], s58 offen offset:1024 lds\n\t"
    "buffer_load_dwordx4 v198, s[40:43], s58 offen offset:2048 lds\n\t"
    "buffer_load_dwordx4 v199, s[40:43], s58 offen offset:3072 lds\n\t"
    "s_add_u32 s58, s58, 262144\n\t"
    "s_waitcnt lgkmcnt(0)\n\t"
      : "+{s[36:43]}"(sxw), "+{s[56:63]}"(sq), "+{v[192:199]}"(vd), "={s[92:95]}"(clk)
      :
      : "scc", "memory");
    asm volatile("" : "+s"(bid));
    asm volatile("" : "+s"(w));
    asm volatile("" : "+v"(tid));
    __builtin_amdgcn_sched_barrier(0);
  }
  DECODE;
  const unsigned c = l & 15u, cb = hp & 7u, h = 2u * hp + hh, gw = bid * 4u + w;
  const u64 ak = (u64)kc + (u64)(b * 32u + h) * 262144u, av = (u64)vc + (u64)(b * 32u + h) * 262144u;
  const u64 ao = (u64)ws + (u64)((b * 2u + qh) * 128u + 4u * h) * 6144u;
  const u64 aa = (u64)wo + (u64)(16u * kh + hh) * 262144u + cb * 256u, ab = (u64)ws + (u64)((b * 2u + qh) * 128u + 4u * (16u * kh + hh)) * 6144u;
  const u64 ad = (u64)out + (u64)(b * 192u + 96u * qh + 48u * kh) * 2048u + cb * 256u + hh * 128u;
  const u64 as = (u64)ws + (32u << 20);
  u32x16 s0, vb, vc2;
  u32x8 s1, skv, va;
  u32x4 so, sk;
  SRD(s0, 0, aa, 7u * 524288u + 127u * 2048u + 256u); SRD(s0, 4, ab, 7u * 49152u + 24576u); SRD(s0, 8, ad, 47u * 2048u + 128u); SRD(s0, 12, as, 32u << 20);
  SRD(skv, 0, ak, 262144u); SRD(skv, 4, av, 262144u); SRD(so, 0, ao, 24576u);
  u64 pwo = (u64)wo + gw * 8192u;
  s1[0] = cb * 524288u; s1[1] = cb; s1[2] = (idx * 4u + w) * 1024u; s1[3] = hh * 65536u + qh * 4096u;
  s1[4] = 0u; s1[5] = 0u; s1[6] = 0u; s1[7] = 0;
  sk[0] = 0x41000000u; sk[1] = 32u * 8192u;
  sk[2] = 0x3e0293eeu; sk[3] = 0x3e0293eeu;
#pragma unroll
  for (unsigned j = 0; j < 4; ++j) {
    va[4 + j] = ((l >> 3) & 3u) * 256u + (l >> 5) * 128u + 16u * ((l & 7u) ^ (2u * j + ((l >> 4) & 1u))) + qh * 4096u;
    vb[2 + j] = hh * 16384u + (c >> 2) * 1024u + (c & 3u) * 128u + 16u * ((4u * (j & 1u) + g) ^ (c >> 1));
    vc2[j] = (16u * qh + 4u * j + q4) * 2048u + (g ^ (w & 2u)) * 64u + 16u * (p4 ^ ((j & 1u) << 1)) - 1024u * j;
  }
#pragma unroll
  for (unsigned j = 0; j < 2; ++j) {
    const unsigned trl = g * 1024u + q4 * 64u + 32u * (j ^ (g & 1u)) + 8u * p4;
    va[j] = 81920u + qh * 12288u + c * 128u + 16u * ((4u * j + g) ^ (c >> 1));
    va[2 + j] = trl + hh * 8192u;
    vb[j] = q4 * 256u + g * 64u + 16u * (p4 ^ (j << 1)) + qh * 4096u;
    vb[6 + j] = trl + hh * 16384u + 8192u;
    vc2[6 + j] = trl + hh * 65536u;
  }
  vb[8] = l * 16u;
  const unsigned wg = b * 16u + hp, g8 = wg & ~7u, oob = 0x7ff00000u;
  const unsigned l8 = l < 8u, l9 = l < 9u;
  vb[9] = SEL(l9, SEL(l8, (wg >> 3) * 128u + cb * 4u, 8192u + 64u * wg), oob);
  vb[10] = SEL(l9, SEL(l8, (wg >> 3) * 128u + l * 4u, 8192u + 64u * (wg ^ 8u)), oob);
  vb[11] = SEL(tid < 9u, SEL(l8, 32768u + 64u * (g8 + l) + cb * 4u, 32768u + 64u * (wg ^ 8u) + 32u), oob);
  vb[12] = SEL(l9, 32768u + 64u * wg + l * 4u, oob);
  vc2[8] = 131072u + w * 4096u + l * 16u; vc2[9] = 131072u + (w ^ 2u) * 4096u + l * 16u;
  vc2[10] = SEL(tid == 0u, vb[9], oob); vc2[11] = SEL(tid == 8u, vb[9], oob);
  vb[13] = vb[0]; vb[14] = 0u; vb[15] = ((idx * 4u + w) & 31u) * 1024u + (l < 7u ? l : 7u) * 128u;
  vc2[4] = l * 16u; vc2[5] = l * 16u + 3072u;
  vc2[12] = c * 2048u + g * 16u; vc2[13] = l * 128u; vc2[14] = vb[1];
  {
    const int t = (int)l - 5, bo = 16 - 4 * t, sp = 1 * (t - 1 + 1);
    int p = 134 + (bo > 0 ? bo : 0) - (sp > 0 ? sp : 0);
    vc2[15] = t < 26 ? (unsigned)(p > 100 ? p : 100) : 0u;
  }

  asm volatile(
    "s_nop 4\n\t"
    "s_getpc_b64 s[68:69]\n\t"
    ".LCT1_%=:\n\t"
    "v_min_u32 v251, .LEND_%=-.LCT1_%=-4, v223\n\t"
    "global_load_dword a255, v251, s[68:69]\n\t"
    "s_mov_b32 s96, s62\n\t"
    "s_mov_b32 s62, 0\n\t"
    "s_mov_b32 s71, 32768\n\t"
    "v_add_u32 v224, s71, v210\n\t"
    "v_add_u32 v225, s71, v211\n\t"
    "v_add_u32 v226, s71, v212\n\t"
    "v_add_u32 v227, s71, v213\n\t"
    "v_add_u32 v228, s71, v214\n\t"
    "v_add_u32 v229, s71, v215\n\t"
    "v_add_u32 v248, 65536, v202\n\t"
    "v_add_u32 v249, 65536, v203\n\t"
    "v_add_u32 v254, s63, v216\n\t"
    "v_add_u32 v255, 65536, v254\n\t"
    "v_add_u32 v231, 131072, v254\n\t"
    "s_add_u32 m0, s61, 32768\n\t"
    "s_nop 0\n\t"
    "buffer_load_dwordx4 v196, s[40:43], s58 offen offset:0 lds\n\t"
    "buffer_load_dwordx4 v197, s[40:43], s58 offen offset:1024 lds\n\t"
    "buffer_load_dwordx4 v198, s[40:43], s58 offen offset:2048 lds\n\t"
    "buffer_load_dwordx4 v199, s[40:43], s58 offen offset:3072 lds\n\t"
    "s_add_u32 s58, s58, 262144\n\t"
    "s_add_u32 m0, s60, 0\n\t"
    "s_nop 0\n\t"
    "buffer_load_dwordx4 v192, s[36:39], s56 offen offset:0 lds\n\t"
    "buffer_load_dwordx4 v193, s[36:39], s56 offen offset:1024 lds\n\t"
    "buffer_load_dwordx4 v194, s[36:39], s56 offen offset:2048 lds\n\t"
    "buffer_load_dwordx4 v195, s[36:39], s56 offen offset:3072 lds\n\t"
    "s_addk_i32 m0, 4096\n\t"
    "s_nop 0\n\t"
    "buffer_load_dwordx4 v192, s[36:39], s57 offen offset:0 lds\n\t"
    "buffer_load_dwordx4 v193, s[36:39], s57 offen offset:1024 lds\n\t"
    "s_addk_i32 s56, 128\n\t"
    "s_addk_i32 s57, 128\n\t"
    "s_add_u32 m0, s61, 49152\n\t"
    "s_nop 0\n\t"
    "buffer_load_dwordx4 v196, s[40:43], s58 offen offset:0 lds\n\t"
    "buffer_load_dwordx4 v197, s[40:43], s58 offen offset:1024 lds\n\t"
    "buffer_load_dwordx4 v198, s[40:43], s58 offen offset:2048 lds\n\t"
    "buffer_load_dwordx4 v199, s[40:43], s58 offen offset:3072 lds\n\t"
    "s_add_u32 s58, s58, 262144\n\t"
    "s_waitcnt vmcnt(19)\n\t"
    "s_memtime s[72:73]\n\t"
    "s_memrealtime s[74:75]\n\t"
    "s_barrier\n\t"
    "ds_read_b128 a[0:3], v200 offset:49152\n\t"
    "ds_read_b64_tr_b16 a[48:49], v202 offset:0\n\t"
    "ds_read_b64_tr_b16 a[50:51], v202 offset:4096\n\t"
    "ds_read_b128 a[4:7], v200 offset:51200\n\t"
    "ds_read_b64_tr_b16 a[52:53], v203 offset:0\n\t"
    "ds_read_b64_tr_b16 a[54:55], v203 offset:4096\n\t"
    "ds_read_b128 a[8:11], v200 offset:53248\n\t"
    "ds_read_b64_tr_b16 a[56:57], v202 offset:256\n\t"
    "ds_read_b64_tr_b16 a[58:59], v202 offset:4352\n\t"
    "ds_read_b128 a[12:15], v200 offset:55296\n\t"
    "ds_read_b64_tr_b16 a[60:61], v203 offset:256\n\t"
    "ds_read_b64_tr_b16 a[62:63], v203 offset:4352\n\t"
    "ds_read_b128 a[16:19], v200 offset:57344\n\t"
    "ds_read_b64_tr_b16 a[64:65], v202 offset:512\n\t"
    "ds_read_b64_tr_b16 a[66:67], v202 offset:4608\n\t"
    "ds_read_b128 a[20:23], v200 offset:59392\n\t"
    "ds_read_b64_tr_b16 a[68:69], v203 offset:512\n\t"
    "ds_read_b64_tr_b16 a[70:71], v203 offset:4608\n\t"
    "ds_read_b64_tr_b16 a[72:73], v202 offset:768\n\t"
    "ds_read_b64_tr_b16 a[74:75], v202 offset:4864\n\t"
    "ds_read_b64_tr_b16 a[76:77], v203 offset:768\n\t"
    "ds_read_b64_tr_b16 a[78:79], v203 offset:4864\n\t"
    "s_waitcnt vmcnt(15) lgkmcnt(0)\n\t"
    "s_barrier\n\t"
    "v_mfma_f32_16x16x32_bf16 v[0:3], a[48:51], a[0:3], 0\n\t"
    "ds_read_b128 a[24:27], v201 offset:49152\n\t"
    "s_sub_u32 s72, s72, s92\n\t"
    "s_sub_u32 s74, s74, s94\n\t"
    "v_mfma_f32_16x16x32_bf16 v[32:35], a[48:51], a[4:7], 0\n\t"
    "ds_read_b64_tr_b16 a[80:81], v202 offset:16384\n\t"
    "v_mfma_f32_16x16x32_bf16 v[64:67], a[48:51], a[8:11], 0\n\t"
    "s_lshl_b32 s72, s72, 1\n\t"
    "s_mul_i32 s74, s74, 40\n\t"
    "v_mfma_f32_16x16x32_bf16 v[96:99], a[48:51], a[12:15], 0\n\t"
    "ds_read_b64_tr_b16 a[82:83], v202 offset:20480\n\t"
    "v_mfma_f32_16x16x32_bf16 v[128:131], a[48:51], a[16:19], 0\n\t"
    "s_cmp_lt_u32 s72, s74\n\t"
    "s_cselect_b32 s72, 8, 0\n\t"
    "s_cselect_b32 s74, 126, -1\n\t"
    "v_mfma_f32_16x16x32_bf16 v[160:163], a[48:51], a[20:23], 0\n\t"
    "ds_read_b128 a[28:31], v201 offset:51200\n\t"
    "s_nop 7\n\t"
    "v_mfma_f32_16x16x32_bf16 v[4:7], a[52:55], a[0:3], 0\n\t"
    "ds_read_b64_tr_b16 a[84:85], v203 offset:16384\n\t"
    "v_sub_u32 v247, v247, s72 clamp\n\t"
    "v_min_u32 v247, s74, v247\n\t"
    "v_mfma_f32_16x16x32_bf16 v[36:39], a[52:55], a[4:7], 0\n\t"
    "v_mfma_f32_16x16x32_bf16 v[68:71], a[52:55], a[8:11], 0\n\t"
    "ds_read_b64_tr_b16 a[86:87], v203 offset:20480\n\t"
    "s_add_u32 m0, s61, 65536\n\t"
    "v_mfma_f32_16x16x32_bf16 v[100:103], a[52:55], a[12:15], 0\n\t"
    "v_mfma_f32_16x16x32_bf16 v[132:135], a[52:55], a[16:19], 0\n\t"
    "ds_read_b128 a[32:35], v201 offset:53248\n\t"
    "buffer_load_dwordx4 v196, s[40:43], s58 offen offset:0 lds\n\t"
    "v_mfma_f32_16x16x32_bf16 v[164:167], a[52:55], a[20:23], 0\n\t"
    "ds_read_b64_tr_b16 a[88:89], v202 offset:16640\n\t"
    "s_nop 7\n\t"
    "v_mfma_f32_16x16x32_bf16 v[8:11], a[56:59], a[0:3], 0\n\t"
    "v_mfma_f32_16x16x32_bf16 v[40:43], a[56:59], a[4:7], 0\n\t"
    "ds_read_b64_tr_b16 a[90:91], v202 offset:20736\n\t"
    "buffer_load_dwordx4 v197, s[40:43], s58 offen offset:1024 lds\n\t"
    "v_mfma_f32_16x16x32_bf16 v[72:75], a[56:59], a[8:11], 0\n\t"
    "v_mfma_f32_16x16x32_bf16 v[104:107], a[56:59], a[12:15], 0\n\t"
    "ds_read_b128 a[36:39], v201 offset:55296\n\t"
    "buffer_load_dwordx4 v198, s[40:43], s58 offen offset:2048 lds\n\t"
    "v_mfma_f32_16x16x32_bf16 v[136:139], a[56:59], a[16:19], 0\n\t"
    "ds_read_b64_tr_b16 a[92:93], v203 offset:16640\n\t"
    "v_mfma_f32_16x16x32_bf16 v[168:171], a[56:59], a[20:23], 0\n\t"
    "buffer_load_dwordx4 v199, s[40:43], s58 offen offset:3072 lds\n\t"
    "s_nop 7\n\t"
    "v_mfma_f32_16x16x32_bf16 v[12:15], a[60:63], a[0:3], 0\n\t"
    "ds_read_b64_tr_b16 a[94:95], v203 offset:20736\n\t"
    "v_mfma_f32_16x16x32_bf16 v[44:47], a[60:63], a[4:7], 0\n\t"
    "s_add_u32 s58, s58, 262144\n\t"
    "v_mfma_f32_16x16x32_bf16 v[76:79], a[60:63], a[8:11], 0\n\t"
    "ds_read_b128 a[40:43], v201 offset:57344\n\t"
    "v_mfma_f32_16x16x32_bf16 v[108:111], a[60:63], a[12:15], 0\n\t"
    "ds_read_b64_tr_b16 a[96:97], v202 offset:16896\n\t"
    "s_add_u32 m0, s60, 24576\n\t"
    "v_mfma_f32_16x16x32_bf16 v[140:143], a[60:63], a[16:19], 0\n\t"
    "v_mfma_f32_16x16x32_bf16 v[172:175], a[60:63], a[20:23], 0\n\t"
    "ds_read_b64_tr_b16 a[98:99], v202 offset:20992\n\t"
    "s_nop 7\n\t"
    "v_mfma_f32_16x16x32_bf16 v[16:19], a[64:67], a[0:3], 0\n\t"
    "buffer_load_dwordx4 v192, s[36:39], s56 offen offset:0 lds\n\t"
    "v_mfma_f32_16x16x32_bf16 v[48:51], a[64:67], a[4:7], 0\n\t"
    "ds_read_b128 a[44:47], v201 offset:59392\n\t"
    "v_mfma_f32_16x16x32_bf16 v[80:83], a[64:67], a[8:11], 0\n\t"
    "ds_read_b64_tr_b16 a[100:101], v203 offset:16896\n\t"
    "buffer_load_dwordx4 v193, s[36:39], s56 offen offset:1024 lds\n\t"
    "v_mfma_f32_16x16x32_bf16 v[112:115], a[64:67], a[12:15], 0\n\t"
    "v_mfma_f32_16x16x32_bf16 v[144:147], a[64:67], a[16:19], 0\n\t"
    "ds_read_b64_tr_b16 a[102:103], v203 offset:20992\n\t"
    "buffer_load_dwordx4 v194, s[36:39], s56 offen offset:2048 lds\n\t"
    "v_mfma_f32_16x16x32_bf16 v[176:179], a[64:67], a[20:23], 0\n\t"
    "s_nop 7\n\t"
    "v_mfma_f32_16x16x32_bf16 v[20:23], a[68:71], a[0:3], 0\n\t"
    "ds_read_b64_tr_b16 a[104:105], v202 offset:17152\n\t"
    "buffer_load_dwordx4 v195, s[36:39], s56 offen offset:3072 lds\n\t"
    "v_mfma_f32_16x16x32_bf16 v[52:55], a[68:71], a[4:7], 0\n\t"
    "ds_read_b64_tr_b16 a[106:107], v202 offset:21248\n\t"
    "v_mfma_f32_16x16x32_bf16 v[84:87], a[68:71], a[8:11], 0\n\t"
    "s_addk_i32 m0, 4096\n\t"
    "v_mfma_f32_16x16x32_bf16 v[116:119], a[68:71], a[12:15], 0\n\t"
    "ds_read_b64_tr_b16 a[108:109], v203 offset:17152\n\t"
    "v_mfma_f32_16x16x32_bf16 v[148:151], a[68:71], a[16:19], 0\n\t"
    "buffer_load_dwordx4 v192, s[36:39], s57 offen offset:0 lds\n\t"
    "v_mfma_f32_16x16x32_bf16 v[180:183], a[68:71], a[20:23], 0\n\t"
    "ds_read_b64_tr_b16 a[110:111], v203 offset:21248\n\t"
    "s_nop 7\n\t"
    "v_mfma_f32_16x16x32_bf16 v[24:27], a[72:75], a[0:3], 0\n\t"
    "v_mfma_f32_16x16x32_bf16 v[56:59], a[72:75], a[4:7], 0\n\t"
    "buffer_load_dwordx4 v193, s[36:39], s57 offen offset:1024 lds\n\t"
    "v_mfma_f32_16x16x32_bf16 v[88:91], a[72:75], a[8:11], 0\n\t"
    "v_mfma_f32_16x16x32_bf16 v[120:123], a[72:75], a[12:15], 0\n\t"
    "s_addk_i32 s56, 128\n\t"
    "v_mfma_f32_16x16x32_bf16 v[152:155], a[72:75], a[16:19], 0\n\t"
    "v_mfma_f32_16x16x32_bf16 v[184:187], a[72:75], a[20:23], 0\n\t"
    "s_addk_i32 s57, 128\n\t"
    "s_nop 7\n\t"
    "v_mfma_f32_16x16x32_bf16 v[28:31], a[76:79], a[0:3], 0\n\t"
    "v_mfma_f32_16x16x32_bf16 v[60:63], a[76:79], a[4:7], 0\n\t"
    "buffer_load_dword a252, v217, s[24:27], 0 offen offset:0 sc1\n\t"
    "v_mfma_f32_16x16x32_bf16 v[92:95], a[76:79], a[8:11], 0\n\t"
    "v_mfma_f32_16x16x32_bf16 v[124:127], a[76:79], a[12:15], 0\n\t"
    "buffer_load_dword v252, v218, s[24:27], 0 offen offset:0 sc1\n\t"
    "v_mfma_f32_16x16x32_bf16 v[156:159], a[76:79], a[16:19], 0\n\t"
    "v_mfma_f32_16x16x32_bf16 v[188:191], a[76:79], a[20:23], 0\n\t"
    "s_nop 7\n\t"
    "s_waitcnt vmcnt(12) lgkmcnt(0)\n\t"
    "s_barrier\n\t"
    "v_mfma_f32_16x16x32_bf16 v[0:3], a[80:83], a[24:27], v[0:3]\n\t"
    "ds_read_b128 a[0:3], v200 offset:0\n\t"
    "s_add_u32 m0, s61, 0\n\t"
    "v_mfma_f32_16x16x32_bf16 v[32:35], a[80:83], a[28:31], v[32:35]\n\t"
    "ds_read_b64_tr_b16 a[48:49], v202 offset:32768\n\t"
    "v_mfma_f32_16x16x32_bf16 v[64:67], a[80:83], a[32:35], v[64:67]\n\t"
    "v_mfma_f32_16x16x32_bf16 v[96:99], a[80:83], a[36:39], v[96:99]\n\t"
    "ds_read_b64_tr_b16 a[50:51], v202 offset:36864\n\t"
    "v_mfma_f32_16x16x32_bf16 v[128:131], a[80:83], a[40:43], v[128:131]\n\t"
    "buffer_load_dwordx4 v196, s[40:43], s58 offen offset:0 lds\n\t"
    "v_mfma_f32_16x16x32_bf16 v[160:163], a[80:83], a[44:47], v[160:163]\n\t"
    "ds_read_b128 a[4:7], v200 offset:2048\n\t"
    "s_nop 7\n\t"
    "v_mfma_f32_16x16x32_bf16 v[4:7], a[84:87], a[24:27], v[4:7]\n\t"
    "ds_read_b64_tr_b16 a[52:53], v203 offset:32768\n\t"
    "v_mfma_f32_16x16x32_bf16 v[36:39], a[84:87], a[28:31], v[36:39]\n\t"
    "v_mfma_f32_16x16x32_bf16 v[68:71], a[84:87], a[32:35], v[68:71]\n\t"
    "ds_read_b64_tr_b16 a[54:55], v203 offset:36864\n\t"
    "buffer_load_dwordx4 v197, s[40:43], s58 offen offset:1024 lds\n\t"
    "v_mfma_f32_16x16x32_bf16 v[100:103], a[84:87], a[36:39], v[100:103]\n\t"
    "v_mfma_f32_16x16x32_bf16 v[132:135], a[84:87], a[40:43], v[132:135]\n\t"
    "ds_read_b128 a[8:11], v200 offset:4096\n\t"
    "v_mfma_f32_16x16x32_bf16 v[164:167], a[84:87], a[44:47], v[164:167]\n\t"
    "ds_read_b64_tr_b16 a[56:57], v202 offset:33024\n\t"
    "s_nop 7\n\t"
    "v_mfma_f32_16x16x32_bf16 v[8:11], a[88:91], a[24:27], v[8:11]\n\t"
    "buffer_load_dwordx4 v198, s[40:43], s58 offen offset:2048 lds\n\t"
    "v_mfma_f32_16x16x32_bf16 v[40:43], a[88:91], a[28:31], v[40:43]\n\t"
    "ds_read_b64_tr_b16 a[58:59], v202 offset:37120\n\t"
    "v_mfma_f32_16x16x32_bf16 v[72:75], a[88:91], a[32:35], v[72:75]\n\t"
    "v_mfma_f32_16x16x32_bf16 v[104:107], a[88:91], a[36:39], v[104:107]\n\t"
    "ds_read_b128 a[12:15], v200 offset:6144\n\t"
    "v_mfma_f32_16x16x32_bf16 v[136:139], a[88:91], a[40:43], v[136:139]\n\t"
    "ds_read_b64_tr_b16 a[60:61], v203 offset:33024\n\t"
    "buffer_load_dwordx4 v199, s[40:43], s58 offen offset:3072 lds\n\t"
    "v_mfma_f32_16x16x32_bf16 v[168:171], a[88:91], a[44:47], v[168:171]\n\t"
    "s_nop 7\n\t"
    "v_mfma_f32_16x16x32_bf16 v[12:15], a[92:95], a[24:27], v[12:15]\n\t"
    "ds_read_b64_tr_b16 a[62:63], v203 offset:37120\n\t"
    "v_mfma_f32_16x16x32_bf16 v[44:47], a[92:95], a[28:31], v[44:47]\n\t"
    "v_mfma_f32_16x16x32_bf16 v[76:79], a[92:95], a[32:35], v[76:79]\n\t"
    "ds_read_b128 a[16:19], v200 offset:8192\n\t"
    "s_add_u32 s58, s58, 262144\n\t"
    "v_mfma_f32_16x16x32_bf16 v[108:111], a[92:95], a[36:39], v[108:111]\n\t"
    "ds_read_b64_tr_b16 a[64:65], v202 offset:33280\n\t"
    "v_mfma_f32_16x16x32_bf16 v[140:143], a[92:95], a[40:43], v[140:143]\n\t"
    "v_mfma_f32_16x16x32_bf16 v[172:175], a[92:95], a[44:47], v[172:175]\n\t"
    "ds_read_b64_tr_b16 a[66:67], v202 offset:37376\n\t"
    "s_nop 7\n\t"
    "v_mfma_f32_16x16x32_bf16 v[16:19], a[96:99], a[24:27], v[16:19]\n\t"
    "s_add_u32 m0, s61, 16384\n\t"
    "v_mfma_f32_16x16x32_bf16 v[48:51], a[96:99], a[28:31], v[48:51]\n\t"
    "ds_read_b128 a[20:23], v200 offset:10240\n\t"
    "v_mfma_f32_16x16x32_bf16 v[80:83], a[96:99], a[32:35], v[80:83]\n\t"
    "ds_read_b64_tr_b16 a[68:69], v203 offset:33280\n\t"
    "v_mfma_f32_16x16x32_bf16 v[112:115], a[96:99], a[36:39], v[112:115]\n\t"
    "v_mfma_f32_16x16x32_bf16 v[144:147], a[96:99], a[40:43], v[144:147]\n\t"
    "ds_read_b64_tr_b16 a[70:71], v203 offset:37376\n\t"
    "buffer_load_dwordx4 v196, s[40:43], s58 offen offset:0 lds\n\t"
    "v_mfma_f32_16x16x32_bf16 v[176:179], a[96:99], a[44:47], v[176:179]\n\t"
    "s_nop 7\n\t"
    "v_mfma_f32_16x16x32_bf16 v[20:23], a[100:103], a[24:27], v[20:23]\n\t"
    "ds_read_b64_tr_b16 a[72:73], v202 offset:33536\n\t"
    "v_mfma_f32_16x16x32_bf16 v[52:55], a[100:103], a[28:31], v[52:55]\n\t"
    "ds_read_b64_tr_b16 a[74:75], v202 offset:37632\n\t"
    "v_mfma_f32_16x16x32_bf16 v[84:87], a[100:103], a[32:35], v[84:87]\n\t"
    "buffer_load_dwordx4 v197, s[40:43], s58 offen offset:1024 lds\n\t"
    "v_mfma_f32_16x16x32_bf16 v[116:119], a[100:103], a[36:39], v[116:119]\n\t"
    "ds_read_b64_tr_b16 a[76:77], v203 offset:33536\n\t"
    "v_mfma_f32_16x16x32_bf16 v[148:151], a[100:103], a[40:43], v[148:151]\n\t"
    "v_mfma_f32_16x16x32_bf16 v[180:183], a[100:103], a[44:47], v[180:183]\n\t"
    "ds_read_b64_tr_b16 a[78:79], v203 offset:37632\n\t"
    "s_nop 7\n\t"
    "v_mfma_f32_16x16x32_bf16 v[24:27], a[104:107], a[24:27], v[24:27]\n\t"
    "buffer_load_dwordx4 v198, s[40:43], s58 offen offset:2048 lds\n\t"
    "v_mfma_f32_16x16x32_bf16 v[56:59], a[104:107], a[28:31], v[56:59]\n\t"
    "v_mfma_f32_16x16x32_bf16 v[88:91], a[104:107], a[32:35], v[88:91]\n\t"
    "v_mfma_f32_16x16x32_bf16 v[120:123], a[104:107], a[36:39], v[120:123]\n\t"
    "v_mfma_f32_16x16x32_bf16 v[152:155], a[104:107], a[40:43], v[152:155]\n\t"
    "buffer_load_dwordx4 v199, s[40:43], s58 offen offset:3072 lds\n\t"
    "v_mfma_f32_16x16x32_bf16 v[184:187], a[104:107], a[44:47], v[184:187]\n\t"
    "s_nop 7\n\t"
    "v_mfma_f32_16x16x32_bf16 v[28:31], a[108:111], a[24:27], v[28:31]\n\t"
    "v_mfma_f32_16x16x32_bf16 v[60:63], a[108:111], a[28:31], v[60:63]\n\t"
    "v_mfma_f32_16x16x32_bf16 v[92:95], a[108:111], a[32:35], v[92:95]\n\t"
    "s_add_u32 s58, s58, 262144\n\t"
    "v_mfma_f32_16x16x32_bf16 v[124:127], a[108:111], a[36:39], v[124:127]\n\t"
    "v_mfma_f32_16x16x32_bf16 v[156:159], a[108:111], a[40:43], v[156:159]\n\t"
    "v_mfma_f32_16x16x32_bf16 v[188:191], a[108:111], a[44:47], v[188:191]\n\t"
    "s_nop 7\n\t"
    "s_waitcnt lgkmcnt(0)\n\t"
    "v_mfma_f32_16x16x32_bf16 v[0:3], a[48:51], a[0:3], v[0:3]\n\t"
    "ds_read_b128 a[24:27], v201 offset:0\n\t"
    "s_add_u32 m0, s60, 49152\n\t"
    "v_mfma_f32_16x16x32_bf16 v[32:35], a[48:51], a[4:7], v[32:35]\n\t"
    "ds_read_b64_tr_b16 a[80:81], v202 offset:49152\n\t"
    "v_mfma_f32_16x16x32_bf16 v[64:67], a[48:51], a[8:11], v[64:67]\n\t"
    "v_mfma_f32_16x16x32_bf16 v[96:99], a[48:51], a[12:15], v[96:99]\n\t"
    "ds_read_b64_tr_b16 a[82:83], v202 offset:53248\n\t"
    "v_mfma_f32_16x16x32_bf16 v[128:131], a[48:51], a[16:19], v[128:131]\n\t"
    "buffer_load_dwordx4 v192, s[36:39], s56 offen offset:0 lds\n\t"
    "v_mfma_f32_16x16x32_bf16 v[160:163], a[48:51], a[20:23], v[160:163]\n\t"
    "ds_read_b128 a[28:31], v201 offset:2048\n\t"
    "s_nop 6\n\t"
    "v_mfma_f32_16x16x32_bf16 v[4:7], a[52:55], a[0:3], v[4:7]\n\t"
    "ds_read_b64_tr_b16 a[84:85], v203 offset:49152\n\t"
    "v_mfma_f32_16x16x32_bf16 v[36:39], a[52:55], a[4:7], v[36:39]\n\t"
    "v_mfma_f32_16x16x32_bf16 v[68:71], a[52:55], a[8:11], v[68:71]\n\t"
    "ds_read_b64_tr_b16 a[86:87], v203 offset:53248\n\t"
    "buffer_load_dwordx4 v193, s[36:39], s56 offen offset:1024 lds\n\t"
    "v_mfma_f32_16x16x32_bf16 v[100:103], a[52:55], a[12:15], v[100:103]\n\t"
    "v_mfma_f32_16x16x32_bf16 v[132:135], a[52:55], a[16:19], v[132:135]\n\t"
    "ds_read_b128 a[32:35], v201 offset:4096\n\t"
    "v_mfma_f32_16x16x32_bf16 v[164:167], a[52:55], a[20:23], v[164:167]\n\t"
    "ds_read_b64_tr_b16 a[88:89], v202 offset:49408\n\t"
    "s_nop 6\n\t"
    "v_mfma_f32_16x16x32_bf16 v[8:11], a[56:59], a[0:3], v[8:11]\n\t"
    "buffer_load_dwordx4 v194, s[36:39], s56 offen offset:2048 lds\n\t"
    "v_mfma_f32_16x16x32_bf16 v[40:43], a[56:59], a[4:7], v[40:43]\n\t"
    "ds_read_b64_tr_b16 a[90:91], v202 offset:53504\n\t"
    "v_mfma_f32_16x16x32_bf16 v[72:75], a[56:59], a[8:11], v[72:75]\n\t"
    "v_mfma_f32_16x16x32_bf16 v[104:107], a[56:59], a[12:15], v[104:107]\n\t"
    "ds_read_b128 a[36:39], v201 offset:6144\n\t"
    "v_mfma_f32_16x16x32_bf16 v[136:139], a[56:59], a[16:19], v[136:139]\n\t"
    "ds_read_b64_tr_b16 a[92:93], v203 offset:49408\n\t"
    "buffer_load_dwordx4 v195, s[36:39], s56 offen offset:3072 lds\n\t"
    "v_mfma_f32_16x16x32_bf16 v[168:171], a[56:59], a[20:23], v[168:171]\n\t"
    "s_nop 6\n\t"
    "v_mfma_f32_16x16x32_bf16 v[12:15], a[60:63], a[0:3], v[12:15]\n\t"
    "ds_read_b64_tr_b16 a[94:95], v203 offset:53504\n\t"
    "v_mfma_f32_16x16x32_bf16 v[44:47], a[60:63], a[4:7], v[44:47]\n\t"
    "v_mfma_f32_16x16x32_bf16 v[76:79], a[60:63], a[8:11], v[76:79]\n\t"
    "ds_read_b128 a[40:43], v201 offset:8192\n\t"
    "s_addk_i32 m0, 4096\n\t"
    "v_mfma_f32_16x16x32_bf16 v[108:111], a[60:63], a[12:15], v[108:111]\n\t"
    "ds_read_b64_tr_b16 a[96:97], v202 offset:49664\n\t"
    "v_mfma_f32_16x16x32_bf16 v[140:143], a[60:63], a[16:19], v[140:143]\n\t"
    "v_mfma_f32_16x16x32_bf16 v[172:175], a[60:63], a[20:23], v[172:175]\n\t"
    "ds_read_b64_tr_b16 a[98:99], v202 offset:53760\n\t"
    "s_nop 6\n\t"
    "v_mfma_f32_16x16x32_bf16 v[16:19], a[64:67], a[0:3], v[16:19]\n\t"
    "buffer_load_dwordx4 v192, s[36:39], s57 offen offset:0 lds\n\t"
    "v_mfma_f32_16x16x32_bf16 v[48:51], a[64:67], a[4:7], v[48:51]\n\t"
    "ds_read_b128 a[44:47], v201 offset:10240\n\t"
    "v_mfma_f32_16x16x32_bf16 v[80:83], a[64:67], a[8:11], v[80:83]\n\t"
    "ds_read_b64_tr_b16 a[100:101], v203 offset:49664\n\t"
    "v_mfma_f32_16x16x32_bf16 v[112:115], a[64:67], a[12:15], v[112:115]\n\t"
    "v_mfma_f32_16x16x32_bf16 v[144:147], a[64:67], a[16:19], v[144:147]\n\t"
    "ds_read_b64_tr_b16 a[102:103], v203 offset:53760\n\t"
    "buffer_load_dwordx4 v193, s[36:39], s57 offen offset:1024 lds\n\t"
    "v_mfma_f32_16x16x32_bf16 v[176:179], a[64:67], a[20:23], v[176:179]\n\t"
    "s_nop 6\n\t"
    "v_mfma_f32_16x16x32_bf16 v[20:23], a[68:71], a[0:3], v[20:23]\n\t"
    "ds_read_b64_tr_b16 a[104:105], v202 offset:49920\n\t"
    "v_mfma_f32_16x16x32_bf16 v[52:55], a[68:71], a[4:7], v[52:55]\n\t"
    "ds_read_b64_tr_b16 a[106:107], v202 offset:54016\n\t"
    "v_mfma_f32_16x16x32_bf16 v[84:87], a[68:71], a[8:11], v[84:87]\n\t"
    "s_addk_i32 s56, 128\n\t"
    "v_mfma_f32_16x16x32_bf16 v[116:119], a[68:71], a[12:15], v[116:119]\n\t"
    "ds_read_b64_tr_b16 a[108:109], v203 offset:49920\n\t"
    "v_mfma_f32_16x16x32_bf16 v[148:151], a[68:71], a[16:19], v[148:151]\n\t"
    "v_mfma_f32_16x16x32_bf16 v[180:183], a[68:71], a[20:23], v[180:183]\n\t"
    "ds_read_b64_tr_b16 a[110:111], v203 offset:54016\n\t"
    "s_nop 6\n\t"
    "v_mfma_f32_16x16x32_bf16 v[24:27], a[72:75], a[0:3], v[24:27]\n\t"
    "s_addk_i32 s57, 128\n\t"
    "v_mfma_f32_16x16x32_bf16 v[56:59], a[72:75], a[4:7], v[56:59]\n\t"
    "v_mfma_f32_16x16x32_bf16 v[88:91], a[72:75], a[8:11], v[88:91]\n\t"
    "v_mfma_f32_16x16x32_bf16 v[120:123], a[72:75], a[12:15], v[120:123]\n\t"
    "v_mfma_f32_16x16x32_bf16 v[152:155], a[72:75], a[16:19], v[152:155]\n\t"
    "s_bitset0_b32 s56, 11\n\t"
    "v_mfma_f32_16x16x32_bf16 v[184:187], a[72:75], a[20:23], v[184:187]\n\t"
    "s_nop 6\n\t"
    "v_mfma_f32_16x16x32_bf16 v[28:31], a[76:79], a[0:3], v[28:31]\n\t"
    "v_mfma_f32_16x16x32_bf16 v[60:63], a[76:79], a[4:7], v[60:63]\n\t"
    "v_mfma_f32_16x16x32_bf16 v[92:95], a[76:79], a[8:11], v[92:95]\n\t"
    "s_bitset0_b32 s57, 11\n\t"
    "v_mfma_f32_16x16x32_bf16 v[124:127], a[76:79], a[12:15], v[124:127]\n\t"
    "v_mfma_f32_16x16x32_bf16 v[156:159], a[76:79], a[16:19], v[156:159]\n\t"
    "v_mfma_f32_16x16x32_bf16 v[188:191], a[76:79], a[20:23], v[188:191]\n\t"
    "s_nop 6\n\t"
    "s_waitcnt vmcnt(10) lgkmcnt(0)\n\t"
    "s_barrier\n\t"
    "v_mfma_f32_16x16x32_bf16 v[0:3], a[80:83], a[24:27], v[0:3]\n\t"
    "ds_read_b128 a[0:3], v200 offset:24576\n\t"
    "s_add_u32 m0, s61, 32768\n\t"
    "v_mfma_f32_16x16x32_bf16 v[32:35], a[80:83], a[28:31], v[32:35]\n\t"
    "ds_read_b64_tr_b16 a[48:49], v248 offset:0\n\t"
    "v_mfma_f32_16x16x32_bf16 v[64:67], a[80:83], a[32:35], v[64:67]\n\t"
    "v_mfma_f32_16x16x32_bf16 v[96:99], a[80:83], a[36:39], v[96:99]\n\t"
    "ds_read_b64_tr_b16 a[50:51], v248 offset:4096\n\t"
    "v_mfma_f32_16x16x32_bf16 v[128:131], a[80:83], a[40:43], v[128:131]\n\t"
    "buffer_load_dwordx4 v196, s[40:43], s58 offen offset:0 lds\n\t"
    "v_mfma_f32_16x16x32_bf16 v[160:163], a[80:83], a[44:47], v[160:163]\n\t"
    "ds_read_b128 a[4:7], v200 offset:26624\n\t"
    "s_nop 6\n\t"
    "v_mfma_f32_16x16x32_bf16 v[4:7], a[84:87], a[24:27], v[4:7]\n\t"
    "ds_read_b64_tr_b16 a[52:53], v249 offset:0\n\t"
    "v_mfma_f32_16x16x32_bf16 v[36:39], a[84:87], a[28:31], v[36:39]\n\t"
    "v_mfma_f32_16x16x32_bf16 v[68:71], a[84:87], a[32:35], v[68:71]\n\t"
    "ds_read_b64_tr_b16 a[54:55], v249 offset:4096\n\t"
    "buffer_load_dwordx4 v197, s[40:43], s58 offen offset:1024 lds\n\t"
    "v_mfma_f32_16x16x32_bf16 v[100:103], a[84:87], a[36:39], v[100:103]\n\t"
    "v_mfma_f32_16x16x32_bf16 v[132:135], a[84:87], a[40:43], v[132:135]\n\t"
    "ds_read_b128 a[8:11], v200 offset:28672\n\t"
    "v_mfma_f32_16x16x32_bf16 v[164:167], a[84:87], a[44:47], v[164:167]\n\t"
    "ds_read_b64_tr_b16 a[56:57], v248 offset:256\n\t"
    "s_nop 6\n\t"
    "v_mfma_f32_16x16x32_bf16 v[8:11], a[88:91], a[24:27], v[8:11]\n\t"
    "buffer_load_dwordx4 v198, s[40:43], s58 offen offset:2048 lds\n\t"
    "v_mfma_f32_16x16x32_bf16 v[40:43], a[88:91], a[28:31], v[40:43]\n\t"
    "ds_read_b64_tr_b16 a[58:59], v248 offset:4352\n\t"
    "v_mfma_f32_16x16x32_bf16 v[72:75], a[88:91], a[32:35], v[72:75]\n\t"
    "v_mfma_f32_16x16x32_bf16 v[104:107], a[88:91], a[36:39], v[104:107]\n\t"
    "ds_read_b128 a[12:15], v200 offset:30720\n\t"
    "v_mfma_f32_16x16x32_bf16 v[136:139], a[88:91], a[40:43], v[136:139]\n\t"
    "ds_read_b64_tr_b16 a[60:61], v249 offset:256\n\t"
    "buffer_load_dwordx4 v199, s[40:43], s58 offen offset:3072 lds\n\t"
    "v_mfma_f32_16x16x32_bf16 v[168:171], a[88:91], a[44:47], v[168:171]\n\t"
    "s_nop 6\n\t"
    "v_mfma_f32_16x16x32_bf16 v[12:15], a[92:95], a[24:27], v[12:15]\n\t"
    "ds_read_b64_tr_b16 a[62:63], v249 offset:4352\n\t"
    "v_mfma_f32_16x16x32_bf16 v[44:47], a[92:95], a[28:31], v[44:47]\n\t"
    "v_mfma_f32_16x16x32_bf16 v[76:79], a[92:95], a[32:35], v[76:79]\n\t"
    "ds_read_b128 a[16:19], v200 offset:32768\n\t"
    "s_add_u32 s58, s58, 262144\n\t"
    "v_mfma_f32_16x16x32_bf16 v[108:111], a[92:95], a[36:39], v[108:111]\n\t"
    "ds_read_b64_tr_b16 a[64:65], v248 offset:512\n\t"
    "v_mfma_f32_16x16x32_bf16 v[140:143], a[92:95], a[40:43], v[140:143]\n\t"
    "v_mfma_f32_16x16x32_bf16 v[172:175], a[92:95], a[44:47], v[172:175]\n\t"
    "ds_read_b64_tr_b16 a[66:67], v248 offset:4608\n\t"
    "s_nop 6\n\t"
    "v_mfma_f32_16x16x32_bf16 v[16:19], a[96:99], a[24:27], v[16:19]\n\t"
    "s_and_b32 s58, s58, 8388607\n\t"
    "v_mfma_f32_16x16x32_bf16 v[48:51], a[96:99], a[28:31], v[48:51]\n\t"
    "ds_read_b128 a[20:23], v200 offset:34816\n\t"
    "v_mfma_f32_16x16x32_bf16 v[80:83], a[96:99], a[32:35], v[80:83]\n\t"
    "ds_read_b64_tr_b16 a[68:69], v249 offset:512\n\t"
    "v_mfma_f32_16x16x32_bf16 v[112:115], a[96:99], a[36:39], v[112:115]\n\t"
    "v_mfma_f32_16x16x32_bf16 v[144:147], a[96:99], a[40:43], v[144:147]\n\t"
    "ds_read_b64_tr_b16 a[70:71], v249 offset:4608\n\t"
    "s_add_u32 m0, s61, 49152\n\t"
    "v_mfma_f32_16x16x32_bf16 v[176:179], a[96:99], a[44:47], v[176:179]\n\t"
    "s_nop 6\n\t"
    "v_mfma_f32_16x16x32_bf16 v[20:23], a[100:103], a[24:27], v[20:23]\n\t"
    "ds_read_b64_tr_b16 a[72:73], v248 offset:768\n\t"
    "v_mfma_f32_16x16x32_bf16 v[52:55], a[100:103], a[28:31], v[52:55]\n\t"
    "ds_read_b64_tr_b16 a[74:75], v248 offset:4864\n\t"
    "v_mfma_f32_16x16x32_bf16 v[84:87], a[100:103], a[32:35], v[84:87]\n\t"
    "buffer_load_dwordx4 v196, s[40:43], s58 offen offset:0 lds\n\t"
    "v_mfma_f32_16x16x32_bf16 v[116:119], a[100:103], a[36:39], v[116:119]\n\t"
    "ds_read_b64_tr_b16 a[76:77], v249 offset:768\n\t"
    "v_mfma_f32_16x16x32_bf16 v[148:151], a[100:103], a[40:43], v[148:151]\n\t"
    "v_mfma_f32_16x16x32_bf16 v[180:183], a[100:103], a[44:47], v[180:183]\n\t"
    "ds_read_b64_tr_b16 a[78:79], v249 offset:4864\n\t"
    "s_nop 6\n\t"
    "v_mfma_f32_16x16x32_bf16 v[24:27], a[104:107], a[24:27], v[24:27]\n\t"
    "buffer_load_dwordx4 v197, s[40:43], s58 offen offset:1024 lds\n\t"
    "v_mfma_f32_16x16x32_bf16 v[56:59], a[104:107], a[28:31], v[56:59]\n\t"
    "v_mfma_f32_16x16x32_bf16 v[88:91], a[104:107], a[32:35], v[88:91]\n\t"
    "v_mfma_f32_16x16x32_bf16 v[120:123], a[104:107], a[36:39], v[120:123]\n\t"
    "v_mfma_f32_16x16x32_bf16 v[152:155], a[104:107], a[40:43], v[152:155]\n\t"
    "buffer_load_dwordx4 v198, s[40:43], s58 offen offset:2048 lds\n\t"
    "v_mfma_f32_16x16x32_bf16 v[184:187], a[104:107], a[44:47], v[184:187]\n\t"
    "s_nop 6\n\t"
    "v_mfma_f32_16x16x32_bf16 v[28:31], a[108:111], a[24:27], v[28:31]\n\t"
    "v_mfma_f32_16x16x32_bf16 v[60:63], a[108:111], a[28:31], v[60:63]\n\t"
    "v_mfma_f32_16x16x32_bf16 v[92:95], a[108:111], a[32:35], v[92:95]\n\t"
    "buffer_load_dwordx4 v199, s[40:43], s58 offen offset:3072 lds\n\t"
    "v_mfma_f32_16x16x32_bf16 v[124:127], a[108:111], a[36:39], v[124:127]\n\t"
    "v_mfma_f32_16x16x32_bf16 v[156:159], a[108:111], a[40:43], v[156:159]\n\t"
    "v_mfma_f32_16x16x32_bf16 v[188:191], a[108:111], a[44:47], v[188:191]\n\t"
    "s_nop 6\n\t"
    "s_waitcnt lgkmcnt(0)\n\t"
    "v_mfma_f32_16x16x32_bf16 v[0:3], a[48:51], a[0:3], v[0:3]\n\t"
    "ds_read_b128 a[24:27], v201 offset:24576\n\t"
    "s_add_u32 s58, s58, 262144\n\t"
    "v_mfma_f32_16x16x32_bf16 v[32:35], a[48:51], a[4:7], v[32:35]\n\t"
    "ds_read_b64_tr_b16 a[80:81], v202 offset:0\n\t"
    "v_mfma_f32_16x16x32_bf16 v[64:67], a[48:51], a[8:11], v[64:67]\n\t"
    "v_mfma_f32_16x16x32_bf16 v[96:99], a[48:51], a[12:15], v[96:99]\n\t"
    "ds_read_b64_tr_b16 a[82:83], v202 offset:4096\n\t"
    "v_mfma_f32_16x16x32_bf16 v[128:131], a[48:51], a[16:19], v[128:131]\n\t"
    "s_add_u32 m0, s60, 0\n\t"
    "v_mfma_f32_16x16x32_bf16 v[160:163], a[48:51], a[20:23], v[160:163]\n\t"
    "ds_read_b128 a[28:31], v201 offset:26624\n\t"
    "s_nop 5\n\t"
    "v_mfma_f32_16x16x32_bf16 v[4:7], a[52:55], a[0:3], v[4:7]\n\t"
    "ds_read_b64_tr_b16 a[84:85], v203 offset:0\n\t"
    "v_mfma_f32_16x16x32_bf16 v[36:39], a[52:55], a[4:7], v[36:39]\n\t"
    "v_mfma_f32_16x16x32_bf16 v[68:71], a[52:55], a[8:11], v[68:71]\n\t"
    "ds_read_b64_tr_b16 a[86:87], v203 offset:4096\n\t"
    "buffer_load_dwordx4 v192, s[36:39], s56 offen offset:0 lds\n\t"
    "v_mfma_f32_16x16x32_bf16 v[100:103], a[52:55], a[12:15], v[100:103]\n\t"
    "v_mfma_f32_16x16x32_bf16 v[132:135], a[52:55], a[16:19], v[132:135]\n\t"
    "ds_read_b128 a[32:35], v201 offset:28672\n\t"
    "v_mfma_f32_16x16x32_bf16 v[164:167], a[52:55], a[20:23], v[164:167]\n\t"
    "ds_read_b64_tr_b16 a[88:89], v202 offset:256\n\t"
    "s_nop 5\n\t"
    "v_mfma_f32_16x16x32_bf16 v[8:11], a[56:59], a[0:3], v[8:11]\n\t"
    "buffer_load_dwordx4 v193, s[36:39], s56 offen offset:1024 lds\n\t"
    "v_mfma_f32_16x16x32_bf16 v[40:43], a[56:59], a[4:7], v[40:43]\n\t"
    "ds_read_b64_tr_b16 a[90:91], v202 offset:4352\n\t"
    "v_mfma_f32_16x16x32_bf16 v[72:75], a[56:59], a[8:11], v[72:75]\n\t"
    "v_mfma_f32_16x16x32_bf16 v[104:107], a[56:59], a[12:15], v[104:107]\n\t"
    "ds_read_b128 a[36:39], v201 offset:30720\n\t"
    "v_mfma_f32_16x16x32_bf16 v[136:139], a[56:59], a[16:19], v[136:139]\n\t"
    "ds_read_b64_tr_b16 a[92:93], v203 offset:256\n\t"
    "buffer_load_dwordx4 v194, s[36:39], s56 offen offset:2048 lds\n\t"
    "v_mfma_f32_16x16x32_bf16 v[168:171], a[56:59], a[20:23], v[168:171]\n\t"
    "s_nop 5\n\t"
    "v_mfma_f32_16x16x32_bf16 v[12:15], a[60:63], a[0:3], v[12:15]\n\t"
    "ds_read_b64_tr_b16 a[94:95], v203 offset:4352\n\t"
    "v_mfma_f32_16x16x32_bf16 v[44:47], a[60:63], a[4:7], v[44:47]\n\t"
    "v_mfma_f32_16x16x32_bf16 v[76:79], a[60:63], a[8:11], v[76:79]\n\t"
    "ds_read_b128 a[40:43], v201 offset:32768\n\t"
    "buffer_load_dwordx4 v195, s[36:39], s56 offen offset:3072 lds\n\t"
    "v_mfma_f32_16x16x32_bf16 v[108:111], a[60:63], a[12:15], v[108:111]\n\t"
    "ds_read_b64_tr_b16 a[96:97], v202 offset:512\n\t"
    "v_mfma_f32_16x16x32_bf16 v[140:143], a[60:63], a[16:19], v[140:143]\n\t"
    "v_mfma_f32_16x16x32_bf16 v[172:175], a[60:63], a[20:23], v[172:175]\n\t"
    "ds_read_b64_tr_b16 a[98:99], v202 offset:4608\n\t"
    "s_nop 5\n\t"
    "v_mfma_f32_16x16x32_bf16 v[16:19], a[64:67], a[0:3], v[16:19]\n\t"
    "s_addk_i32 m0, 4096\n\t"
    "v_mfma_f32_16x16x32_bf16 v[48:51], a[64:67], a[4:7], v[48:51]\n\t"
    "ds_read_b128 a[44:47], v201 offset:34816\n\t"
    "v_mfma_f32_16x16x32_bf16 v[80:83], a[64:67], a[8:11], v[80:83]\n\t"
    "ds_read_b64_tr_b16 a[100:101], v203 offset:512\n\t"
    "v_mfma_f32_16x16x32_bf16 v[112:115], a[64:67], a[12:15], v[112:115]\n\t"
    "v_mfma_f32_16x16x32_bf16 v[144:147], a[64:67], a[16:19], v[144:147]\n\t"
    "ds_read_b64_tr_b16 a[102:103], v203 offset:4608\n\t"
    "buffer_load_dwordx4 v192, s[36:39], s57 offen offset:0 lds\n\t"
    "v_mfma_f32_16x16x32_bf16 v[176:179], a[64:67], a[20:23], v[176:179]\n\t"
    "s_nop 5\n\t"
    "v_mfma_f32_16x16x32_bf16 v[20:23], a[68:71], a[0:3], v[20:23]\n\t"
    "ds_read_b64_tr_b16 a[104:105], v202 offset:768\n\t"
    "v_mfma_f32_16x16x32_bf16 v[52:55], a[68:71], a[4:7], v[52:55]\n\t"
    "ds_read_b64_tr_b16 a[106:107], v202 offset:4864\n\t"
    "v_mfma_f32_16x16x32_bf16 v[84:87], a[68:71], a[8:11], v[84:87]\n\t"
    "buffer_load_dwordx4 v193, s[36:39], s57 offen offset:1024 lds\n\t"
    "v_mfma_f32_16x16x32_bf16 v[116:119], a[68:71], a[12:15], v[116:119]\n\t"
    "ds_read_b64_tr_b16 a[108:109], v203 offset:768\n\t"
    "v_mfma_f32_16x16x32_bf16 v[148:151], a[68:71], a[16:19], v[148:151]\n\t"
    "v_mfma_f32_16x16x32_bf16 v[180:183], a[68:71], a[20:23], v[180:183]\n\t"
    "ds_read_b64_tr_b16 a[110:111], v203 offset:4864\n\t"
    "s_nop 5\n\t"
    "v_mfma_f32_16x16x32_bf16 v[24:27], a[72:75], a[0:3], v[24:27]\n\t"
    "s_addk_i32 s56, 128\n\t"
    "v_mfma_f32_16x16x32_bf16 v[56:59], a[72:75], a[4:7], v[56:59]\n\t"
    "v_mfma_f32_16x16x32_bf16 v[88:91], a[72:75], a[8:11], v[88:91]\n\t"
    "v_mfma_f32_16x16x32_bf16 v[120:123], a[72:75], a[12:15], v[120:123]\n\t"
    "v_mfma_f32_16x16x32_bf16 v[152:155], a[72:75], a[16:19], v[152:155]\n\t"
    "s_addk_i32 s57, 128\n\t"
    "v_mfma_f32_16x16x32_bf16 v[184:187], a[72:75], a[20:23], v[184:187]\n\t"
    "s_nop 5\n\t"
    "v_mfma_f32_16x16x32_bf16 v[28:31], a[76:79], a[0:3], v[28:31]\n\t"
    "v_mfma_f32_16x16x32_bf16 v[60:63], a[76:79], a[4:7], v[60:63]\n\t"
    "v_mfma_f32_16x16x32_bf16 v[92:95], a[76:79], a[8:11], v[92:95]\n\t"
    "v_add_u32 v253, 1540483477, v252\n\t"
    "v_accvgpr_write_b32 a253, v252\n\t"
    "buffer_store_dword v253, v219, s[24:27], 0 offen offset:0 sc1\n\t"
    "v_mfma_f32_16x16x32_bf16 v[124:127], a[76:79], a[12:15], v[124:127]\n\t"
    "v_mfma_f32_16x16x32_bf16 v[156:159], a[76:79], a[16:19], v[156:159]\n\t"
    "v_mfma_f32_16x16x32_bf16 v[188:191], a[76:79], a[20:23], v[188:191]\n\t"
    "s_nop 5\n\t"
    "s_waitcnt vmcnt(11) lgkmcnt(0)\n\t"
    "s_barrier\n\t"
    "v_mfma_f32_16x16x32_bf16 v[0:3], a[80:83], a[24:27], v[0:3]\n\t"
    "ds_read_b128 a[0:3], v200 offset:49152\n\t"
    "s_add_u32 m0, s61, 65536\n\t"
    "v_mfma_f32_16x16x32_bf16 v[32:35], a[80:83], a[28:31], v[32:35]\n\t"
    "ds_read_b64_tr_b16 a[48:49], v202 offset:16384\n\t"
    "v_mfma_f32_16x16x32_bf16 v[64:67], a[80:83], a[32:35], v[64:67]\n\t"
    "v_mfma_f32_16x16x32_bf16 v[96:99], a[80:83], a[36:39], v[96:99]\n\t"
    "ds_read_b64_tr_b16 a[50:51], v202 offset:20480\n\t"
    "v_mfma_f32_16x16x32_bf16 v[128:131], a[80:83], a[40:43], v[128:131]\n\t"
    "buffer_load_dwordx4 v196, s[40:43], s58 offen offset:0 lds\n\t"
    "v_mfma_f32_16x16x32_bf16 v[160:163], a[80:83], a[44:47], v[160:163]\n\t"
    "ds_read_b128 a[4:7], v200 offset:51200\n\t"
    "s_nop 5\n\t"
    "v_mfma_f32_16x16x32_bf16 v[4:7], a[84:87], a[24:27], v[4:7]\n\t"
    "ds_read_b64_tr_b16 a[52:53], v203 offset:16384\n\t"
    "v_mfma_f32_16x16x32_bf16 v[36:39], a[84:87], a[28:31], v[36:39]\n\t"
    "v_mfma_f32_16x16x32_bf16 v[68:71], a[84:87], a[32:35], v[68:71]\n\t"
    "ds_read_b64_tr_b16 a[54:55], v203 offset:20480\n\t"
    "buffer_load_dwordx4 v197, s[40:43], s58 offen offset:1024 lds\n\t"
    "v_mfma_f32_16x16x32_bf16 v[100:103], a[84:87], a[36:39], v[100:103]\n\t"
    "v_mfma_f32_16x16x32_bf16 v[132:135], a[84:87], a[40:43], v[132:135]\n\t"
    "ds_read_b128 a[8:11], v200 offset:53248\n\t"
    "v_mfma_f32_16x16x32_bf16 v[164:167], a[84:87], a[44:47], v[164:167]\n\t"
    "ds_read_b64_tr_b16 a[56:57], v202 offset:16640\n\t"
    "s_nop 5\n\t"
    "v_mfma_f32_16x16x32_bf16 v[8:11], a[88:91], a[24:27], v[8:11]\n\t"
    "buffer_load_dwordx4 v198, s[40:43], s58 offen offset:2048 lds\n\t"
    "v_mfma_f32_16x16x32_bf16 v[40:43], a[88:91], a[28:31], v[40:43]\n\t"
    "ds_read_b64_tr_b16 a[58:59], v202 offset:20736\n\t"
    "v_mfma_f32_16x16x32_bf16 v[72:75], a[88:91], a[32:35], v[72:75]\n\t"
    "v_mfma_f32_16x16x32_bf16 v[104:107], a[88:91], a[36:39], v[104:107]\n\t"
    "ds_read_b128 a[12:15], v200 offset:55296\n\t"
    "v_mfma_f32_16x16x32_bf16 v[136:139], a[88:91], a[40:43], v[136:139]\n\t"
    "ds_read_b64_tr_b16 a[60:61], v203 offset:16640\n\t"
    "buffer_load_dwordx4 v199, s[40:43], s58 offen offset:3072 lds\n\t"
    "v_mfma_f32_16x16x32_bf16 v[168:171], a[88:91], a[44:47], v[168:171]\n\t"
    "s_nop 5\n\t"
    "v_mfma_f32_16x16x32_bf16 v[12:15], a[92:95], a[24:27], v[12:15]\n\t"
    "ds_read_b64_tr_b16 a[62:63], v203 offset:20736\n\t"
    "v_mfma_f32_16x16x32_bf16 v[44:47], a[92:95], a[28:31], v[44:47]\n\t"
    "v_mfma_f32_16x16x32_bf16 v[76:79], a[92:95], a[32:35], v[76:79]\n\t"
    "ds_read_b128 a[16:19], v200 offset:57344\n\t"
    "s_add_u32 s58, s58, 262144\n\t"
    "v_mfma_f32_16x16x32_bf16 v[108:111], a[92:95], a[36:39], v[108:111]\n\t"
    "ds_read_b64_tr_b16 a[64:65], v202 offset:16896\n\t"
    "v_mfma_f32_16x16x32_bf16 v[140:143], a[92:95], a[40:43], v[140:143]\n\t"
    "v_mfma_f32_16x16x32_bf16 v[172:175], a[92:95], a[44:47], v[172:175]\n\t"
    "ds_read_b64_tr_b16 a[66:67], v202 offset:20992\n\t"
    "s_nop 5\n\t"
    "v_mfma_f32_16x16x32_bf16 v[16:19], a[96:99], a[24:27], v[16:19]\n\t"
    "s_add_u32 m0, s61, 0\n\t"
    "v_mfma_f32_16x16x32_bf16 v[48:51], a[96:99], a[28:31], v[48:51]\n\t"
    "ds_read_b128 a[20:23], v200 offset:59392\n\t"
    "v_mfma_f32_16x16x32_bf16 v[80:83], a[96:99], a[32:35], v[80:83]\n\t"
    "ds_read_b64_tr_b16 a[68:69], v203 offset:16896\n\t"
    "v_mfma_f32_16x16x32_bf16 v[112:115], a[96:99], a[36:39], v[112:115]\n\t"
    "v_mfma_f32_16x16x32_bf16 v[144:147], a[96:99], a[40:43], v[144:147]\n\t"
    "ds_read_b64_tr_b16 a[70:71], v203 offset:20992\n\t"
    "buffer_load_dwordx4 v196, s[40:43], s58 offen offset:0 lds\n\t"
    "v_mfma_f32_16x16x32_bf16 v[176:179], a[96:99], a[44:47], v[176:179]\n\t"
    "s_nop 5\n\t"
    "v_mfma_f32_16x16x32_bf16 v[20:23], a[100:103], a[24:27], v[20:23]\n\t"
    "ds_read_b64_tr_b16 a[72:73], v202 offset:17152\n\t"
    "v_mfma_f32_16x16x32_bf16 v[52:55], a[100:103], a[28:31], v[52:55]\n\t"
    "ds_read_b64_tr_b16 a[74:75], v202 offset:21248\n\t"
    "v_mfma_f32_16x16x32_bf16 v[84:87], a[100:103], a[32:35], v[84:87]\n\t"
    "buffer_load_dwordx4 v197, s[40:43], s58 offen offset:1024 lds\n\t"
    "v_mfma_f32_16x16x32_bf16 v[116:119], a[100:103], a[36:39], v[116:119]\n\t"
    "ds_read_b64_tr_b16 a[76:77], v203 offset:17152\n\t"
    "v_mfma_f32_16x16x32_bf16 v[148:151], a[100:103], a[40:43], v[148:151]\n\t"
    "v_mfma_f32_16x16x32_bf16 v[180:183], a[100:103], a[44:47], v[180:183]\n\t"
    "ds_read_b64_tr_b16 a[78:79], v203 offset:21248\n\t"
    "s_nop 5\n\t"
    "v_mfma_f32_16x16x32_bf16 v[24:27], a[104:107], a[24:27], v[24:27]\n\t"
    "buffer_load_dwordx4 v198, s[40:43], s58 offen offset:2048 lds\n\t"
    "v_mfma_f32_16x16x32_bf16 v[56:59], a[104:107], a[28:31], v[56:59]\n\t"
    "v_mfma_f32_16x16x32_bf16 v[88:91], a[104:107], a[32:35], v[88:91]\n\t"
    "v_mfma_f32_16x16x32_bf16 v[120:123], a[104:107], a[36:39], v[120:123]\n\t"
    "v_mfma_f32_16x16x32_bf16 v[152:155], a[104:107], a[40:43], v[152:155]\n\t"
    "buffer_load_dwordx4 v199, s[40:43], s58 offen offset:3072 lds\n\t"
    "v_mfma_f32_16x16x32_bf16 v[184:187], a[104:107], a[44:47], v[184:187]\n\t"
    "s_nop 5\n\t"
    "v_mfma_f32_16x16x32_bf16 v[28:31], a[108:111], a[24:27], v[28:31]\n\t"
    "v_mfma_f32_16x16x32_bf16 v[60:63], a[108:111], a[28:31], v[60:63]\n\t"
    "v_mfma_f32_16x16x32_bf16 v[92:95], a[108:111], a[32:35], v[92:95]\n\t"
    "s_add_u32 s58, s58, 262144\n\t"
    "v_mfma_f32_16x16x32_bf16 v[124:127], a[108:111], a[36:39], v[124:127]\n\t"
    "v_mfma_f32_16x16x32_bf16 v[156:159], a[108:111], a[40:43], v[156:159]\n\t"
    "v_mfma_f32_16x16x32_bf16 v[188:191], a[108:111], a[44:47], v[188:191]\n\t"
    "s_nop 5\n\t"
    "s_waitcnt lgkmcnt(0)\n\t"
    "v_mfma_f32_16x16x32_bf16 v[0:3], a[48:51], a[0:3], v[0:3]\n\t"
    "ds_read_b128 a[24:27], v201 offset:49152\n\t"
    "s_add_u32 m0, s60, 24576\n\t"
    "v_mfma_f32_16x16x32_bf16 v[32:35], a[48:51], a[4:7], v[32:35]\n\t"
    "ds_read_b64_tr_b16 a[80:81], v202 offset:32768\n\t"
    "v_mfma_f32_16x16x32_bf16 v[64:67], a[48:51], a[8:11], v[64:67]\n\t"
    "v_mfma_f32_16x16x32_bf16 v[96:99], a[48:51], a[12:15], v[96:99]\n\t"
    "ds_read_b64_tr_b16 a[82:83], v202 offset:36864\n\t"
    "v_mfma_f32_16x16x32_bf16 v[128:131], a[48:51], a[16:19], v[128:131]\n\t"
    "buffer_load_dwordx4 v192, s[36:39], s56 offen offset:0 lds\n\t"
    "v_mfma_f32_16x16x32_bf16 v[160:163], a[48:51], a[20:23], v[160:163]\n\t"
    "ds_read_b128 a[28:31], v201 offset:51200\n\t"
    "s_nop 4\n\t"
    "v_mfma_f32_16x16x32_bf16 v[4:7], a[52:55], a[0:3], v[4:7]\n\t"
    "ds_read_b64_tr_b16 a[84:85], v203 offset:32768\n\t"
    "v_mfma_f32_16x16x32_bf16 v[36:39], a[52:55], a[4:7], v[36:39]\n\t"
    "v_mfma_f32_16x16x32_bf16 v[68:71], a[52:55], a[8:11], v[68:71]\n\t"
    "ds_read_b64_tr_b16 a[86:87], v203 offset:36864\n\t"
    "buffer_load_dwordx4 v193, s[36:39], s56 offen offset:1024 lds\n\t"
    "v_mfma_f32_16x16x32_bf16 v[100:103], a[52:55], a[12:15], v[100:103]\n\t"
    "v_mfma_f32_16x16x32_bf16 v[132:135], a[52:55], a[16:19], v[132:135]\n\t"
    "ds_read_b128 a[32:35], v201 offset:53248\n\t"
    "v_mfma_f32_16x16x32_bf16 v[164:167], a[52:55], a[20:23], v[164:167]\n\t"
    "ds_read_b64_tr_b16 a[88:89], v202 offset:33024\n\t"
    "s_nop 4\n\t"
    "v_mfma_f32_16x16x32_bf16 v[8:11], a[56:59], a[0:3], v[8:11]\n\t"
    "buffer_load_dwordx4 v194, s[36:39], s56 offen offset:2048 lds\n\t"
    "v_mfma_f32_16x16x32_bf16 v[40:43], a[56:59], a[4:7], v[40:43]\n\t"
    "ds_read_b64_tr_b16 a[90:91], v202 offset:37120\n\t"
    "v_mfma_f32_16x16x32_bf16 v[72:75], a[56:59], a[8:11], v[72:75]\n\t"
    "v_mfma_f32_16x16x32_bf16 v[104:107], a[56:59], a[12:15], v[104:107]\n\t"
    "ds_read_b128 a[36:39], v201 offset:55296\n\t"
    "v_mfma_f32_16x16x32_bf16 v[136:139], a[56:59], a[16:19], v[136:139]\n\t"
    "ds_read_b64_tr_b16 a[92:93], v203 offset:33024\n\t"
    "buffer_load_dwordx4 v195, s[36:39], s56 offen offset:3072 lds\n\t"
    "v_mfma_f32_16x16x32_bf16 v[168:171], a[56:59], a[20:23], v[168:171]\n\t"
    "s_nop 4\n\t"
    "v_mfma_f32_16x16x32_bf16 v[12:15], a[60:63], a[0:3], v[12:15]\n\t"
    "ds_read_b64_tr_b16 a[94:95], v203 offset:37120\n\t"
    "v_mfma_f32_16x16x32_bf16 v[44:47], a[60:63], a[4:7], v[44:47]\n\t"
    "v_mfma_f32_16x16x32_bf16 v[76:79], a[60:63], a[8:11], v[76:79]\n\t"
    "ds_read_b128 a[40:43], v201 offset:57344\n\t"
    "s_addk_i32 m0, 4096\n\t"
    "v_mfma_f32_16x16x32_bf16 v[108:111], a[60:63], a[12:15], v[108:111]\n\t"
    "ds_read_b64_tr_b16 a[96:97], v202 offset:33280\n\t"
    "v_mfma_f32_16x16x32_bf16 v[140:143], a[60:63], a[16:19], v[140:143]\n\t"
    "v_mfma_f32_16x16x32_bf16 v[172:175], a[60:63], a[20:23], v[172:175]\n\t"
    "ds_read_b64_tr_b16 a[98:99], v202 offset:37376\n\t"
    "s_nop 4\n\t"
    "v_mfma_f32_16x16x32_bf16 v[16:19], a[64:67], a[0:3], v[16:19]\n\t"
    "buffer_load_dwordx4 v192, s[36:39], s57 offen offset:0 lds\n\t"
    "v_mfma_f32_16x16x32_bf16 v[48:51], a[64:67], a[4:7], v[48:51]\n\t"
    "ds_read_b128 a[44:47], v201 offset:59392\n\t"
    "v_mfma_f32_16x16x32_bf16 v[80:83], a[64:67], a[8:11], v[80:83]\n\t"
    "ds_read_b64_tr_b16 a[100:101], v203 offset:33280\n\t"
    "v_mfma_f32_16x16x32_bf16 v[112:115], a[64:67], a[12:15], v[112:115]\n\t"
    "v_mfma_f32_16x16x32_bf16 v[144:147], a[64:67], a[16:19], v[144:147]\n\t"
    "ds_read_b64_tr_b16 a[102:103], v203 offset:37376\n\t"
    "buffer_load_dwordx4 v193, s[36:39], s57 offen offset:1024 lds\n\t"
    "v_mfma_f32_16x16x32_bf16 v[176:179], a[64:67], a[20:23], v[176:179]\n\t"
    "s_nop 4\n\t"
    "v_mfma_f32_16x16x32_bf16 v[20:23], a[68:71], a[0:3], v[20:23]\n\t"
    "ds_read_b64_tr_b16 a[104:105], v202 offset:33536\n\t"
    "v_mfma_f32_16x16x32_bf16 v[52:55], a[68:71], a[4:7], v[52:55]\n\t"
    "ds_read_b64_tr_b16 a[106:107], v202 offset:37632\n\t"
    "v_mfma_f32_16x16x32_bf16 v[84:87], a[68:71], a[8:11], v[84:87]\n\t"
    "s_addk_i32 s56, 128\n\t"
    "v_mfma_f32_16x16x32_bf16 v[116:119], a[68:71], a[12:15], v[116:119]\n\t"
    "ds_read_b64_tr_b16 a[108:109], v203 offset:33536\n\t"
    "v_mfma_f32_16x16x32_bf16 v[148:151], a[68:71], a[16:19], v[148:151]\n\t"
    "v_mfma_f32_16x16x32_bf16 v[180:183], a[68:71], a[20:23], v[180:183]\n\t"
    "ds_read_b64_tr_b16 a[110:111], v203 offset:37632\n\t"
    "s_nop 4\n\t"
    "v_mfma_f32_16x16x32_bf16 v[24:27], a[72:75], a[0:3], v[24:27]\n\t"
    "s_addk_i32 s57, 128\n\t"
    "v_mfma_f32_16x16x32_bf16 v[56:59], a[72:75], a[4:7], v[56:59]\n\t"
    "v_mfma_f32_16x16x32_bf16 v[88:91], a[72:75], a[8:11], v[88:91]\n\t"
    "v_mfma_f32_16x16x32_bf16 v[120:123], a[72:75], a[12:15], v[120:123]\n\t"
    "v_mfma_f32_16x16x32_bf16 v[152:155], a[72:75], a[16:19], v[152:155]\n\t"
    "buffer_load_dwordx4 a[112:115], v204, s[44:47], s62 offen offset:0 nt\n\t"
    "v_mfma_f32_16x16x32_bf16 v[184:187], a[72:75], a[20:23], v[184:187]\n\t"
    "s_nop 4\n\t"
    "v_mfma_f32_16x16x32_bf16 v[28:31], a[76:79], a[0:3], v[28:31]\n\t"
    "v_mfma_f32_16x16x32_bf16 v[60:63], a[76:79], a[4:7], v[60:63]\n\t"
    "v_mfma_f32_16x16x32_bf16 v[92:95], a[76:79], a[8:11], v[92:95]\n\t"
    "buffer_load_dwordx4 a[116:119], v205, s[44:47], s62 offen offset:1024 nt\n\t"
    "v_mfma_f32_16x16x32_bf16 v[124:127], a[76:79], a[12:15], v[124:127]\n\t"
    "v_mfma_f32_16x16x32_bf16 v[156:159], a[76:79], a[16:19], v[156:159]\n\t"
    "v_mfma_f32_16x16x32_bf16 v[188:191], a[76:79], a[20:23], v[188:191]\n\t"
    "s_nop 4\n\t"
    "s_waitcnt vmcnt(12) lgkmcnt(0)\n\t"
    "s_barrier\n\t"
    "v_mfma_f32_16x16x32_bf16 v[0:3], a[80:83], a[24:27], v[0:3]\n\t"
    "ds_read_b128 a[0:3], v200 offset:0\n\t"
    "s_add_u32 m0, s61, 16384\n\t"
    "v_mfma_f32_16x16x32_bf16 v[32:35], a[80:83], a[28:31], v[32:35]\n\t"
    "ds_read_b64_tr_b16 a[48:49], v202 offset:49152\n\t"
    "v_mfma_f32_16x16x32_bf16 v[64:67], a[80:83], a[32:35], v[64:67]\n\t"
    "v_mfma_f32_16x16x32_bf16 v[96:99], a[80:83], a[36:39], v[96:99]\n\t"
    "ds_read_b64_tr_b16 a[50:51], v202 offset:53248\n\t"
    "v_mfma_f32_16x16x32_bf16 v[128:131], a[80:83], a[40:43], v[128:131]\n\t"
    "buffer_load_dwordx4 v196, s[40:43], s58 offen offset:0 lds\n\t"
    "v_mfma_f32_16x16x32_bf16 v[160:163], a[80:83], a[44:47], v[160:163]\n\t"
    "ds_read_b128 a[4:7], v200 offset:2048\n\t"
    "s_nop 4\n\t"
    "v_mfma_f32_16x16x32_bf16 v[4:7], a[84:87], a[24:27], v[4:7]\n\t"
    "ds_read_b64_tr_b16 a[52:53], v203 offset:49152\n\t"
    "v_mfma_f32_16x16x32_bf16 v[36:39], a[84:87], a[28:31], v[36:39]\n\t"
    "v_mfma_f32_16x16x32_bf16 v[68:71], a[84:87], a[32:35], v[68:71]\n\t"
    "ds_read_b64_tr_b16 a[54:55], v203 offset:53248\n\t"
    "buffer_load_dwordx4 v197, s[40:43], s58 offen offset:1024 lds\n\t"
    "v_mfma_f32_16x16x32_bf16 v[100:103], a[84:87], a[36:39], v[100:103]\n\t"
    "v_mfma_f32_16x16x32_bf16 v[132:135], a[84:87], a[40:43], v[132:135]\n\t"
    "ds_read_b128 a[8:11], v200 offset:4096\n\t"
    "v_mfma_f32_16x16x32_bf16 v[164:167], a[84:87], a[44:47], v[164:167]\n\t"
    "ds_read_b64_tr_b16 a[56:57], v202 offset:49408\n\t"
    "s_nop 4\n\t"
    "v_mfma_f32_16x16x32_bf16 v[8:11], a[88:91], a[24:27], v[8:11]\n\t"
    "buffer_load_dwordx4 v198, s[40:43], s58 offen offset:2048 lds\n\t"
    "v_mfma_f32_16x16x32_bf16 v[40:43], a[88:91], a[28:31], v[40:43]\n\t"
    "ds_read_b64_tr_b16 a[58:59], v202 offset:53504\n\t"
    "v_mfma_f32_16x16x32_bf16 v[72:75], a[88:91], a[32:35], v[72:75]\n\t"
    "v_mfma_f32_16x16x32_bf16 v[104:107], a[88:91], a[36:39], v[104:107]\n\t"
    "ds_read_b128 a[12:15], v200 offset:6144\n\t"
    "v_mfma_f32_16x16x32_bf16 v[136:139], a[88:91], a[40:43], v[136:139]\n\t"
    "ds_read_b64_tr_b16 a[60:61], v203 offset:49408\n\t"
    "buffer_load_dwordx4 v199, s[40:43], s58 offen offset:3072 lds\n\t"
    "v_mfma_f32_16x16x32_bf16 v[168:171], a[88:91], a[44:47], v[168:171]\n\t"
    "s_nop 4\n\t"
    "v_mfma_f32_16x16x32_bf16 v[12:15], a[92:95], a[24:27], v[12:15]\n\t"
    "ds_read_b64_tr_b16 a[62:63], v203 offset:53504\n\t"
    "v_mfma_f32_16x16x32_bf16 v[44:47], a[92:95], a[28:31], v[44:47]\n\t"
    "v_mfma_f32_16x16x32_bf16 v[76:79], a[92:95], a[32:35], v[76:79]\n\t"
    "ds_read_b128 a[16:19], v200 offset:8192\n\t"
    "s_add_u32 s58, s58, 262144\n\t"
    "v_mfma_f32_16x16x32_bf16 v[108:111], a[92:95], a[36:39], v[108:111]\n\t"
    "ds_read_b64_tr_b16 a[64:65], v202 offset:49664\n\t"
    "v_mfma_f32_16x16x32_bf16 v[140:143], a[92:95], a[40:43], v[140:143]\n\t"
    "v_mfma_f32_16x16x32_bf16 v[172:175], a[92:95], a[44:47], v[172:175]\n\t"
    "ds_read_b64_tr_b16 a[66:67], v202 offset:53760\n\t"
    "s_nop 4\n\t"
    "v_mfma_f32_16x16x32_bf16 v[16:19], a[96:99], a[24:27], v[16:19]\n\t"
    "s_add_u32 m0, s61, 32768\n\t"
    "v_mfma_f32_16x16x32_bf16 v[48:51], a[96:99], a[28:31], v[48:51]\n\t"
    "ds_read_b128 a[20:23], v200 offset:10240\n\t"
    "v_mfma_f32_16x16x32_bf16 v[80:83], a[96:99], a[32:35], v[80:83]\n\t"
    "ds_read_b64_tr_b16 a[68:69], v203 offset:49664\n\t"
    "v_mfma_f32_16x16x32_bf16 v[112:115], a[96:99], a[36:39], v[112:115]\n\t"
    "v_mfma_f32_16x16x32_bf16 v[144:147], a[96:99], a[40:43], v[144:147]\n\t"
    "ds_read_b64_tr_b16 a[70:71], v203 offset:53760\n\t"
    "buffer_load_dwordx4 v196, s[40:43], s58 offen offset:0 lds\n\t"
    "v_mfma_f32_16x16x32_bf16 v[176:179], a[96:99], a[44:47], v[176:179]\n\t"
    "s_nop 4\n\t"
    "v_mfma_f32_16x16x32_bf16 v[20:23], a[100:103], a[24:27], v[20:23]\n\t"
    "ds_read_b64_tr_b16 a[72:73], v202 offset:49920\n\t"
    "v_mfma_f32_16x16x32_bf16 v[52:55], a[100:103], a[28:31], v[52:55]\n\t"
    "ds_read_b64_tr_b16 a[74:75], v202 offset:54016\n\t"
    "v_mfma_f32_16x16x32_bf16 v[84:87], a[100:103], a[32:35], v[84:87]\n\t"
    "buffer_load_dwordx4 v197, s[40:43], s58 offen offset:1024 lds\n\t"
    "v_mfma_f32_16x16x32_bf16 v[116:119], a[100:103], a[36:39], v[116:119]\n\t"
    "ds_read_b64_tr_b16 a[76:77], v203 offset:49920\n\t"
    "v_mfma_f32_16x16x32_bf16 v[148:151], a[100:103], a[40:43], v[148:151]\n\t"
    "v_mfma_f32_16x16x32_bf16 v[180:183], a[100:103], a[44:47], v[180:183]\n\t"
    "ds_read_b64_tr_b16 a[78:79], v203 offset:54016\n\t"
    "s_nop 4\n\t"
    "v_mfma_f32_16x16x32_bf16 v[24:27], a[104:107], a[24:27], v[24:27]\n\t"
    "buffer_load_dwordx4 v198, s[40:43], s58 offen offset:2048 lds\n\t"
    "v_mfma_f32_16x16x32_bf16 v[56:59], a[104:107], a[28:31], v[56:59]\n\t"
    "v_mfma_f32_16x16x32_bf16 v[88:91], a[104:107], a[32:35], v[88:91]\n\t"
    "v_mfma_f32_16x16x32_bf16 v[120:123], a[104:107], a[36:39], v[120:123]\n\t"
    "v_mfma_f32_16x16x32_bf16 v[152:155], a[104:107], a[40:43], v[152:155]\n\t"
    "buffer_load_dwordx4 v199, s[40:43], s58 offen offset:3072 lds\n\t"
    "v_mfma_f32_16x16x32_bf16 v[184:187], a[104:107], a[44:47], v[184:187]\n\t"
    "s_nop 4\n\t"
    "v_mfma_f32_16x16x32_bf16 v[28:31], a[108:111], a[24:27], v[28:31]\n\t"
    "v_mfma_f32_16x16x32_bf16 v[60:63], a[108:111], a[28:31], v[60:63]\n\t"
    "v_mfma_f32_16x16x32_bf16 v[92:95], a[108:111], a[32:35], v[92:95]\n\t"
    "s_add_u32 s58, s58, 262144\n\t"
    "v_mfma_f32_16x16x32_bf16 v[124:127], a[108:111], a[36:39], v[124:127]\n\t"
    "v_mfma_f32_16x16x32_bf16 v[156:159], a[108:111], a[40:43], v[156:159]\n\t"
    "v_mfma_f32_16x16x32_bf16 v[188:191], a[108:111], a[44:47], v[188:191]\n\t"
    "s_nop 4\n\t"
    "s_waitcnt lgkmcnt(0)\n\t"
    "v_mfma_f32_16x16x32_bf16 v[0:3], a[48:51], a[0:3], v[0:3]\n\t"
    "ds_read_b128 a[24:27], v201 offset:0\n\t"
    "s_add_u32 m0, s60, 49152\n\t"
    "v_mfma_f32_16x16x32_bf16 v[32:35], a[48:51], a[4:7], v[32:35]\n\t"
    "ds_read_b64_tr_b16 a[80:81], v248 offset:0\n\t"
    "v_mfma_f32_16x16x32_bf16 v[64:67], a[48:51], a[8:11], v[64:67]\n\t"
    "v_mfma_f32_16x16x32_bf16 v[96:99], a[48:51], a[12:15], v[96:99]\n\t"
    "ds_read_b64_tr_b16 a[82:83], v248 offset:4096\n\t"
    "buffer_load_dwordx4 v192, s[36:39], s56 offen offset:0 lds\n\t"
    "v_mfma_f32_16x16x32_bf16 v[128:131], a[48:51], a[16:19], v[128:131]\n\t"
    "v_mfma_f32_16x16x32_bf16 v[160:163], a[48:51], a[20:23], v[160:163]\n\t"
    "ds_read_b128 a[28:31], v201 offset:2048\n\t"
    "s_nop 3\n\t"
    "v_mfma_f32_16x16x32_bf16 v[4:7], a[52:55], a[0:3], v[4:7]\n\t"
    "ds_read_b64_tr_b16 a[84:85], v249 offset:0\n\t"
    "v_mfma_f32_16x16x32_bf16 v[36:39], a[52:55], a[4:7], v[36:39]\n\t"
    "buffer_load_dwordx4 v193, s[36:39], s56 offen offset:1024 lds\n\t"
    "v_mfma_f32_16x16x32_bf16 v[68:71], a[52:55], a[8:11], v[68:71]\n\t"
    "ds_read_b64_tr_b16 a[86:87], v249 offset:4096\n\t"
    "v_mfma_f32_16x16x32_bf16 v[100:103], a[52:55], a[12:15], v[100:103]\n\t"
    "v_mfma_f32_16x16x32_bf16 v[132:135], a[52:55], a[16:19], v[132:135]\n\t"
    "ds_read_b128 a[32:35], v201 offset:4096\n\t"
    "v_mfma_f32_16x16x32_bf16 v[164:167], a[52:55], a[20:23], v[164:167]\n\t"
    "ds_read_b64_tr_b16 a[88:89], v248 offset:256\n\t"
    "buffer_load_dwordx4 v194, s[36:39], s56 offen offset:2048 lds\n\t"
    "s_nop 3\n\t"
    "v_mfma_f32_16x16x32_bf16 v[8:11], a[56:59], a[0:3], v[8:11]\n\t"
    "v_mfma_f32_16x16x32_bf16 v[40:43], a[56:59], a[4:7], v[40:43]\n\t"
    "ds_read_b64_tr_b16 a[90:91], v248 offset:4352\n\t"
    "v_mfma_f32_16x16x32_bf16 v[72:75], a[56:59], a[8:11], v[72:75]\n\t"
    "buffer_load_dwordx4 v195, s[36:39], s56 offen offset:3072 lds\n\t"
    "v_mfma_f32_16x16x32_bf16 v[104:107], a[56:59], a[12:15], v[104:107]\n\t"
    "ds_read_b128 a[36:39], v201 offset:6144\n\t"
    "v_mfma_f32_16x16x32_bf16 v[136:139], a[56:59], a[16:19], v[136:139]\n\t"
    "ds_read_b64_tr_b16 a[92:93], v249 offset:256\n\t"
    "v_mfma_f32_16x16x32_bf16 v[168:171], a[56:59], a[20:23], v[168:171]\n\t"
    "s_nop 3\n\t"
    "v_mfma_f32_16x16x32_bf16 v[12:15], a[60:63], a[0:3], v[12:15]\n\t"
    "ds_read_b64_tr_b16 a[94:95], v249 offset:4352\n\t"
    "s_addk_i32 m0, 4096\n\t"
    "v_mfma_f32_16x16x32_bf16 v[44:47], a[60:63], a[4:7], v[44:47]\n\t"
    "v_mfma_f32_16x16x32_bf16 v[76:79], a[60:63], a[8:11], v[76:79]\n\t"
    "ds_read_b128 a[40:43], v201 offset:8192\n\t"
    "v_mfma_f32_16x16x32_bf16 v[108:111], a[60:63], a[12:15], v[108:111]\n\t"
    "ds_read_b64_tr_b16 a[96:97], v248 offset:512\n\t"
    "v_mfma_f32_16x16x32_bf16 v[140:143], a[60:63], a[16:19], v[140:143]\n\t"
    "buffer_load_dwordx4 v192, s[36:39], s57 offen offset:0 lds\n\t"
    "v_mfma_f32_16x16x32_bf16 v[172:175], a[60:63], a[20:23], v[172:175]\n\t"
    "ds_read_b64_tr_b16 a[98:99], v248 offset:4608\n\t"
    "s_nop 3\n\t"
    "v_mfma_f32_16x16x32_bf16 v[16:19], a[64:67], a[0:3], v[16:19]\n\t"
    "v_mfma_f32_16x16x32_bf16 v[48:51], a[64:67], a[4:7], v[48:51]\n\t"
    "ds_read_b128 a[44:47], v201 offset:10240\n\t"
    "buffer_load_dwordx4 v193, s[36:39], s57 offen offset:1024 lds\n\t"
    "v_mfma_f32_16x16x32_bf16 v[80:83], a[64:67], a[8:11], v[80:83]\n\t"
    "ds_read_b64_tr_b16 a[100:101], v249 offset:512\n\t"
    "v_mfma_f32_16x16x32_bf16 v[112:115], a[64:67], a[12:15], v[112:115]\n\t"
    "v_mfma_f32_16x16x32_bf16 v[144:147], a[64:67], a[16:19], v[144:147]\n\t"
    "ds_read_b64_tr_b16 a[102:103], v249 offset:4608\n\t"
    "v_mfma_f32_16x16x32_bf16 v[176:179], a[64:67], a[20:23], v[176:179]\n\t"
    "s_addk_i32 s56, 128\n\t"
    "s_nop 3\n\t"
    "v_mfma_f32_16x16x32_bf16 v[20:23], a[68:71], a[0:3], v[20:23]\n\t"
    "ds_read_b64_tr_b16 a[104:105], v248 offset:768\n\t"
    "v_mfma_f32_16x16x32_bf16 v[52:55], a[68:71], a[4:7], v[52:55]\n\t"
    "ds_read_b64_tr_b16 a[106:107], v248 offset:4864\n\t"
    "v_mfma_f32_16x16x32_bf16 v[84:87], a[68:71], a[8:11], v[84:87]\n\t"
    "v_mfma_f32_16x16x32_bf16 v[116:119], a[68:71], a[12:15], v[116:119]\n\t"
    "ds_read_b64_tr_b16 a[108:109], v249 offset:768\n\t"
    "s_addk_i32 s57, 128\n\t"
    "v_mfma_f32_16x16x32_bf16 v[148:151], a[68:71], a[16:19], v[148:151]\n\t"
    "v_mfma_f32_16x16x32_bf16 v[180:183], a[68:71], a[20:23], v[180:183]\n\t"
    "ds_read_b64_tr_b16 a[110:111], v249 offset:4864\n\t"
    "s_nop 3\n\t"
    "v_mfma_f32_16x16x32_bf16 v[24:27], a[72:75], a[0:3], v[24:27]\n\t"
    "buffer_load_dwordx4 a[120:123], v206, s[44:47], s62 offen offset:2048 nt\n\t"
    "v_mfma_f32_16x16x32_bf16 v[56:59], a[72:75], a[4:7], v[56:59]\n\t"
    "v_mfma_f32_16x16x32_bf16 v[88:91], a[72:75], a[8:11], v[88:91]\n\t"
    "v_mfma_f32_16x16x32_bf16 v[120:123], a[72:75], a[12:15], v[120:123]\n\t"
    "v_mfma_f32_16x16x32_bf16 v[152:155], a[72:75], a[16:19], v[152:155]\n\t"
    "buffer_load_dwordx4 a[124:127], v207, s[44:47], s62 offen offset:3072 nt\n\t"
    "v_mfma_f32_16x16x32_bf16 v[184:187], a[72:75], a[20:23], v[184:187]\n\t"
    "s_nop 3\n\t"
    "v_mfma_f32_16x16x32_bf16 v[28:31], a[76:79], a[0:3], v[28:31]\n\t"
    "v_mfma_f32_16x16x32_bf16 v[60:63], a[76:79], a[4:7], v[60:63]\n\t"
    "v_mfma_f32_16x16x32_bf16 v[92:95], a[76:79], a[8:11], v[92:95]\n\t"
    "buffer_load_dwordx4 a[128:131], v208, s[48:51], s62 offen offset:0 nt\n\t"
    "v_mfma_f32_16x16x32_bf16 v[124:127], a[76:79], a[12:15], v[124:127]\n\t"
    "v_mfma_f32_16x16x32_bf16 v[156:159], a[76:79], a[16:19], v[156:159]\n\t"
    "v_mfma_f32_16x16x32_bf16 v[188:191], a[76:79], a[20:23], v[188:191]\n\t"
    "s_nop 3\n\t"
    "s_waitcnt vmcnt(13) lgkmcnt(0)\n\t"
    "s_barrier\n\t"
    "v_mfma_f32_16x16x32_bf16 v[0:3], a[80:83], a[24:27], v[0:3]\n\t"
    "ds_read_b128 a[0:3], v200 offset:24576\n\t"
    "s_add_u32 m0, s61, 49152\n\t"
    "v_mfma_f32_16x16x32_bf16 v[32:35], a[80:83], a[28:31], v[32:35]\n\t"
    "ds_read_b64_tr_b16 a[48:49], v202 offset:0\n\t"
    "v_mfma_f32_16x16x32_bf16 v[64:67], a[80:83], a[32:35], v[64:67]\n\t"
    "v_mfma_f32_16x16x32_bf16 v[96:99], a[80:83], a[36:39], v[96:99]\n\t"
    "ds_read_b64_tr_b16 a[50:51], v202 offset:4096\n\t"
    "buffer_load_dwordx4 v196, s[40:43], s58 offen offset:0 lds\n\t"
    "v_mfma_f32_16x16x32_bf16 v[128:131], a[80:83], a[40:43], v[128:131]\n\t"
    "v_mfma_f32_16x16x32_bf16 v[160:163], a[80:83], a[44:47], v[160:163]\n\t"
    "ds_read_b128 a[4:7], v200 offset:26624\n\t"
    "s_nop 3\n\t"
    "v_mfma_f32_16x16x32_bf16 v[4:7], a[84:87], a[24:27], v[4:7]\n\t"
    "ds_read_b64_tr_b16 a[52:53], v203 offset:0\n\t"
    "v_mfma_f32_16x16x32_bf16 v[36:39], a[84:87], a[28:31], v[36:39]\n\t"
    "buffer_load_dwordx4 v197, s[40:43], s58 offen offset:1024 lds\n\t"
    "v_mfma_f32_16x16x32_bf16 v[68:71], a[84:87], a[32:35], v[68:71]\n\t"
    "ds_read_b64_tr_b16 a[54:55], v203 offset:4096\n\t"
    "v_mfma_f32_16x16x32_bf16 v[100:103], a[84:87], a[36:39], v[100:103]\n\t"
    "v_mfma_f32_16x16x32_bf16 v[132:135], a[84:87], a[40:43], v[132:135]\n\t"
    "ds_read_b128 a[8:11], v200 offset:28672\n\t"
    "v_mfma_f32_16x16x32_bf16 v[164:167], a[84:87], a[44:47], v[164:167]\n\t"
    "ds_read_b64_tr_b16 a[56:57], v202 offset:256\n\t"
    "buffer_load_dwordx4 v198, s[40:43], s58 offen offset:2048 lds\n\t"
    "s_nop 3\n\t"
    "v_mfma_f32_16x16x32_bf16 v[8:11], a[88:91], a[24:27], v[8:11]\n\t"
    "v_mfma_f32_16x16x32_bf16 v[40:43], a[88:91], a[28:31], v[40:43]\n\t"
    "ds_read_b64_tr_b16 a[58:59], v202 offset:4352\n\t"
    "v_mfma_f32_16x16x32_bf16 v[72:75], a[88:91], a[32:35], v[72:75]\n\t"
    "buffer_load_dwordx4 v199, s[40:43], s58 offen offset:3072 lds\n\t"
    "v_mfma_f32_16x16x32_bf16 v[104:107], a[88:91], a[36:39], v[104:107]\n\t"
    "ds_read_b128 a[12:15], v200 offset:30720\n\t"
    "v_mfma_f32_16x16x32_bf16 v[136:139], a[88:91], a[40:43], v[136:139]\n\t"
    "ds_read_b64_tr_b16 a[60:61], v203 offset:256\n\t"
    "v_mfma_f32_16x16x32_bf16 v[168:171], a[88:91], a[44:47], v[168:171]\n\t"
    "s_nop 3\n\t"
    "v_mfma_f32_16x16x32_bf16 v[12:15], a[92:95], a[24:27], v[12:15]\n\t"
    "ds_read_b64_tr_b16 a[62:63], v203 offset:4352\n\t"
    "s_add_u32 s58, s58, 262144\n\t"
    "v_mfma_f32_16x16x32_bf16 v[44:47], a[92:95], a[28:31], v[44:47]\n\t"
    "v_mfma_f32_16x16x32_bf16 v[76:79], a[92:95], a[32:35], v[76:79]\n\t"
    "ds_read_b128 a[16:19], v200 offset:32768\n\t"
    "v_mfma_f32_16x16x32_bf16 v[108:111], a[92:95], a[36:39], v[108:111]\n\t"
    "ds_read_b64_tr_b16 a[64:65], v202 offset:512\n\t"
    "v_mfma_f32_16x16x32_bf16 v[140:143], a[92:95], a[40:43], v[140:143]\n\t"
    "s_add_u32 m0, s61, 65536\n\t"
    "v_mfma_f32_16x16x32_bf16 v[172:175], a[92:95], a[44:47], v[172:175]\n\t"
    "ds_read_b64_tr_b16 a[66:67], v202 offset:4608\n\t"
    "s_nop 3\n\t"
    "v_mfma_f32_16x16x32_bf16 v[16:19], a[96:99], a[24:27], v[16:19]\n\t"
    "v_mfma_f32_16x16x32_bf16 v[48:51], a[96:99], a[28:31], v[48:51]\n\t"
    "ds_read_b128 a[20:23], v200 offset:34816\n\t"
    "buffer_load_dwordx4 v196, s[40:43], s58 offen offset:0 lds\n\t"
    "v_mfma_f32_16x16x32_bf16 v[80:83], a[96:99], a[32:35], v[80:83]\n\t"
    "ds_read_b64_tr_b16 a[68:69], v203 offset:512\n\t"
    "v_mfma_f32_16x16x32_bf16 v[112:115], a[96:99], a[36:39], v[112:115]\n\t"
    "v_mfma_f32_16x16x32_bf16 v[144:147], a[96:99], a[40:43], v[144:147]\n\t"
    "ds_read_b64_tr_b16 a[70:71], v203 offset:4608\n\t"
    "v_mfma_f32_16x16x32_bf16 v[176:179], a[96:99], a[44:47], v[176:179]\n\t"
    "buffer_load_dwordx4 v197, s[40:43], s58 offen offset:1024 lds\n\t"
    "s_nop 3\n\t"
    "v_mfma_f32_16x16x32_bf16 v[20:23], a[100:103], a[24:27], v[20:23]\n\t"
    "ds_read_b64_tr_b16 a[72:73], v202 offset:768\n\t"
    "v_mfma_f32_16x16x32_bf16 v[52:55], a[100:103], a[28:31], v[52:55]\n\t"
    "ds_read_b64_tr_b16 a[74:75], v202 offset:4864\n\t"
    "v_mfma_f32_16x16x32_bf16 v[84:87], a[100:103], a[32:35], v[84:87]\n\t"
    "v_mfma_f32_16x16x32_bf16 v[116:119], a[100:103], a[36:39], v[116:119]\n\t"
    "ds_read_b64_tr_b16 a[76:77], v203 offset:768\n\t"
    "buffer_load_dwordx4 v198, s[40:43], s58 offen offset:2048 lds\n\t"
    "v_mfma_f32_16x16x32_bf16 v[148:151], a[100:103], a[40:43], v[148:151]\n\t"
    "v_mfma_f32_16x16x32_bf16 v[180:183], a[100:103], a[44:47], v[180:183]\n\t"
    "ds_read_b64_tr_b16 a[78:79], v203 offset:4864\n\t"
    "s_nop 3\n\t"
    "v_mfma_f32_16x16x32_bf16 v[24:27], a[104:107], a[24:27], v[24:27]\n\t"
    "buffer_load_dwordx4 v199, s[40:43], s58 offen offset:3072 lds\n\t"
    "v_mfma_f32_16x16x32_bf16 v[56:59], a[104:107], a[28:31], v[56:59]\n\t"
    "v_mfma_f32_16x16x32_bf16 v[88:91], a[104:107], a[32:35], v[88:91]\n\t"
    "v_mfma_f32_16x16x32_bf16 v[120:123], a[104:107], a[36:39], v[120:123]\n\t"
    "v_mfma_f32_16x16x32_bf16 v[152:155], a[104:107], a[40:43], v[152:155]\n\t"
    "s_add_u32 s58, s58, 262144\n\t"
    "v_mfma_f32_16x16x32_bf16 v[184:187], a[104:107], a[44:47], v[184:187]\n\t"
    "s_nop 3\n\t"
    "v_mfma_f32_16x16x32_bf16 v[28:31], a[108:111], a[24:27], v[28:31]\n\t"
    "v_mfma_f32_16x16x32_bf16 v[60:63], a[108:111], a[28:31], v[60:63]\n\t"
    "v_mfma_f32_16x16x32_bf16 v[92:95], a[108:111], a[32:35], v[92:95]\n\t"
    "s_add_u32 m0, s60, 0\n\t"
    "v_mfma_f32_16x16x32_bf16 v[124:127], a[108:111], a[36:39], v[124:127]\n\t"
    "v_mfma_f32_16x16x32_bf16 v[156:159], a[108:111], a[40:43], v[156:159]\n\t"
    "v_mfma_f32_16x16x32_bf16 v[188:191], a[108:111], a[44:47], v[188:191]\n\t"
    "s_nop 3\n\t"
    "s_waitcnt lgkmcnt(0)\n\t"
    "v_mfma_f32_16x16x32_bf16 v[0:3], a[48:51], a[0:3], v[0:3]\n\t"
    "ds_read_b128 a[24:27], v201 offset:24576\n\t"
    "buffer_load_dwordx4 v192, s[36:39], s56 offen offset:0 lds\n\t"
    "v_mfma_f32_16x16x32_bf16 v[32:35], a[48:51], a[4:7], v[32:35]\n\t"
    "ds_read_b64_tr_b16 a[80:81], v202 offset:16384\n\t"
    "v_mfma_f32_16x16x32_bf16 v[64:67], a[48:51], a[8:11], v[64:67]\n\t"
    "v_mfma_f32_16x16x32_bf16 v[96:99], a[48:51], a[12:15], v[96:99]\n\t"
    "ds_read_b64_tr_b16 a[82:83], v202 offset:20480\n\t"
    "buffer_load_dwordx4 v193, s[36:39], s56 offen offset:1024 lds\n\t"
    "v_mfma_f32_16x16x32_bf16 v[128:131], a[48:51], a[16:19], v[128:131]\n\t"
    "v_mfma_f32_16x16x32_bf16 v[160:163], a[48:51], a[20:23], v[160:163]\n\t"
    "ds_read_b128 a[28:31], v201 offset:26624\n\t"
    "s_nop 2\n\t"
    "v_mfma_f32_16x16x32_bf16 v[4:7], a[52:55], a[0:3], v[4:7]\n\t"
    "ds_read_b64_tr_b16 a[84:85], v203 offset:16384\n\t"
    "buffer_load_dwordx4 v194, s[36:39], s56 offen offset:2048 lds\n\t"
    "v_mfma_f32_16x16x32_bf16 v[36:39], a[52:55], a[4:7], v[36:39]\n\t"
    "v_mfma_f32_16x16x32_bf16 v[68:71], a[52:55], a[8:11], v[68:71]\n\t"
    "ds_read_b64_tr_b16 a[86:87], v203 offset:20480\n\t"
    "v_mfma_f32_16x16x32_bf16 v[100:103], a[52:55], a[12:15], v[100:103]\n\t"
    "v_mfma_f32_16x16x32_bf16 v[132:135], a[52:55], a[16:19], v[132:135]\n\t"
    "ds_read_b128 a[32:35], v201 offset:28672\n\t"
    "buffer_load_dwordx4 v195, s[36:39], s56 offen offset:3072 lds\n\t"
    "v_mfma_f32_16x16x32_bf16 v[164:167], a[52:55], a[20:23], v[164:167]\n\t"
    "ds_read_b64_tr_b16 a[88:89], v202 offset:16640\n\t"
    "s_nop 2\n\t"
    "v_mfma_f32_16x16x32_bf16 v[8:11], a[56:59], a[0:3], v[8:11]\n\t"
    "v_mfma_f32_16x16x32_bf16 v[40:43], a[56:59], a[4:7], v[40:43]\n\t"
    "ds_read_b64_tr_b16 a[90:91], v202 offset:20736\n\t"
    "s_addk_i32 m0, 4096\n\t"
    "v_mfma_f32_16x16x32_bf16 v[72:75], a[56:59], a[8:11], v[72:75]\n\t"
    "v_mfma_f32_16x16x32_bf16 v[104:107], a[56:59], a[12:15], v[104:107]\n\t"
    "ds_read_b128 a[36:39], v201 offset:30720\n\t"
    "v_mfma_f32_16x16x32_bf16 v[136:139], a[56:59], a[16:19], v[136:139]\n\t"
    "ds_read_b64_tr_b16 a[92:93], v203 offset:16640\n\t"
    "v_mfma_f32_16x16x32_bf16 v[168:171], a[56:59], a[20:23], v[168:171]\n\t"
    "buffer_load_dwordx4 v192, s[36:39], s57 offen offset:0 lds\n\t"
    "s_nop 2\n\t"
    "v_mfma_f32_16x16x32_bf16 v[12:15], a[60:63], a[0:3], v[12:15]\n\t"
    "ds_read_b64_tr_b16 a[94:95], v203 offset:20736\n\t"
    "v_mfma_f32_16x16x32_bf16 v[44:47], a[60:63], a[4:7], v[44:47]\n\t"
    "v_mfma_f32_16x16x32_bf16 v[76:79], a[60:63], a[8:11], v[76:79]\n\t"
    "ds_read_b128 a[40:43], v201 offset:32768\n\t"
    "buffer_load_dwordx4 v193, s[36:39], s57 offen offset:1024 lds\n\t"
    "v_mfma_f32_16x16x32_bf16 v[108:111], a[60:63], a[12:15], v[108:111]\n\t"
    "ds_read_b64_tr_b16 a[96:97], v202 offset:16896\n\t"
    "v_mfma_f32_16x16x32_bf16 v[140:143], a[60:63], a[16:19], v[140:143]\n\t"
    "v_mfma_f32_16x16x32_bf16 v[172:175], a[60:63], a[20:23], v[172:175]\n\t"
    "ds_read_b64_tr_b16 a[98:99], v202 offset:20992\n\t"
    "s_nop 2\n\t"
    "v_mfma_f32_16x16x32_bf16 v[16:19], a[64:67], a[0:3], v[16:19]\n\t"
    "s_addk_i32 s56, 128\n\t"
    "v_mfma_f32_16x16x32_bf16 v[48:51], a[64:67], a[4:7], v[48:51]\n\t"
    "ds_read_b128 a[44:47], v201 offset:34816\n\t"
    "v_mfma_f32_16x16x32_bf16 v[80:83], a[64:67], a[8:11], v[80:83]\n\t"
    "ds_read_b64_tr_b16 a[100:101], v203 offset:16896\n\t"
    "v_mfma_f32_16x16x32_bf16 v[112:115], a[64:67], a[12:15], v[112:115]\n\t"
    "s_addk_i32 s57, 128\n\t"
    "v_mfma_f32_16x16x32_bf16 v[144:147], a[64:67], a[16:19], v[144:147]\n\t"
    "ds_read_b64_tr_b16 a[102:103], v203 offset:20992\n\t"
    "v_mfma_f32_16x16x32_bf16 v[176:179], a[64:67], a[20:23], v[176:179]\n\t"
    "s_nop 2\n\t"
    "v_mfma_f32_16x16x32_bf16 v[20:23], a[68:71], a[0:3], v[20:23]\n\t"
    "ds_read_b64_tr_b16 a[104:105], v202 offset:17152\n\t"
    "s_bitset0_b32 s56, 11\n\t"
    "v_mfma_f32_16x16x32_bf16 v[52:55], a[68:71], a[4:7], v[52:55]\n\t"
    "ds_read_b64_tr_b16 a[106:107], v202 offset:21248\n\t"
    "v_mfma_f32_16x16x32_bf16 v[84:87], a[68:71], a[8:11], v[84:87]\n\t"
    "v_mfma_f32_16x16x32_bf16 v[116:119], a[68:71], a[12:15], v[116:119]\n\t"
    "ds_read_b64_tr_b16 a[108:109], v203 offset:17152\n\t"
    "v_mfma_f32_16x16x32_bf16 v[148:151], a[68:71], a[16:19], v[148:151]\n\t"
    "s_bitset0_b32 s57, 11\n\t"
    "v_mfma_f32_16x16x32_bf16 v[180:183], a[68:71], a[20:23], v[180:183]\n\t"
    "ds_read_b64_tr_b16 a[110:111], v203 offset:21248\n\t"
    "s_nop 2\n\t"
    "v_mfma_f32_16x16x32_bf16 v[24:27], a[72:75], a[0:3], v[24:27]\n\t"
    "v_mfma_f32_16x16x32_bf16 v[56:59], a[72:75], a[4:7], v[56:59]\n\t"
    "buffer_load_dwordx4 a[132:135], v209, s[48:51], s62 offen offset:1024 nt\n\t"
    "v_mfma_f32_16x16x32_bf16 v[88:91], a[72:75], a[8:11], v[88:91]\n\t"
    "v_mfma_f32_16x16x32_bf16 v[120:123], a[72:75], a[12:15], v[120:123]\n\t"
    "v_mfma_f32_16x16x32_bf16 v[152:155], a[72:75], a[16:19], v[152:155]\n\t"
    "v_mfma_f32_16x16x32_bf16 v[184:187], a[72:75], a[20:23], v[184:187]\n\t"
    "buffer_load_dwordx4 a[136:139], v221, s[48:51], s62 offen offset:2048 nt\n\t"
    "s_nop 2\n\t"
    "v_mfma_f32_16x16x32_bf16 v[28:31], a[76:79], a[0:3], v[28:31]\n\t"
    "v_mfma_f32_16x16x32_bf16 v[60:63], a[76:79], a[4:7], v[60:63]\n\t"
    "v_mfma_f32_16x16x32_bf16 v[92:95], a[76:79], a[8:11], v[92:95]\n\t"
    "buffer_load_dwordx4 a[140:143], v246, s[48:51], s62 offen offset:3072 nt\n\t"
    "s_add_u32 s62, s62, 8192\n\t"
    "v_mfma_f32_16x16x32_bf16 v[124:127], a[76:79], a[12:15], v[124:127]\n\t"
    "v_mfma_f32_16x16x32_bf16 v[156:159], a[76:79], a[16:19], v[156:159]\n\t"
    "v_mfma_f32_16x16x32_bf16 v[188:191], a[76:79], a[20:23], v[188:191]\n\t"
    "s_nop 2\n\t"
    "s_waitcnt vmcnt(13) lgkmcnt(0)\n\t"
    "s_barrier\n\t"
    "v_mfma_f32_16x16x32_bf16 v[0:3], a[80:83], a[24:27], v[0:3]\n\t"
    "ds_read_b128 a[0:3], v200 offset:49152\n\t"
    "s_add_u32 m0, s61, 0\n\t"
    "v_mfma_f32_16x16x32_bf16 v[32:35], a[80:83], a[28:31], v[32:35]\n\t"
    "ds_read_b64_tr_b16 a[48:49], v202 offset:32768\n\t"
    "v_mfma_f32_16x16x32_bf16 v[64:67], a[80:83], a[32:35], v[64:67]\n\t"
    "v_mfma_f32_16x16x32_bf16 v[96:99], a[80:83], a[36:39], v[96:99]\n\t"
    "ds_read_b64_tr_b16 a[50:51], v202 offset:36864\n\t"
    "buffer_load_dwordx4 v196, s[40:43], s58 offen offset:0 lds\n\t"
    "v_mfma_f32_16x16x32_bf16 v[128:131], a[80:83], a[40:43], v[128:131]\n\t"
    "v_mfma_f32_16x16x32_bf16 v[160:163], a[80:83], a[44:47], v[160:163]\n\t"
    "ds_read_b128 a[4:7], v200 offset:51200\n\t"
    "s_nop 2\n\t"
    "v_mfma_f32_16x16x32_bf16 v[4:7], a[84:87], a[24:27], v[4:7]\n\t"
    "ds_read_b64_tr_b16 a[52:53], v203 offset:32768\n\t"
    "v_mfma_f32_16x16x32_bf16 v[36:39], a[84:87], a[28:31], v[36:39]\n\t"
    "buffer_load_dwordx4 v197, s[40:43], s58 offen offset:1024 lds\n\t"
    "v_mfma_f32_16x16x32_bf16 v[68:71], a[84:87], a[32:35], v[68:71]\n\t"
    "ds_read_b64_tr_b16 a[54:55], v203 offset:36864\n\t"
    "v_mfma_f32_16x16x32_bf16 v[100:103], a[84:87], a[36:39], v[100:103]\n\t"
    "v_mfma_f32_16x16x32_bf16 v[132:135], a[84:87], a[40:43], v[132:135]\n\t"
    "ds_read_b128 a[8:11], v200 offset:53248\n\t"
    "v_mfma_f32_16x16x32_bf16 v[164:167], a[84:87], a[44:47], v[164:167]\n\t"
    "ds_read_b64_tr_b16 a[56:57], v202 offset:33024\n\t"
    "buffer_load_dwordx4 v198, s[40:43], s58 offen offset:2048 lds\n\t"
    "s_nop 2\n\t"
    "v_mfma_f32_16x16x32_bf16 v[8:11], a[88:91], a[24:27], v[8:11]\n\t"
    "v_mfma_f32_16x16x32_bf16 v[40:43], a[88:91], a[28:31], v[40:43]\n\t"
    "ds_read_b64_tr_b16 a[58:59], v202 offset:37120\n\t"
    "v_mfma_f32_16x16x32_bf16 v[72:75], a[88:91], a[32:35], v[72:75]\n\t"
    "buffer_load_dwordx4 v199, s[40:43], s58 offen offset:3072 lds\n\t"
    "v_mfma_f32_16x16x32_bf16 v[104:107], a[88:91], a[36:39], v[104:107]\n\t"
    "ds_read_b128 a[12:15], v200 offset:55296\n\t"
    "v_mfma_f32_16x16x32_bf16 v[136:139], a[88:91], a[40:43], v[136:139]\n\t"
    "ds_read_b64_tr_b16 a[60:61], v203 offset:33024\n\t"
    "v_mfma_f32_16x16x32_bf16 v[168:171], a[88:91], a[44:47], v[168:171]\n\t"
    "s_nop 2\n\t"
    "v_mfma_f32_16x16x32_bf16 v[12:15], a[92:95], a[24:27], v[12:15]\n\t"
    "ds_read_b64_tr_b16 a[62:63], v203 offset:37120\n\t"
    "s_add_u32 s58, s58, 262144\n\t"
    "v_mfma_f32_16x16x32_bf16 v[44:47], a[92:95], a[28:31], v[44:47]\n\t"
    "v_mfma_f32_16x16x32_bf16 v[76:79], a[92:95], a[32:35], v[76:79]\n\t"
    "ds_read_b128 a[16:19], v200 offset:57344\n\t"
    "v_mfma_f32_16x16x32_bf16 v[108:111], a[92:95], a[36:39], v[108:111]\n\t"
    "ds_read_b64_tr_b16 a[64:65], v202 offset:33280\n\t"
    "v_mfma_f32_16x16x32_bf16 v[140:143], a[92:95], a[40:43], v[140:143]\n\t"
    "s_and_b32 s58, s58, 8388607\n\t"
    "v_mfma_f32_16x16x32_bf16 v[172:175], a[92:95], a[44:47], v[172:175]\n\t"
    "ds_read_b64_tr_b16 a[66:67], v202 offset:37376\n\t"
    "s_nop 2\n\t"
    "v_mfma_f32_16x16x32_bf16 v[16:19], a[96:99], a[24:27], v[16:19]\n\t"
    "v_mfma_f32_16x16x32_bf16 v[48:51], a[96:99], a[28:31], v[48:51]\n\t"
    "ds_read_b128 a[20:23], v200 offset:59392\n\t"
    "s_add_u32 m0, s61, 16384\n\t"
    "v_mfma_f32_16x16x32_bf16 v[80:83], a[96:99], a[32:35], v[80:83]\n\t"
    "ds_read_b64_tr_b16 a[68:69], v203 offset:33280\n\t"
    "v_mfma_f32_16x16x32_bf16 v[112:115], a[96:99], a[36:39], v[112:115]\n\t"
    "v_mfma_f32_16x16x32_bf16 v[144:147], a[96:99], a[40:43], v[144:147]\n\t"
    "ds_read_b64_tr_b16 a[70:71], v203 offset:37376\n\t"
    "v_mfma_f32_16x16x32_bf16 v[176:179], a[96:99], a[44:47], v[176:179]\n\t"
    "buffer_load_dwordx4 v196, s[40:43], s58 offen offset:0 lds\n\t"
    "s_nop 2\n\t"
    "v_mfma_f32_16x16x32_bf16 v[20:23], a[100:103], a[24:27], v[20:23]\n\t"
    "ds_read_b64_tr_b16 a[72:73], v202 offset:33536\n\t"
    "v_mfma_f32_16x16x32_bf16 v[52:55], a[100:103], a[28:31], v[52:55]\n\t"
    "ds_read_b64_tr_b16 a[74:75], v202 offset:37632\n\t"
    "v_mfma_f32_16x16x32_bf16 v[84:87], a[100:103], a[32:35], v[84:87]\n\t"
    "v_mfma_f32_16x16x32_bf16 v[116:119], a[100:103], a[36:39], v[116:119]\n\t"
    "ds_read_b64_tr_b16 a[76:77], v203 offset:33536\n\t"
    "buffer_load_dwordx4 v197, s[40:43], s58 offen offset:1024 lds\n\t"
    "v_mfma_f32_16x16x32_bf16 v[148:151], a[100:103], a[40:43], v[148:151]\n\t"
    "v_mfma_f32_16x16x32_bf16 v[180:183], a[100:103], a[44:47], v[180:183]\n\t"
    "ds_read_b64_tr_b16 a[78:79], v203 offset:37632\n\t"
    "s_nop 2\n\t"
    "v_mfma_f32_16x16x32_bf16 v[24:27], a[104:107], a[24:27], v[24:27]\n\t"
    "buffer_load_dwordx4 v198, s[40:43], s58 offen offset:2048 lds\n\t"
    "v_mfma_f32_16x16x32_bf16 v[56:59], a[104:107], a[28:31], v[56:59]\n\t"
    "v_mfma_f32_16x16x32_bf16 v[88:91], a[104:107], a[32:35], v[88:91]\n\t"
    "v_mfma_f32_16x16x32_bf16 v[120:123], a[104:107], a[36:39], v[120:123]\n\t"
    "v_mfma_f32_16x16x32_bf16 v[152:155], a[104:107], a[40:43], v[152:155]\n\t"
    "buffer_load_dwordx4 v199, s[40:43], s58 offen offset:3072 lds\n\t"
    "v_mfma_f32_16x16x32_bf16 v[184:187], a[104:107], a[44:47], v[184:187]\n\t"
    "s_nop 2\n\t"
    "v_mfma_f32_16x16x32_bf16 v[28:31], a[108:111], a[24:27], v[28:31]\n\t"
    "v_mfma_f32_16x16x32_bf16 v[60:63], a[108:111], a[28:31], v[60:63]\n\t"
    "v_mfma_f32_16x16x32_bf16 v[92:95], a[108:111], a[32:35], v[92:95]\n\t"
    "s_add_u32 s58, s58, 262144\n\t"
    "v_mfma_f32_16x16x32_bf16 v[124:127], a[108:111], a[36:39], v[124:127]\n\t"
    "v_mfma_f32_16x16x32_bf16 v[156:159], a[108:111], a[40:43], v[156:159]\n\t"
    "v_mfma_f32_16x16x32_bf16 v[188:191], a[108:111], a[44:47], v[188:191]\n\t"
    "s_nop 2\n\t"
    "s_waitcnt lgkmcnt(0)\n\t"
    "v_mfma_f32_16x16x32_bf16 v[0:3], a[48:51], a[0:3], v[0:3]\n\t"
    "ds_read_b128 a[24:27], v201 offset:49152\n\t"
    "s_add_u32 m0, s60, 24576\n\t"
    "v_mfma_f32_16x16x32_bf16 v[32:35], a[48:51], a[4:7], v[32:35]\n\t"
    "ds_read_b64_tr_b16 a[80:81], v202 offset:49152\n\t"
    "v_mfma_f32_16x16x32_bf16 v[64:67], a[48:51], a[8:11], v[64:67]\n\t"
    "v_mfma_f32_16x16x32_bf16 v[96:99], a[48:51], a[12:15], v[96:99]\n\t"
    "ds_read_b64_tr_b16 a[82:83], v202 offset:53248\n\t"
    "buffer_load_dwordx4 v192, s[36:39], s56 offen offset:0 lds\n\t"
    "v_mfma_f32_16x16x32_bf16 v[128:131], a[48:51], a[16:19], v[128:131]\n\t"
    "v_mfma_f32_16x16x32_bf16 v[160:163], a[48:51], a[20:23], v[160:163]\n\t"
    "ds_read_b128 a[28:31], v201 offset:51200\n\t"
    "v_mfma_f32_16x16x32_bf16 v[4:7], a[52:55], a[0:3], v[4:7]\n\t"
    "ds_read_b64_tr_b16 a[84:85], v203 offset:49152\n\t"
    "v_mfma_f32_16x16x32_bf16 v[36:39], a[52:55], a[4:7], v[36:39]\n\t"
    "buffer_load_dwordx4 v193, s[36:39], s56 offen offset:1024 lds\n\t"
    "v_mfma_f32_16x16x32_bf16 v[68:71], a[52:55], a[8:11], v[68:71]\n\t"
    "ds_read_b64_tr_b16 a[86:87], v203 offset:53248\n\t"
    "v_mfma_f32_16x16x32_bf16 v[100:103], a[52:55], a[12:15], v[100:103]\n\t"
    "v_mfma_f32_16x16x32_bf16 v[132:135], a[52:55], a[16:19], v[132:135]\n\t"
    "ds_read_b128 a[32:35], v201 offset:53248\n\t"
    "v_mfma_f32_16x16x32_bf16 v[164:167], a[52:55], a[20:23], v[164:167]\n\t"
    "ds_read_b64_tr_b16 a[88:89], v202 offset:49408\n\t"
    "buffer_load_dwordx4 v194, s[36:39], s56 offen offset:2048 lds\n\t"
    "v_mfma_f32_16x16x32_bf16 v[8:11], a[56:59], a[0:3], v[8:11]\n\t"
    "v_mfma_f32_16x16x32_bf16 v[40:43], a[56:59], a[4:7], v[40:43]\n\t"
    "ds_read_b64_tr_b16 a[90:91], v202 offset:53504\n\t"
    "v_mfma_f32_16x16x32_bf16 v[72:75], a[56:59], a[8:11], v[72:75]\n\t"
    "buffer_load_dwordx4 v195, s[36:39], s56 offen offset:3072 lds\n\t"
    "v_mfma_f32_16x16x32_bf16 v[104:107], a[56:59], a[12:15], v[104:107]\n\t"
    "ds_read_b128 a[36:39], v201 offset:55296\n\t"
    "v_mfma_f32_16x16x32_bf16 v[136:139], a[56:59], a[16:19], v[136:139]\n\t"
    "ds_read_b64_tr_b16 a[92:93], v203 offset:49408\n\t"
    "v_mfma_f32_16x16x32_bf16 v[168:171], a[56:59], a[20:23], v[168:171]\n\t"
    "v_mfma_f32_16x16x32_bf16 v[12:15], a[60:63], a[0:3], v[12:15]\n\t"
    "ds_read_b64_tr_b16 a[94:95], v203 offset:53504\n\t"
    "s_addk_i32 m0, 4096\n\t"
    "v_mfma_f32_16x16x32_bf16 v[44:47], a[60:63], a[4:7], v[44:47]\n\t"
    "v_mfma_f32_16x16x32_bf16 v[76:79], a[60:63], a[8:11], v[76:79]\n\t"
    "ds_read_b128 a[40:43], v201 offset:57344\n\t"
    "v_mfma_f32_16x16x32_bf16 v[108:111], a[60:63], a[12:15], v[108:111]\n\t"
    "ds_read_b64_tr_b16 a[96:97], v202 offset:49664\n\t"
    "v_mfma_f32_16x16x32_bf16 v[140:143], a[60:63], a[16:19], v[140:143]\n\t"
    "buffer_load_dwordx4 v192, s[36:39], s57 offen offset:0 lds\n\t"
    "v_mfma_f32_16x16x32_bf16 v[172:175], a[60:63], a[20:23], v[172:175]\n\t"
    "ds_read_b64_tr_b16 a[98:99], v202 offset:53760\n\t"
    "v_mfma_f32_16x16x32_bf16 v[16:19], a[64:67], a[0:3], v[16:19]\n\t"
    "v_mfma_f32_16x16x32_bf16 v[48:51], a[64:67], a[4:7], v[48:51]\n\t"
    "ds_read_b128 a[44:47], v201 offset:59392\n\t"
    "buffer_load_dwordx4 v193, s[36:39], s57 offen offset:1024 lds\n\t"
    "v_mfma_f32_16x16x32_bf16 v[80:83], a[64:67], a[8:11], v[80:83]\n\t"
    "ds_read_b64_tr_b16 a[100:101], v203 offset:49664\n\t"
    "v_mfma_f32_16x16x32_bf16 v[112:115], a[64:67], a[12:15], v[112:115]\n\t"
    "v_mfma_f32_16x16x32_bf16 v[144:147], a[64:67], a[16:19], v[144:147]\n\t"
    "ds_read_b64_tr_b16 a[102:103], v203 offset:53760\n\t"
    "v_mfma_f32_16x16x32_bf16 v[176:179], a[64:67], a[20:23], v[176:179]\n\t"
    "s_addk_i32 s56, 128\n\t"
    "v_mfma_f32_16x16x32_bf16 v[20:23], a[68:71], a[0:3], v[20:23]\n\t"
    "ds_read_b64_tr_b16 a[104:105], v202 offset:49920\n\t"
    "v_mfma_f32_16x16x32_bf16 v[52:55], a[68:71], a[4:7], v[52:55]\n\t"
    "ds_read_b64_tr_b16 a[106:107], v202 offset:54016\n\t"
    "v_mfma_f32_16x16x32_bf16 v[84:87], a[68:71], a[8:11], v[84:87]\n\t"
    "v_mfma_f32_16x16x32_bf16 v[116:119], a[68:71], a[12:15], v[116:119]\n\t"
    "ds_read_b64_tr_b16 a[108:109], v203 offset:49920\n\t"
    "s_addk_i32 s57, 128\n\t"
    "v_mfma_f32_16x16x32_bf16 v[148:151], a[68:71], a[16:19], v[148:151]\n\t"
    "v_mfma_f32_16x16x32_bf16 v[180:183], a[68:71], a[20:23], v[180:183]\n\t"
    "ds_read_b64_tr_b16 a[110:111], v203 offset:54016\n\t"
    "v_mfma_f32_16x16x32_bf16 v[24:27], a[72:75], a[0:3], v[24:27]\n\t"
    "buffer_load_dwordx4 a[144:147], v204, s[44:47], s62 offen offset:0 nt\n\t"
    "v_mfma_f32_16x16x32_bf16 v[56:59], a[72:75], a[4:7], v[56:59]\n\t"
    "v_mfma_f32_16x16x32_bf16 v[88:91], a[72:75], a[8:11], v[88:91]\n\t"
    "v_mfma_f32_16x16x32_bf16 v[120:123], a[72:75], a[12:15], v[120:123]\n\t"
    "v_mfma_f32_16x16x32_bf16 v[152:155], a[72:75], a[16:19], v[152:155]\n\t"
    "buffer_load_dwordx4 a[148:151], v205, s[44:47], s62 offen offset:1024 nt\n\t"
    "v_mfma_f32_16x16x32_bf16 v[184:187], a[72:75], a[20:23], v[184:187]\n\t"
    "v_mfma_f32_16x16x32_bf16 v[28:31], a[76:79], a[0:3], v[28:31]\n\t"
    "v_mfma_f32_16x16x32_bf16 v[60:63], a[76:79], a[4:7], v[60:63]\n\t"
    "v_mfma_f32_16x16x32_bf16 v[92:95], a[76:79], a[8:11], v[92:95]\n\t"
    "s_getpc_b64 s[68:69]\n\t"
    ".LCT2_%=:\n\t"
    "v_min_u32 v251, .LEND_%=-.LCT2_%=-4, v223\n\t"
    "global_load_dword a255, v251, s[68:69]\n\t"
    "v_mfma_f32_16x16x32_bf16 v[124:127], a[76:79], a[12:15], v[124:127]\n\t"
    "v_mfma_f32_16x16x32_bf16 v[156:159], a[76:79], a[16:19], v[156:159]\n\t"
    "v_mfma_f32_16x16x32_bf16 v[188:191], a[76:79], a[20:23], v[188:191]\n\t"
    "s_waitcnt vmcnt(13) lgkmcnt(0)\n\t"
    "s_barrier\n\t"
    "v_mfma_f32_16x16x32_bf16 v[0:3], a[80:83], a[24:27], v[0:3]\n\t"
    "ds_read_b128 a[0:3], v200 offset:0\n\t"
    "s_add_u32 m0, s61, 32768\n\t"
    "v_mfma_f32_16x16x32_bf16 v[32:35], a[80:83], a[28:31], v[32:35]\n\t"
    "ds_read_b64_tr_b16 a[48:49], v248 offset:0\n\t"
    "v_mfma_f32_16x16x32_bf16 v[64:67], a[80:83], a[32:35], v[64:67]\n\t"
    "v_mfma_f32_16x16x32_bf16 v[96:99], a[80:83], a[36:39], v[96:99]\n\t"
    "ds_read_b64_tr_b16 a[50:51], v248 offset:4096\n\t"
    "v_mfma_f32_16x16x32_bf16 v[128:131], a[80:83], a[40:43], v[128:131]\n\t"
    "buffer_load_dwordx4 v196, s[40:43], s58 offen offset:0 lds\n\t"
    "v_mfma_f32_16x16x32_bf16 v[160:163], a[80:83], a[44:47], v[160:163]\n\t"
    "ds_read_b128 a[4:7], v200 offset:2048\n\t"
    "v_mfma_f32_16x16x32_bf16 v[4:7], a[84:87], a[24:27], v[4:7]\n\t"
    "ds_read_b64_tr_b16 a[52:53], v249 offset:0\n\t"
    "v_mfma_f32_16x16x32_bf16 v[36:39], a[84:87], a[28:31], v[36:39]\n\t"
    "v_mfma_f32_16x16x32_bf16 v[68:71], a[84:87], a[32:35], v[68:71]\n\t"
    "ds_read_b64_tr_b16 a[54:55], v249 offset:4096\n\t"
    "buffer_load_dwordx4 v197, s[40:43], s58 offen offset:1024 lds\n\t"
    "v_mfma_f32_16x16x32_bf16 v[100:103], a[84:87], a[36:39], v[100:103]\n\t"
    "v_mfma_f32_16x16x32_bf16 v[132:135], a[84:87], a[40:43], v[132:135]\n\t"
    "ds_read_b128 a[8:11], v200 offset:4096\n\t"
    "v_mfma_f32_16x16x32_bf16 v[164:167], a[84:87], a[44:47], v[164:167]\n\t"
    "ds_read_b64_tr_b16 a[56:57], v248 offset:256\n\t"
    "v_mfma_f32_16x16x32_bf16 v[8:11], a[88:91], a[24:27], v[8:11]\n\t"
    "buffer_load_dwordx4 v198, s[40:43], s58 offen offset:2048 lds\n\t"
    "v_mfma_f32_16x16x32_bf16 v[40:43], a[88:91], a[28:31], v[40:43]\n\t"
    "ds_read_b64_tr_b16 a[58:59], v248 offset:4352\n\t"
    "v_mfma_f32_16x16x32_bf16 v[72:75], a[88:91], a[32:35], v[72:75]\n\t"
    "v_mfma_f32_16x16x32_bf16 v[104:107], a[88:91], a[36:39], v[104:107]\n\t"
    "ds_read_b128 a[12:15], v200 offset:6144\n\t"
    "v_mfma_f32_16x16x32_bf16 v[136:139], a[88:91], a[40:43], v[136:139]\n\t"
    "ds_read_b64_tr_b16 a[60:61], v249 offset:256\n\t"
    "buffer_load_dwordx4 v199, s[40:43], s58 offen offset:3072 lds\n\t"
    "v_mfma_f32_16x16x32_bf16 v[168:171], a[88:91], a[44:47], v[168:171]\n\t"
    "v_mfma_f32_16x16x32_bf16 v[12:15], a[92:95], a[24:27], v[12:15]\n\t"
    "ds_read_b64_tr_b16 a[62:63], v249 offset:4352\n\t"
    "v_mfma_f32_16x16x32_bf16 v[44:47], a[92:95], a[28:31], v[44:47]\n\t"
    "v_mfma_f32_16x16x32_bf16 v[76:79], a[92:95], a[32:35], v[76:79]\n\t"
    "ds_read_b128 a[16:19], v200 offset:8192\n\t"
    "s_add_u32 s58, s58, 262144\n\t"
    "v_mfma_f32_16x16x32_bf16 v[108:111], a[92:95], a[36:39], v[108:111]\n\t"
    "ds_read_b64_tr_b16 a[64:65], v248 offset:512\n\t"
    "v_mfma_f32_16x16x32_bf16 v[140:143], a[92:95], a[40:43], v[140:143]\n\t"
    "v_mfma_f32_16x16x32_bf16 v[172:175], a[92:95], a[44:47], v[172:175]\n\t"
    "ds_read_b64_tr_b16 a[66:67], v248 offset:4608\n\t"
    "v_mfma_f32_16x16x32_bf16 v[16:19], a[96:99], a[24:27], v[16:19]\n\t"
    "s_add_u32 m0, s61, 49152\n\t"
    "v_mfma_f32_16x16x32_bf16 v[48:51], a[96:99], a[28:31], v[48:51]\n\t"
    "ds_read_b128 a[20:23], v200 offset:10240\n\t"
    "v_mfma_f32_16x16x32_bf16 v[80:83], a[96:99], a[32:35], v[80:83]\n\t"
    "ds_read_b64_tr_b16 a[68:69], v249 offset:512\n\t"
    "v_mfma_f32_16x16x32_bf16 v[112:115], a[96:99], a[36:39], v[112:115]\n\t"
    "v_mfma_f32_16x16x32_bf16 v[144:147], a[96:99], a[40:43], v[144:147]\n\t"
    "ds_read_b64_tr_b16 a[70:71], v249 offset:4608\n\t"
    "buffer_load_dwordx4 v196, s[40:43], s58 offen offset:0 lds\n\t"
    "v_mfma_f32_16x16x32_bf16 v[176:179], a[96:99], a[44:47], v[176:179]\n\t"
    "v_mfma_f32_16x16x32_bf16 v[20:23], a[100:103], a[24:27], v[20:23]\n\t"
    "ds_read_b64_tr_b16 a[72:73], v248 offset:768\n\t"
    "v_mfma_f32_16x16x32_bf16 v[52:55], a[100:103], a[28:31], v[52:55]\n\t"
    "ds_read_b64_tr_b16 a[74:75], v248 offset:4864\n\t"
    "v_mfma_f32_16x16x32_bf16 v[84:87], a[100:103], a[32:35], v[84:87]\n\t"
    "buffer_load_dwordx4 v197, s[40:43], s58 offen offset:1024 lds\n\t"
    "v_mfma_f32_16x16x32_bf16 v[116:119], a[100:103], a[36:39], v[116:119]\n\t"
    "ds_read_b64_tr_b16 a[76:77], v249 offset:768\n\t"
    "v_mfma_f32_16x16x32_bf16 v[148:151], a[100:103], a[40:43], v[148:151]\n\t"
    "v_mfma_f32_16x16x32_bf16 v[180:183], a[100:103], a[44:47], v[180:183]\n\t"
    "ds_read_b64_tr_b16 a[78:79], v249 offset:4864\n\t"
    "v_mfma_f32_16x16x32_bf16 v[24:27], a[104:107], a[24:27], v[24:27]\n\t"
    "buffer_load_dwordx4 v198, s[40:43], s58 offen offset:2048 lds\n\t"
    "v_mfma_f32_16x16x32_bf16 v[56:59], a[104:107], a[28:31], v[56:59]\n\t"
    "v_mfma_f32_16x16x32_bf16 v[88:91], a[104:107], a[32:35], v[88:91]\n\t"
    "v_mfma_f32_16x16x32_bf16 v[120:123], a[104:107], a[36:39], v[120:123]\n\t"
    "v_mfma_f32_16x16x32_bf16 v[152:155], a[104:107], a[40:43], v[152:155]\n\t"
    "buffer_load_dwordx4 v199, s[40:43], s58 offen offset:3072 lds\n\t"
    "v_mfma_f32_16x16x32_bf16 v[184:187], a[104:107], a[44:47], v[184:187]\n\t"
    "v_mfma_f32_16x16x32_bf16 v[28:31], a[108:111], a[24:27], v[28:31]\n\t"
    "v_mfma_f32_16x16x32_bf16 v[60:63], a[108:111], a[28:31], v[60:63]\n\t"
    "v_mfma_f32_16x16x32_bf16 v[92:95], a[108:111], a[32:35], v[92:95]\n\t"
    "s_add_u32 s58, s58, 262144\n\t"
    "v_mfma_f32_16x16x32_bf16 v[124:127], a[108:111], a[36:39], v[124:127]\n\t"
    "v_mfma_f32_16x16x32_bf16 v[156:159], a[108:111], a[40:43], v[156:159]\n\t"
    "v_mfma_f32_16x16x32_bf16 v[188:191], a[108:111], a[44:47], v[188:191]\n\t"
    "s_waitcnt lgkmcnt(0)\n\t"
    "v_mfma_f32_16x16x32_bf16 v[0:3], a[48:51], a[0:3], v[0:3]\n\t"
    "ds_read_b128 a[24:27], v201 offset:0\n\t"
    "s_add_u32 m0, s60, 49152\n\t"
    "v_mfma_f32_16x16x32_bf16 v[32:35], a[48:51], a[4:7], v[32:35]\n\t"
    "ds_read_b64_tr_b16 a[80:81], v202 offset:0\n\t"
    "v_mfma_f32_16x16x32_bf16 v[64:67], a[48:51], a[8:11], v[64:67]\n\t"
    "v_mfma_f32_16x16x32_bf16 v[96:99], a[48:51], a[12:15], v[96:99]\n\t"
    "ds_read_b64_tr_b16 a[82:83], v202 offset:4096\n\t"
    "buffer_load_dwordx4 v192, s[36:39], s56 offen offset:0 lds\n\t"
    "v_mfma_f32_16x16x32_bf16 v[128:131], a[48:51], a[16:19], v[128:131]\n\t"
    "v_mfma_f32_16x16x32_bf16 v[160:163], a[48:51], a[20:23], v[160:163]\n\t"
    "ds_read_b128 a[28:31], v201 offset:2048\n\t"
    "v_mfma_f32_16x16x32_bf16 v[4:7], a[52:55], a[0:3], v[4:7]\n\t"
    "ds_read_b64_tr_b16 a[84:85], v203 offset:0\n\t"
    "v_mfma_f32_16x16x32_bf16 v[36:39], a[52:55], a[4:7], v[36:39]\n\t"
    "buffer_load_dwordx4 v193, s[36:39], s56 offen offset:1024 lds\n\t"
    "v_mfma_f32_16x16x32_bf16 v[68:71], a[52:55], a[8:11], v[68:71]\n\t"
    "ds_read_b64_tr_b16 a[86:87], v203 offset:4096\n\t"
    "v_mfma_f32_16x16x32_bf16 v[100:103], a[52:55], a[12:15], v[100:103]\n\t"
    "v_mfma_f32_16x16x32_bf16 v[132:135], a[52:55], a[16:19], v[132:135]\n\t"
    "ds_read_b128 a[32:35], v201 offset:4096\n\t"
    "v_mfma_f32_16x16x32_bf16 v[164:167], a[52:55], a[20:23], v[164:167]\n\t"
    "ds_read_b64_tr_b16 a[88:89], v202 offset:256\n\t"
    "buffer_load_dwordx4 v194, s[36:39], s56 offen offset:2048 lds\n\t"
    "v_mfma_f32_16x16x32_bf16 v[8:11], a[56:59], a[0:3], v[8:11]\n\t"
    "v_mfma_f32_16x16x32_bf16 v[40:43], a[56:59], a[4:7], v[40:43]\n\t"
    "ds_read_b64_tr_b16 a[90:91], v202 offset:4352\n\t"
    "v_mfma_f32_16x16x32_bf16 v[72:75], a[56:59], a[8:11], v[72:75]\n\t"
    "buffer_load_dwordx4 v195, s[36:39], s56 offen offset:3072 lds\n\t"
    "v_mfma_f32_16x16x32_bf16 v[104:107], a[56:59], a[12:15], v[104:107]\n\t"
    "ds_read_b128 a[36:39], v201 offset:6144\n\t"
    "v_mfma_f32_16x16x32_bf16 v[136:139], a[56:59], a[16:19], v[136:139]\n\t"
    "ds_read_b64_tr_b16 a[92:93], v203 offset:256\n\t"
    "v_mfma_f32_16x16x32_bf16 v[168:171], a[56:59], a[20:23], v[168:171]\n\t"
    "v_mfma_f32_16x16x32_bf16 v[12:15], a[60:63], a[0:3], v[12:15]\n\t"
    "ds_read_b64_tr_b16 a[94:95], v203 offset:4352\n\t"
    "s_addk_i32 m0, 4096\n\t"
    "v_mfma_f32_16x16x32_bf16 v[44:47], a[60:63], a[4:7], v[44:47]\n\t"
    "v_mfma_f32_16x16x32_bf16 v[76:79], a[60:63], a[8:11], v[76:79]\n\t"
    "ds_read_b128 a[40:43], v201 offset:8192\n\t"
    "v_mfma_f32_16x16x32_bf16 v[108:111], a[60:63], a[12:15], v[108:111]\n\t"
    "ds_read_b64_tr_b16 a[96:97], v202 offset:512\n\t"
    "v_mfma_f32_16x16x32_bf16 v[140:143], a[60:63], a[16:19], v[140:143]\n\t"
    "buffer_load_dwordx4 v192, s[36:39], s57 offen offset:0 lds\n\t"
    "v_mfma_f32_16x16x32_bf16 v[172:175], a[60:63], a[20:23], v[172:175]\n\t"
    "ds_read_b64_tr_b16 a[98:99], v202 offset:4608\n\t"
    "v_mfma_f32_16x16x32_bf16 v[16:19], a[64:67], a[0:3], v[16:19]\n\t"
    "v_mfma_f32_16x16x32_bf16 v[48:51], a[64:67], a[4:7], v[48:51]\n\t"
    "ds_read_b128 a[44:47], v201 offset:10240\n\t"
    "buffer_load_dwordx4 v193, s[36:39], s57 offen offset:1024 lds\n\t"
    "v_mfma_f32_16x16x32_bf16 v[80:83], a[64:67], a[8:11], v[80:83]\n\t"
    "ds_read_b64_tr_b16 a[100:101], v203 offset:512\n\t"
    "v_mfma_f32_16x16x32_bf16 v[112:115], a[64:67], a[12:15], v[112:115]\n\t"
    "v_mfma_f32_16x16x32_bf16 v[144:147], a[64:67], a[16:19], v[144:147]\n\t"
    "ds_read_b64_tr_b16 a[102:103], v203 offset:4608\n\t"
    "v_mfma_f32_16x16x32_bf16 v[176:179], a[64:67], a[20:23], v[176:179]\n\t"
    "s_addk_i32 s56, 128\n\t"
    "v_mfma_f32_16x16x32_bf16 v[20:23], a[68:71], a[0:3], v[20:23]\n\t"
    "ds_read_b64_tr_b16 a[104:105], v202 offset:768\n\t"
    "v_mfma_f32_16x16x32_bf16 v[52:55], a[68:71], a[4:7], v[52:55]\n\t"
    "ds_read_b64_tr_b16 a[106:107], v202 offset:4864\n\t"
    "v_mfma_f32_16x16x32_bf16 v[84:87], a[68:71], a[8:11], v[84:87]\n\t"
    "v_mfma_f32_16x16x32_bf16 v[116:119], a[68:71], a[12:15], v[116:119]\n\t"
    "ds_read_b64_tr_b16 a[108:109], v203 offset:768\n\t"
    "s_addk_i32 s57, 128\n\t"
    "v_mfma_f32_16x16x32_bf16 v[148:151], a[68:71], a[16:19], v[148:151]\n\t"
    "v_mfma_f32_16x16x32_bf16 v[180:183], a[68:71], a[20:23], v[180:183]\n\t"
    "ds_read_b64_tr_b16 a[110:111], v203 offset:4864\n\t"
    "v_mfma_f32_16x16x32_bf16 v[24:27], a[72:75], a[0:3], v[24:27]\n\t"
    "buffer_load_dwordx4 a[152:155], v206, s[44:47], s62 offen offset:2048 nt\n\t"
    "v_mfma_f32_16x16x32_bf16 v[56:59], a[72:75], a[4:7], v[56:59]\n\t"
    "v_mfma_f32_16x16x32_bf16 v[88:91], a[72:75], a[8:11], v[88:91]\n\t"
    "v_mfma_f32_16x16x32_bf16 v[120:123], a[72:75], a[12:15], v[120:123]\n\t"
    "v_mfma_f32_16x16x32_bf16 v[152:155], a[72:75], a[16:19], v[152:155]\n\t"
    "buffer_load_dwordx4 a[156:159], v207, s[44:47], s62 offen offset:3072 nt\n\t"
    "v_mfma_f32_16x16x32_bf16 v[184:187], a[72:75], a[20:23], v[184:187]\n\t"
    "v_mfma_f32_16x16x32_bf16 v[28:31], a[76:79], a[0:3], v[28:31]\n\t"
    "v_mfma_f32_16x16x32_bf16 v[60:63], a[76:79], a[4:7], v[60:63]\n\t"
    "v_mfma_f32_16x16x32_bf16 v[92:95], a[76:79], a[8:11], v[92:95]\n\t"
    "buffer_load_dwordx4 a[160:163], v208, s[48:51], s62 offen offset:0 nt\n\t"
    "v_mfma_f32_16x16x32_bf16 v[124:127], a[76:79], a[12:15], v[124:127]\n\t"
    "v_mfma_f32_16x16x32_bf16 v[156:159], a[76:79], a[16:19], v[156:159]\n\t"
    "v_mfma_f32_16x16x32_bf16 v[188:191], a[76:79], a[20:23], v[188:191]\n\t"
    "s_waitcnt vmcnt(13) lgkmcnt(0)\n\t"
    "s_barrier\n\t"
    "v_mfma_f32_16x16x32_bf16 v[0:3], a[80:83], a[24:27], v[0:3]\n\t"
    "ds_read_b128 a[0:3], v200 offset:24576\n\t"
    "s_add_u32 m0, s61, 65536\n\t"
    "v_mfma_f32_16x16x32_bf16 v[32:35], a[80:83], a[28:31], v[32:35]\n\t"
    "ds_read_b64_tr_b16 a[48:49], v202 offset:16384\n\t"
    "v_mfma_f32_16x16x32_bf16 v[64:67], a[80:83], a[32:35], v[64:67]\n\t"
    "v_mfma_f32_16x16x32_bf16 v[96:99], a[80:83], a[36:39], v[96:99]\n\t"
    "ds_read_b64_tr_b16 a[50:51], v202 offset:20480\n\t"
    "v_mfma_f32_16x16x32_bf16 v[128:131], a[80:83], a[40:43], v[128:131]\n\t"
    "buffer_load_dwordx4 v196, s[40:43], s58 offen offset:0 lds\n\t"
    "v_mfma_f32_16x16x32_bf16 v[160:163], a[80:83], a[44:47], v[160:163]\n\t"
    "ds_read_b128 a[4:7], v200 offset:26624\n\t"
    "v_mfma_f32_16x16x32_bf16 v[4:7], a[84:87], a[24:27], v[4:7]\n\t"
    "ds_read_b64_tr_b16 a[52:53], v203 offset:16384\n\t"
    "v_mfma_f32_16x16x32_bf16 v[36:39], a[84:87], a[28:31], v[36:39]\n\t"
    "v_mfma_f32_16x16x32_bf16 v[68:71], a[84:87], a[32:35], v[68:71]\n\t"
    "ds_read_b64_tr_b16 a[54:55], v203 offset:20480\n\t"
    "buffer_load_dwordx4 v197, s[40:43], s58 offen offset:1024 lds\n\t"
    "v_mfma_f32_16x16x32_bf16 v[100:103], a[84:87], a[36:39], v[100:103]\n\t"
    "v_mfma_f32_16x16x32_bf16 v[132:135], a[84:87], a[40:43], v[132:135]\n\t"
    "ds_read_b128 a[8:11], v200 offset:28672\n\t"
    "v_mfma_f32_16x16x32_bf16 v[164:167], a[84:87], a[44:47], v[164:167]\n\t"
    "ds_read_b64_tr_b16 a[56:57], v202 offset:16640\n\t"
    "v_mfma_f32_16x16x32_bf16 v[8:11], a[88:91], a[24:27], v[8:11]\n\t"
    "buffer_load_dwordx4 v198, s[40:43], s58 offen offset:2048 lds\n\t"
    "v_mfma_f32_16x16x32_bf16 v[40:43], a[88:91], a[28:31], v[40:43]\n\t"
    "ds_read_b64_tr_b16 a[58:59], v202 offset:20736\n\t"
    "v_mfma_f32_16x16x32_bf16 v[72:75], a[88:91], a[32:35], v[72:75]\n\t"
    "v_mfma_f32_16x16x32_bf16 v[104:107], a[88:91], a[36:39], v[104:107]\n\t"
    "ds_read_b128 a[12:15], v200 offset:30720\n\t"
    "v_mfma_f32_16x16x32_bf16 v[136:139], a[88:91], a[40:43], v[136:139]\n\t"
    "ds_read_b64_tr_b16 a[60:61], v203 offset:16640\n\t"
    "buffer_load_dwordx4 v199, s[40:43], s58 offen offset:3072 lds\n\t"
    "v_mfma_f32_16x16x32_bf16 v[168:171], a[88:91], a[44:47], v[168:171]\n\t"
    "v_mfma_f32_16x16x32_bf16 v[12:15], a[92:95], a[24:27], v[12:15]\n\t"
    "ds_read_b64_tr_b16 a[62:63], v203 offset:20736\n\t"
    "v_mfma_f32_16x16x32_bf16 v[44:47], a[92:95], a[28:31], v[44:47]\n\t"
    "v_mfma_f32_16x16x32_bf16 v[76:79], a[92:95], a[32:35], v[76:79]\n\t"
    "ds_read_b128 a[16:19], v200 offset:32768\n\t"
    "s_add_u32 s58, s58, 262144\n\t"
    "v_mfma_f32_16x16x32_bf16 v[108:111], a[92:95], a[36:39], v[108:111]\n\t"
    "ds_read_b64_tr_b16 a[64:65], v202 offset:16896\n\t"
    "v_mfma_f32_16x16x32_bf16 v[140:143], a[92:95], a[40:43], v[140:143]\n\t"
    "v_mfma_f32_16x16x32_bf16 v[172:175], a[92:95], a[44:47], v[172:175]\n\t"
    "ds_read_b64_tr_b16 a[66:67], v202 offset:20992\n\t"
    "v_mfma_f32_16x16x32_bf16 v[16:19], a[96:99], a[24:27], v[16:19]\n\t"
    "s_add_u32 m0, s61, 0\n\t"
    "v_mfma_f32_16x16x32_bf16 v[48:51], a[96:99], a[28:31], v[48:51]\n\t"
    "ds_read_b128 a[20:23], v200 offset:34816\n\t"
    "v_mfma_f32_16x16x32_bf16 v[80:83], a[96:99], a[32:35], v[80:83]\n\t"
    "ds_read_b64_tr_b16 a[68:69], v203 offset:16896\n\t"
    "v_mfma_f32_16x16x32_bf16 v[112:115], a[96:99], a[36:39], v[112:115]\n\t"
    "v_mfma_f32_16x16x32_bf16 v[144:147], a[96:99], a[40:43], v[144:147]\n\t"
    "ds_read_b64_tr_b16 a[70:71], v203 offset:20992\n\t"
    "buffer_load_dwordx4 v196, s[40:43], s58 offen offset:0 lds\n\t"
    "v_mfma_f32_16x16x32_bf16 v[176:179], a[96:99], a[44:47], v[176:179]\n\t"
    "v_mfma_f32_16x16x32_bf16 v[20:23], a[100:103], a[24:27], v[20:23]\n\t"
    "ds_read_b64_tr_b16 a[72:73], v202 offset:17152\n\t"
    "v_mfma_f32_16x16x32_bf16 v[52:55], a[100:103], a[28:31], v[52:55]\n\t"
    "ds_read_b64_tr_b16 a[74:75], v202 offset:21248\n\t"
    "v_mfma_f32_16x16x32_bf16 v[84:87], a[100:103], a[32:35], v[84:87]\n\t"
    "buffer_load_dwordx4 v197, s[40:43], s58 offen offset:1024 lds\n\t"
    "v_mfma_f32_16x16x32_bf16 v[116:119], a[100:103], a[36:39], v[116:119]\n\t"
    "ds_read_b64_tr_b16 a[76:77], v203 offset:17152\n\t"
    "v_mfma_f32_16x16x32_bf16 v[148:151], a[100:103], a[40:43], v[148:151]\n\t"
    "v_mfma_f32_16x16x32_bf16 v[180:183], a[100:103], a[44:47], v[180:183]\n\t"
    "ds_read_b64_tr_b16 a[78:79], v203 offset:21248\n\t"
    "v_mfma_f32_16x16x32_bf16 v[24:27], a[104:107], a[24:27], v[24:27]\n\t"
    "buffer_load_dwordx4 v198, s[40:43], s58 offen offset:2048 lds\n\t"
    "v_mfma_f32_16x16x32_bf16 v[56:59], a[104:107], a[28:31], v[56:59]\n\t"
    "v_mfma_f32_16x16x32_bf16 v[88:91], a[104:107], a[32:35], v[88:91]\n\t"
    "v_mfma_f32_16x16x32_bf16 v[120:123], a[104:107], a[36:39], v[120:123]\n\t"
    "v_mfma_f32_16x16x32_bf16 v[152:155], a[104:107], a[40:43], v[152:155]\n\t"
    "buffer_load_dwordx4 v199, s[40:43], s58 offen offset:3072 lds\n\t"
    "v_mfma_f32_16x16x32_bf16 v[184:187], a[104:107], a[44:47], v[184:187]\n\t"
    "v_mfma_f32_16x16x32_bf16 v[28:31], a[108:111], a[24:27], v[28:31]\n\t"
    "v_mfma_f32_16x16x32_bf16 v[60:63], a[108:111], a[28:31], v[60:63]\n\t"
    "v_mfma_f32_16x16x32_bf16 v[92:95], a[108:111], a[32:35], v[92:95]\n\t"
    "s_add_u32 s58, s58, 262144\n\t"
    "v_mfma_f32_16x16x32_bf16 v[124:127], a[108:111], a[36:39], v[124:127]\n\t"
    "v_mfma_f32_16x16x32_bf16 v[156:159], a[108:111], a[40:43], v[156:159]\n\t"
    "v_mfma_f32_16x16x32_bf16 v[188:191], a[108:111], a[44:47], v[188:191]\n\t"
    "s_waitcnt lgkmcnt(0)\n\t"
    "v_mfma_f32_16x16x32_bf16 v[0:3], a[48:51], a[0:3], v[0:3]\n\t"
    "ds_read_b128 a[24:27], v201 offset:24576\n\t"
    "s_add_u32 m0, s60, 0\n\t"
    "v_mfma_f32_16x16x32_bf16 v[32:35], a[48:51], a[4:7], v[32:35]\n\t"
    "ds_read_b64_tr_b16 a[80:81], v202 offset:32768\n\t"
    "v_mfma_f32_16x16x32_bf16 v[64:67], a[48:51], a[8:11], v[64:67]\n\t"
    "v_mfma_f32_16x16x32_bf16 v[96:99], a[48:51], a[12:15], v[96:99]\n\t"
    "ds_read_b64_tr_b16 a[82:83], v202 offset:36864\n\t"
    "buffer_load_dwordx4 v192, s[36:39], s56 offen offset:0 lds\n\t"
    "v_mfma_f32_16x16x32_bf16 v[128:131], a[48:51], a[16:19], v[128:131]\n\t"
    "v_mfma_f32_16x16x32_bf16 v[160:163], a[48:51], a[20:23], v[160:163]\n\t"
    "ds_read_b128 a[28:31], v201 offset:26624\n\t"
    "v_mfma_f32_16x16x32_bf16 v[4:7], a[52:55], a[0:3], v[4:7]\n\t"
    "ds_read_b64_tr_b16 a[84:85], v203 offset:32768\n\t"
    "v_mfma_f32_16x16x32_bf16 v[36:39], a[52:55], a[4:7], v[36:39]\n\t"
    "buffer_load_dwordx4 v193, s[36:39], s56 offen offset:1024 lds\n\t"
    "v_mfma_f32_16x16x32_bf16 v[68:71], a[52:55], a[8:11], v[68:71]\n\t"
    "ds_read_b64_tr_b16 a[86:87], v203 offset:36864\n\t"
    "v_mfma_f32_16x16x32_bf16 v[100:103], a[52:55], a[12:15], v[100:103]\n\t"
    "v_mfma_f32_16x16x32_bf16 v[132:135], a[52:55], a[16:19], v[132:135]\n\t"
    "ds_read_b128 a[32:35], v201 offset:28672\n\t"
    "v_mfma_f32_16x16x32_bf16 v[164:167], a[52:55], a[20:23], v[164:167]\n\t"
    "ds_read_b64_tr_b16 a[88:89], v202 offset:33024\n\t"
    "buffer_load_dwordx4 v194, s[36:39], s56 offen offset:2048 lds\n\t"
    "v_mfma_f32_16x16x32_bf16 v[8:11], a[56:59], a[0:3], v[8:11]\n\t"
    "v_mfma_f32_16x16x32_bf16 v[40:43], a[56:59], a[4:7], v[40:43]\n\t"
    "ds_read_b64_tr_b16 a[90:91], v202 offset:37120\n\t"
    "v_mfma_f32_16x16x32_bf16 v[72:75], a[56:59], a[8:11], v[72:75]\n\t"
    "buffer_load_dwordx4 v195, s[36:39], s56 offen offset:3072 lds\n\t"
    "v_mfma_f32_16x16x32_bf16 v[104:107], a[56:59], a[12:15], v[104:107]\n\t"
    "ds_read_b128 a[36:39], v201 offset:30720\n\t"
    "v_mfma_f32_16x16x32_bf16 v[136:139], a[56:59], a[16:19], v[136:139]\n\t"
    "ds_read_b64_tr_b16 a[92:93], v203 offset:33024\n\t"
    "v_mfma_f32_16x16x32_bf16 v[168:171], a[56:59], a[20:23], v[168:171]\n\t"
    "v_mfma_f32_16x16x32_bf16 v[12:15], a[60:63], a[0:3], v[12:15]\n\t"
    "ds_read_b64_tr_b16 a[94:95], v203 offset:37120\n\t"
    "s_addk_i32 m0, 4096\n\t"
    "v_mfma_f32_16x16x32_bf16 v[44:47], a[60:63], a[4:7], v[44:47]\n\t"
    "v_mfma_f32_16x16x32_bf16 v[76:79], a[60:63], a[8:11], v[76:79]\n\t"
    "ds_read_b128 a[40:43], v201 offset:32768\n\t"
    "v_mfma_f32_16x16x32_bf16 v[108:111], a[60:63], a[12:15], v[108:111]\n\t"
    "ds_read_b64_tr_b16 a[96:97], v202 offset:33280\n\t"
    "v_mfma_f32_16x16x32_bf16 v[140:143], a[60:63], a[16:19], v[140:143]\n\t"
    "buffer_load_dwordx4 v192, s[36:39], s57 offen offset:0 lds\n\t"
    "v_mfma_f32_16x16x32_bf16 v[172:175], a[60:63], a[20:23], v[172:175]\n\t"
    "ds_read_b64_tr_b16 a[98:99], v202 offset:37376\n\t"
    "v_mfma_f32_16x16x32_bf16 v[16:19], a[64:67], a[0:3], v[16:19]\n\t"
    "v_mfma_f32_16x16x32_bf16 v[48:51], a[64:67], a[4:7], v[48:51]\n\t"
    "ds_read_b128 a[44:47], v201 offset:34816\n\t"
    "buffer_load_dwordx4 v193, s[36:39], s57 offen offset:1024 lds\n\t"
    "v_mfma_f32_16x16x32_bf16 v[80:83], a[64:67], a[8:11], v[80:83]\n\t"
    "ds_read_b64_tr_b16 a[100:101], v203 offset:33280\n\t"
    "v_mfma_f32_16x16x32_bf16 v[112:115], a[64:67], a[12:15], v[112:115]\n\t"
    "v_mfma_f32_16x16x32_bf16 v[144:147], a[64:67], a[16:19], v[144:147]\n\t"
    "ds_read_b64_tr_b16 a[102:103], v203 offset:37376\n\t"
    "v_mfma_f32_16x16x32_bf16 v[176:179], a[64:67], a[20:23], v[176:179]\n\t"
    "s_addk_i32 s56, 128\n\t"
    "v_mfma_f32_16x16x32_bf16 v[20:23], a[68:71], a[0:3], v[20:23]\n\t"
    "ds_read_b64_tr_b16 a[104:105], v202 offset:33536\n\t"
    "v_mfma_f32_16x16x32_bf16 v[52:55], a[68:71], a[4:7], v[52:55]\n\t"
    "ds_read_b64_tr_b16 a[106:107], v202 offset:37632\n\t"
    "v_mfma_f32_16x16x32_bf16 v[84:87], a[68:71], a[8:11], v[84:87]\n\t"
    "v_mfma_f32_16x16x32_bf16 v[116:119], a[68:71], a[12:15], v[116:119]\n\t"
    "ds_read_b64_tr_b16 a[108:109], v203 offset:33536\n\t"
    "s_addk_i32 s57, 128\n\t"
    "v_mfma_f32_16x16x32_bf16 v[148:151], a[68:71], a[16:19], v[148:151]\n\t"
    "v_mfma_f32_16x16x32_bf16 v[180:183], a[68:71], a[20:23], v[180:183]\n\t"
    "ds_read_b64_tr_b16 a[110:111], v203 offset:37632\n\t"
    "v_mfma_f32_16x16x32_bf16 v[24:27], a[72:75], a[0:3], v[24:27]\n\t"
    "buffer_load_dwordx4 a[164:167], v209, s[48:51], s62 offen offset:1024 nt\n\t"
    "v_mfma_f32_16x16x32_bf16 v[56:59], a[72:75], a[4:7], v[56:59]\n\t"
    "v_mfma_f32_16x16x32_bf16 v[88:91], a[72:75], a[8:11], v[88:91]\n\t"
    "v_mfma_f32_16x16x32_bf16 v[120:123], a[72:75], a[12:15], v[120:123]\n\t"
    "v_mfma_f32_16x16x32_bf16 v[152:155], a[72:75], a[16:19], v[152:155]\n\t"
    "buffer_load_dwordx4 a[168:171], v221, s[48:51], s62 offen offset:2048 nt\n\t"
    "v_mfma_f32_16x16x32_bf16 v[184:187], a[72:75], a[20:23], v[184:187]\n\t"
    "v_mfma_f32_16x16x32_bf16 v[28:31], a[76:79], a[0:3], v[28:31]\n\t"
    "v_mfma_f32_16x16x32_bf16 v[60:63], a[76:79], a[4:7], v[60:63]\n\t"
    "v_mfma_f32_16x16x32_bf16 v[92:95], a[76:79], a[8:11], v[92:95]\n\t"
    "buffer_load_dwordx4 a[172:175], v246, s[48:51], s62 offen offset:3072 nt\n\t"
    "s_add_u32 s62, s62, 8192\n\t"
    "v_mfma_f32_16x16x32_bf16 v[124:127], a[76:79], a[12:15], v[124:127]\n\t"
    "v_mfma_f32_16x16x32_bf16 v[156:159], a[76:79], a[16:19], v[156:159]\n\t"
    "v_mfma_f32_16x16x32_bf16 v[188:191], a[76:79], a[20:23], v[188:191]\n\t"
    "s_waitcnt vmcnt(13) lgkmcnt(0)\n\t"
    "s_barrier\n\t"
    "v_mfma_f32_16x16x32_bf16 v[0:3], a[80:83], a[24:27], v[0:3]\n\t"
    "ds_read_b128 a[0:3], v200 offset:49152\n\t"
    "s_add_u32 m0, s61, 16384\n\t"
    "v_mfma_f32_16x16x32_bf16 v[32:35], a[80:83], a[28:31], v[32:35]\n\t"
    "ds_read_b64_tr_b16 a[48:49], v202 offset:49152\n\t"
    "v_mfma_f32_16x16x32_bf16 v[64:67], a[80:83], a[32:35], v[64:67]\n\t"
    "v_mfma_f32_16x16x32_bf16 v[96:99], a[80:83], a[36:39], v[96:99]\n\t"
    "ds_read_b64_tr_b16 a[50:51], v202 offset:53248\n\t"
    "buffer_load_dwordx4 v196, s[40:43], s58 offen offset:0 lds\n\t"
    "v_mfma_f32_16x16x32_bf16 v[128:131], a[80:83], a[40:43], v[128:131]\n\t"
    "v_mfma_f32_16x16x32_bf16 v[160:163], a[80:83], a[44:47], v[160:163]\n\t"
    "ds_read_b128 a[4:7], v200 offset:51200\n\t"
    "v_mfma_f32_16x16x32_bf16 v[4:7], a[84:87], a[24:27], v[4:7]\n\t"
    "ds_read_b64_tr_b16 a[52:53], v203 offset:49152\n\t"
    "v_mfma_f32_16x16x32_bf16 v[36:39], a[84:87], a[28:31], v[36:39]\n\t"
    "buffer_load_dwordx4 v197, s[40:43], s58 offen offset:1024 lds\n\t"
    "v_mfma_f32_16x16x32_bf16 v[68:71], a[84:87], a[32:35], v[68:71]\n\t"
    "ds_read_b64_tr_b16 a[54:55], v203 offset:53248\n\t"
    "v_mfma_f32_16x16x32_bf16 v[100:103], a[84:87], a[36:39], v[100:103]\n\t"
    "v_mfma_f32_16x16x32_bf16 v[132:135], a[84:87], a[40:43], v[132:135]\n\t"
    "ds_read_b128 a[8:11], v200 offset:53248\n\t"
    "v_mfma_f32_16x16x32_bf16 v[164:167], a[84:87], a[44:47], v[164:167]\n\t"
    "ds_read_b64_tr_b16 a[56:57], v202 offset:49408\n\t"
    "buffer_load_dwordx4 v198, s[40:43], s58 offen offset:2048 lds\n\t"
    "v_mfma_f32_16x16x32_bf16 v[8:11], a[88:91], a[24:27], v[8:11]\n\t"
    "v_mfma_f32_16x16x32_bf16 v[40:43], a[88:91], a[28:31], v[40:43]\n\t"
    "ds_read_b64_tr_b16 a[58:59], v202 offset:53504\n\t"
    "v_mfma_f32_16x16x32_bf16 v[72:75], a[88:91], a[32:35], v[72:75]\n\t"
    "buffer_load_dwordx4 v199, s[40:43], s58 offen offset:3072 lds\n\t"
    "v_mfma_f32_16x16x32_bf16 v[104:107], a[88:91], a[36:39], v[104:107]\n\t"
    "ds_read_b128 a[12:15], v200 offset:55296\n\t"
    "v_mfma_f32_16x16x32_bf16 v[136:139], a[88:91], a[40:43], v[136:139]\n\t"
    "ds_read_b64_tr_b16 a[60:61], v203 offset:49408\n\t"
    "v_mfma_f32_16x16x32_bf16 v[168:171], a[88:91], a[44:47], v[168:171]\n\t"
    "v_mfma_f32_16x16x32_bf16 v[12:15], a[92:95], a[24:27], v[12:15]\n\t"
    "ds_read_b64_tr_b16 a[62:63], v203 offset:53504\n\t"
    "s_add_u32 s58, s58, 262144\n\t"
    "v_mfma_f32_16x16x32_bf16 v[44:47], a[92:95], a[28:31], v[44:47]\n\t"
    "v_mfma_f32_16x16x32_bf16 v[76:79], a[92:95], a[32:35], v[76:79]\n\t"
    "ds_read_b128 a[16:19], v200 offset:57344\n\t"
    "v_mfma_f32_16x16x32_bf16 v[108:111], a[92:95], a[36:39], v[108:111]\n\t"
    "ds_read_b64_tr_b16 a[64:65], v202 offset:49664\n\t"
    "v_mfma_f32_16x16x32_bf16 v[140:143], a[92:95], a[40:43], v[140:143]\n\t"
    "s_add_u32 m0, s61, 32768\n\t"
    "v_mfma_f32_16x16x32_bf16 v[172:175], a[92:95], a[44:47], v[172:175]\n\t"
    "ds_read_b64_tr_b16 a[66:67], v202 offset:53760\n\t"
    "v_mfma_f32_16x16x32_bf16 v[16:19], a[96:99], a[24:27], v[16:19]\n\t"
    "v_mfma_f32_16x16x32_bf16 v[48:51], a[96:99], a[28:31], v[48:51]\n\t"
    "ds_read_b128 a[20:23], v200 offset:59392\n\t"
    "buffer_load_dwordx4 v196, s[40:43], s58 offen offset:0 lds\n\t"
    "v_mfma_f32_16x16x32_bf16 v[80:83], a[96:99], a[32:35], v[80:83]\n\t"
    "ds_read_b64_tr_b16 a[68:69], v203 offset:49664\n\t"
    "v_mfma_f32_16x16x32_bf16 v[112:115], a[96:99], a[36:39], v[112:115]\n\t"
    "v_mfma_f32_16x16x32_bf16 v[144:147], a[96:99], a[40:43], v[144:147]\n\t"
    "ds_read_b64_tr_b16 a[70:71], v203 offset:53760\n\t"
    "v_mfma_f32_16x16x32_bf16 v[176:179], a[96:99], a[44:47], v[176:179]\n\t"
    "buffer_load_dwordx4 v197, s[40:43], s58 offen offset:1024 lds\n\t"
    "v_mfma_f32_16x16x32_bf16 v[20:23], a[100:103], a[24:27], v[20:23]\n\t"
    "ds_read_b64_tr_b16 a[72:73], v202 offset:49920\n\t"
    "v_mfma_f32_16x16x32_bf16 v[52:55], a[100:103], a[28:31], v[52:55]\n\t"
    "ds_read_b64_tr_b16 a[74:75], v202 offset:54016\n\t"
    "v_mfma_f32_16x16x32_bf16 v[84:87], a[100:103], a[32:35], v[84:87]\n\t"
    "v_mfma_f32_16x16x32_bf16 v[116:119], a[100:103], a[36:39], v[116:119]\n\t"
    "ds_read_b64_tr_b16 a[76:77], v203 offset:49920\n\t"
    "buffer_load_dwordx4 v198, s[40:43], s58 offen offset:2048 lds\n\t"
    "v_mfma_f32_16x16x32_bf16 v[148:151], a[100:103], a[40:43], v[148:151]\n\t"
    "v_mfma_f32_16x16x32_bf16 v[180:183], a[100:103], a[44:47], v[180:183]\n\t"
    "ds_read_b64_tr_b16 a[78:79], v203 offset:54016\n\t"
    "v_mfma_f32_16x16x32_bf16 v[24:27], a[104:107], a[24:27], v[24:27]\n\t"
    "buffer_load_dwordx4 v199, s[40:43], s58 offen offset:3072 lds\n\t"
    "v_mfma_f32_16x16x32_bf16 v[56:59], a[104:107], a[28:31], v[56:59]\n\t"
    "v_mfma_f32_16x16x32_bf16 v[88:91], a[104:107], a[32:35], v[88:91]\n\t"
    "v_mfma_f32_16x16x32_bf16 v[120:123], a[104:107], a[36:39], v[120:123]\n\t"
    "v_mfma_f32_16x16x32_bf16 v[152:155], a[104:107], a[40:43], v[152:155]\n\t"
    "s_add_u32 s58, s58, 262144\n\t"
    "v_mfma_f32_16x16x32_bf16 v[184:187], a[104:107], a[44:47], v[184:187]\n\t"
    "v_mfma_f32_16x16x32_bf16 v[28:31], a[108:111], a[24:27], v[28:31]\n\t"
    "v_mfma_f32_16x16x32_bf16 v[60:63], a[108:111], a[28:31], v[60:63]\n\t"
    "v_mfma_f32_16x16x32_bf16 v[92:95], a[108:111], a[32:35], v[92:95]\n\t"
    "s_add_u32 m0, s60, 24576\n\t"
    "v_mfma_f32_16x16x32_bf16 v[124:127], a[108:111], a[36:39], v[124:127]\n\t"
    "v_mfma_f32_16x16x32_bf16 v[156:159], a[108:111], a[40:43], v[156:159]\n\t"
    "v_mfma_f32_16x16x32_bf16 v[188:191], a[108:111], a[44:47], v[188:191]\n\t"
    "s_waitcnt lgkmcnt(0)\n\t"
    "v_mfma_f32_16x16x32_bf16 v[0:3], a[48:51], a[0:3], v[0:3]\n\t"
    "ds_read_b128 a[24:27], v201 offset:49152\n\t"
    "buffer_load_dwordx4 v192, s[36:39], s56 offen offset:0 lds\n\t"
    "v_mfma_f32_16x16x32_bf16 v[32:35], a[48:51], a[4:7], v[32:35]\n\t"
    "ds_read_b64_tr_b16 a[80:81], v248 offset:0\n\t"
    "v_mfma_f32_16x16x32_bf16 v[64:67], a[48:51], a[8:11], v[64:67]\n\t"
    "v_mfma_f32_16x16x32_bf16 v[96:99], a[48:51], a[12:15], v[96:99]\n\t"
    "ds_read_b64_tr_b16 a[82:83], v248 offset:4096\n\t"
    "buffer_load_dwordx4 v193, s[36:39], s56 offen offset:1024 lds\n\t"
    "v_mfma_f32_16x16x32_bf16 v[128:131], a[48:51], a[16:19], v[128:131]\n\t"
    "v_mfma_f32_16x16x32_bf16 v[160:163], a[48:51], a[20:23], v[160:163]\n\t"
    "ds_read_b128 a[28:31], v201 offset:51200\n\t"
    "v_mfma_f32_16x16x32_bf16 v[4:7], a[52:55], a[0:3], v[4:7]\n\t"
    "ds_read_b64_tr_b16 a[84:85], v249 offset:0\n\t"
    "v_mfma_f32_16x16x32_bf16 v[36:39], a[52:55], a[4:7], v[36:39]\n\t"
    "buffer_load_dwordx4 v194, s[36:39], s56 offen offset:2048 lds\n\t"
    "v_mfma_f32_16x16x32_bf16 v[68:71], a[52:55], a[8:11], v[68:71]\n\t"
    "ds_read_b64_tr_b16 a[86:87], v249 offset:4096\n\t"
    "v_mfma_f32_16x16x32_bf16 v[100:103], a[52:55], a[12:15], v[100:103]\n\t"
    "v_mfma_f32_16x16x32_bf16 v[132:135], a[52:55], a[16:19], v[132:135]\n\t"
    "ds_read_b128 a[32:35], v201 offset:53248\n\t"
    "v_mfma_f32_16x16x32_bf16 v[164:167], a[52:55], a[20:23], v[164:167]\n\t"
    "ds_read_b64_tr_b16 a[88:89], v248 offset:256\n\t"
    "buffer_load_dwordx4 v195, s[36:39], s56 offen offset:3072 lds\n\t"
    "v_mfma_f32_16x16x32_bf16 v[8:11], a[56:59], a[0:3], v[8:11]\n\t"
    "v_mfma_f32_16x16x32_bf16 v[40:43], a[56:59], a[4:7], v[40:43]\n\t"
    "ds_read_b64_tr_b16 a[90:91], v248 offset:4352\n\t"
    "v_mfma_f32_16x16x32_bf16 v[72:75], a[56:59], a[8:11], v[72:75]\n\t"
    "s_addk_i32 m0, 4096\n\t"
    "v_mfma_f32_16x16x32_bf16 v[104:107], a[56:59], a[12:15], v[104:107]\n\t"
    "ds_read_b128 a[36:39], v201 offset:55296\n\t"
    "v_mfma_f32_16x16x32_bf16 v[136:139], a[56:59], a[16:19], v[136:139]\n\t"
    "ds_read_b64_tr_b16 a[92:93], v249 offset:256\n\t"
    "v_mfma_f32_16x16x32_bf16 v[168:171], a[56:59], a[20:23], v[168:171]\n\t"
    "v_mfma_f32_16x16x32_bf16 v[12:15], a[60:63], a[0:3], v[12:15]\n\t"
    "ds_read_b64_tr_b16 a[94:95], v249 offset:4352\n\t"
    "buffer_load_dwordx4 v192, s[36:39], s57 offen offset:0 lds\n\t"
    "v_mfma_f32_16x16x32_bf16 v[44:47], a[60:63], a[4:7], v[44:47]\n\t"
    "v_mfma_f32_16x16x32_bf16 v[76:79], a[60:63], a[8:11], v[76:79]\n\t"
    "ds_read_b128 a[40:43], v201 offset:57344\n\t"
    "v_mfma_f32_16x16x32_bf16 v[108:111], a[60:63], a[12:15], v[108:111]\n\t"
    "ds_read_b64_tr_b16 a[96:97], v248 offset:512\n\t"
    "v_mfma_f32_16x16x32_bf16 v[140:143], a[60:63], a[16:19], v[140:143]\n\t"
    "buffer_load_dwordx4 v193, s[36:39], s57 offen offset:1024 lds\n\t"
    "v_mfma_f32_16x16x32_bf16 v[172:175], a[60:63], a[20:23], v[172:175]\n\t"
    "ds_read_b64_tr_b16 a[98:99], v248 offset:4608\n\t"
    "v_mfma_f32_16x16x32_bf16 v[16:19], a[64:67], a[0:3], v[16:19]\n\t"
    "v_mfma_f32_16x16x32_bf16 v[48:51], a[64:67], a[4:7], v[48:51]\n\t"
    "ds_read_b128 a[44:47], v201 offset:59392\n\t"
    "s_addk_i32 s56, 128\n\t"
    "v_mfma_f32_16x16x32_bf16 v[80:83], a[64:67], a[8:11], v[80:83]\n\t"
    "ds_read_b64_tr_b16 a[100:101], v249 offset:512\n\t"
    "v_mfma_f32_16x16x32_bf16 v[112:115], a[64:67], a[12:15], v[112:115]\n\t"
    "v_mfma_f32_16x16x32_bf16 v[144:147], a[64:67], a[16:19], v[144:147]\n\t"
    "ds_read_b64_tr_b16 a[102:103], v249 offset:4608\n\t"
    "v_mfma_f32_16x16x32_bf16 v[176:179], a[64:67], a[20:23], v[176:179]\n\t"
    "s_addk_i32 s57, 128\n\t"
    "v_mfma_f32_16x16x32_bf16 v[20:23], a[68:71], a[0:3], v[20:23]\n\t"
    "ds_read_b64_tr_b16 a[104:105], v248 offset:768\n\t"
    "v_mfma_f32_16x16x32_bf16 v[52:55], a[68:71], a[4:7], v[52:55]\n\t"
    "ds_read_b64_tr_b16 a[106:107], v248 offset:4864\n\t"
    "v_mfma_f32_16x16x32_bf16 v[84:87], a[68:71], a[8:11], v[84:87]\n\t"
    "v_mfma_f32_16x16x32_bf16 v[116:119], a[68:71], a[12:15], v[116:119]\n\t"
    "ds_read_b64_tr_b16 a[108:109], v249 offset:768\n\t"
    "s_bitset0_b32 s56, 11\n\t"
    "v_mfma_f32_16x16x32_bf16 v[148:151], a[68:71], a[16:19], v[148:151]\n\t"
    "v_mfma_f32_16x16x32_bf16 v[180:183], a[68:71], a[20:23], v[180:183]\n\t"
    "ds_read_b64_tr_b16 a[110:111], v249 offset:4864\n\t"
    "v_mfma_f32_16x16x32_bf16 v[24:27], a[72:75], a[0:3], v[24:27]\n\t"
    "s_bitset0_b32 s57, 11\n\t"
    "v_mfma_f32_16x16x32_bf16 v[56:59], a[72:75], a[4:7], v[56:59]\n\t"
    "v_mfma_f32_16x16x32_bf16 v[88:91], a[72:75], a[8:11], v[88:91]\n\t"
    "v_mfma_f32_16x16x32_bf16 v[120:123], a[72:75], a[12:15], v[120:123]\n\t"
    "v_mfma_f32_16x16x32_bf16 v[152:155], a[72:75], a[16:19], v[152:155]\n\t"
    "buffer_load_dwordx4 a[176:179], v204, s[44:47], s62 offen offset:0 nt\n\t"
    "v_mfma_f32_16x16x32_bf16 v[184:187], a[72:75], a[20:23], v[184:187]\n\t"
    "v_mfma_f32_16x16x32_bf16 v[28:31], a[76:79], a[0:3], v[28:31]\n\t"
    "v_mfma_f32_16x16x32_bf16 v[60:63], a[76:79], a[4:7], v[60:63]\n\t"
    "v_mfma_f32_16x16x32_bf16 v[92:95], a[76:79], a[8:11], v[92:95]\n\t"
    "buffer_load_dwordx4 a[180:183], v205, s[44:47], s62 offen offset:1024 nt\n\t"
    "v_mfma_f32_16x16x32_bf16 v[124:127], a[76:79], a[12:15], v[124:127]\n\t"
    "v_mfma_f32_16x16x32_bf16 v[156:159], a[76:79], a[16:19], v[156:159]\n\t"
    "v_mfma_f32_16x16x32_bf16 v[188:191], a[76:79], a[20:23], v[188:191]\n\t"
    "s_waitcnt vmcnt(12) lgkmcnt(0)\n\t"
    "s_barrier\n\t"
    "v_mfma_f32_16x16x32_bf16 v[0:3], a[80:83], a[24:27], v[0:3]\n\t"
    "ds_read_b128 a[0:3], v200 offset:0\n\t"
    "s_add_u32 m0, s61, 49152\n\t"
    "v_mfma_f32_16x16x32_bf16 v[32:35], a[80:83], a[28:31], v[32:35]\n\t"
    "ds_read_b64_tr_b16 a[48:49], v202 offset:0\n\t"
    "v_mfma_f32_16x16x32_bf16 v[64:67], a[80:83], a[32:35], v[64:67]\n\t"
    "v_mfma_f32_16x16x32_bf16 v[96:99], a[80:83], a[36:39], v[96:99]\n\t"
    "ds_read_b64_tr_b16 a[50:51], v202 offset:4096\n\t"
    "buffer_load_dwordx4 v196, s[40:43], s58 offen offset:0 lds\n\t"
    "v_mfma_f32_16x16x32_bf16 v[128:131], a[80:83], a[40:43], v[128:131]\n\t"
    "v_mfma_f32_16x16x32_bf16 v[160:163], a[80:83], a[44:47], v[160:163]\n\t"
    "ds_read_b128 a[4:7], v200 offset:2048\n\t"
    "v_mfma_f32_16x16x32_bf16 v[4:7], a[84:87], a[24:27], v[4:7]\n\t"
    "ds_read_b64_tr_b16 a[52:53], v203 offset:0\n\t"
    "v_mfma_f32_16x16x32_bf16 v[36:39], a[84:87], a[28:31], v[36:39]\n\t"
    "buffer_load_dwordx4 v197, s[40:43], s58 offen offset:1024 lds\n\t"
    "v_mfma_f32_16x16x32_bf16 v[68:71], a[84:87], a[32:35], v[68:71]\n\t"
    "ds_read_b64_tr_b16 a[54:55], v203 offset:4096\n\t"
    "v_mfma_f32_16x16x32_bf16 v[100:103], a[84:87], a[36:39], v[100:103]\n\t"
    "v_mfma_f32_16x16x32_bf16 v[132:135], a[84:87], a[40:43], v[132:135]\n\t"
    "ds_read_b128 a[8:11], v200 offset:4096\n\t"
    "v_mfma_f32_16x16x32_bf16 v[164:167], a[84:87], a[44:47], v[164:167]\n\t"
    "ds_read_b64_tr_b16 a[56:57], v202 offset:256\n\t"
    "buffer_load_dwordx4 v198, s[40:43], s58 offen offset:2048 lds\n\t"
    "v_mfma_f32_16x16x32_bf16 v[8:11], a[88:91], a[24:27], v[8:11]\n\t"
    "v_mfma_f32_16x16x32_bf16 v[40:43], a[88:91], a[28:31], v[40:43]\n\t"
    "ds_read_b64_tr_b16 a[58:59], v202 offset:4352\n\t"
    "v_mfma_f32_16x16x32_bf16 v[72:75], a[88:91], a[32:35], v[72:75]\n\t"
    "buffer_load_dwordx4 v199, s[40:43], s58 offen offset:3072 lds\n\t"
    "v_mfma_f32_16x16x32_bf16 v[104:107], a[88:91], a[36:39], v[104:107]\n\t"
    "ds_read_b128 a[12:15], v200 offset:6144\n\t"
    "v_mfma_f32_16x16x32_bf16 v[136:139], a[88:91], a[40:43], v[136:139]\n\t"
    "ds_read_b64_tr_b16 a[60:61], v203 offset:256\n\t"
    "v_mfma_f32_16x16x32_bf16 v[168:171], a[88:91], a[44:47], v[168:171]\n\t"
    "v_mfma_f32_16x16x32_bf16 v[12:15], a[92:95], a[24:27], v[12:15]\n\t"
    "ds_read_b64_tr_b16 a[62:63], v203 offset:4352\n\t"
    "s_add_u32 s58, s58, 262144\n\t"
    "v_mfma_f32_16x16x32_bf16 v[44:47], a[92:95], a[28:31], v[44:47]\n\t"
    "v_mfma_f32_16x16x32_bf16 v[76:79], a[92:95], a[32:35], v[76:79]\n\t"
    "ds_read_b128 a[16:19], v200 offset:8192\n\t"
    "v_mfma_f32_16x16x32_bf16 v[108:111], a[92:95], a[36:39], v[108:111]\n\t"
    "ds_read_b64_tr_b16 a[64:65], v202 offset:512\n\t"
    "v_mfma_f32_16x16x32_bf16 v[140:143], a[92:95], a[40:43], v[140:143]\n\t"
    "s_and_b32 s58, s58, 8388607\n\t"
    "v_mfma_f32_16x16x32_bf16 v[172:175], a[92:95], a[44:47], v[172:175]\n\t"
    "ds_read_b64_tr_b16 a[66:67], v202 offset:4608\n\t"
    "v_mfma_f32_16x16x32_bf16 v[16:19], a[96:99], a[24:27], v[16:19]\n\t"
    "v_mfma_f32_16x16x32_bf16 v[48:51], a[96:99], a[28:31], v[48:51]\n\t"
    "ds_read_b128 a[20:23], v200 offset:10240\n\t"
    "s_add_u32 m0, s61, 65536\n\t"
    "v_mfma_f32_16x16x32_bf16 v[80:83], a[96:99], a[32:35], v[80:83]\n\t"
    "ds_read_b64_tr_b16 a[68:69], v203 offset:512\n\t"
    "v_mfma_f32_16x16x32_bf16 v[112:115], a[96:99], a[36:39], v[112:115]\n\t"
    "v_mfma_f32_16x16x32_bf16 v[144:147], a[96:99], a[40:43], v[144:147]\n\t"
    "ds_read_b64_tr_b16 a[70:71], v203 offset:4608\n\t"
    "v_mfma_f32_16x16x32_bf16 v[176:179], a[96:99], a[44:47], v[176:179]\n\t"
    "buffer_load_dwordx4 v196, s[40:43], s58 offen offset:0 lds\n\t"
    "v_mfma_f32_16x16x32_bf16 v[20:23], a[100:103], a[24:27], v[20:23]\n\t"
    "ds_read_b64_tr_b16 a[72:73], v202 offset:768\n\t"
    "v_mfma_f32_16x16x32_bf16 v[52:55], a[100:103], a[28:31], v[52:55]\n\t"
    "ds_read_b64_tr_b16 a[74:75], v202 offset:4864\n\t"
    "v_mfma_f32_16x16x32_bf16 v[84:87], a[100:103], a[32:35], v[84:87]\n\t"
    "v_mfma_f32_16x16x32_bf16 v[116:119], a[100:103], a[36:39], v[116:119]\n\t"
    "ds_read_b64_tr_b16 a[76:77], v203 offset:768\n\t"
    "buffer_load_dwordx4 v197, s[40:43], s58 offen offset:1024 lds\n\t"
    "v_mfma_f32_16x16x32_bf16 v[148:151], a[100:103], a[40:43], v[148:151]\n\t"
    "v_mfma_f32_16x16x32_bf16 v[180:183], a[100:103], a[44:47], v[180:183]\n\t"
    "ds_read_b64_tr_b16 a[78:79], v203 offset:4864\n\t"
    "v_mfma_f32_16x16x32_bf16 v[24:27], a[104:107], a[24:27], v[24:27]\n\t"
    "buffer_load_dwordx4 v198, s[40:43], s58 offen offset:2048 lds\n\t"
    "v_mfma_f32_16x16x32_bf16 v[56:59], a[104:107], a[28:31], v[56:59]\n\t"
    "v_mfma_f32_16x16x32_bf16 v[88:91], a[104:107], a[32:35], v[88:91]\n\t"
    "v_mfma_f32_16x16x32_bf16 v[120:123], a[104:107], a[36:39], v[120:123]\n\t"
    "v_mfma_f32_16x16x32_bf16 v[152:155], a[104:107], a[40:43], v[152:155]\n\t"
    "buffer_load_dwordx4 v199, s[40:43], s58 offen offset:3072 lds\n\t"
    "v_mfma_f32_16x16x32_bf16 v[184:187], a[104:107], a[44:47], v[184:187]\n\t"
    "v_mfma_f32_16x16x32_bf16 v[28:31], a[108:111], a[24:27], v[28:31]\n\t"
    "v_mfma_f32_16x16x32_bf16 v[60:63], a[108:111], a[28:31], v[60:63]\n\t"
    "v_mfma_f32_16x16x32_bf16 v[92:95], a[108:111], a[32:35], v[92:95]\n\t"
    "s_add_u32 s58, s58, 262144\n\t"
    "v_mfma_f32_16x16x32_bf16 v[124:127], a[108:111], a[36:39], v[124:127]\n\t"
    "v_mfma_f32_16x16x32_bf16 v[156:159], a[108:111], a[40:43], v[156:159]\n\t"
    "v_mfma_f32_16x16x32_bf16 v[188:191], a[108:111], a[44:47], v[188:191]\n\t"
    "s_waitcnt lgkmcnt(0)\n\t"
    "v_mfma_f32_16x16x32_bf16 v[0:3], a[48:51], a[0:3], v[0:3]\n\t"
    "ds_read_b128 a[24:27], v201 offset:0\n\t"
    "s_add_u32 m0, s60, 49152\n\t"
    "v_mfma_f32_16x16x32_bf16 v[32:35], a[48:51], a[4:7], v[32:35]\n\t"
    "ds_read_b64_tr_b16 a[80:81], v202 offset:16384\n\t"
    "v_mfma_f32_16x16x32_bf16 v[64:67], a[48:51], a[8:11], v[64:67]\n\t"
    "v_mfma_f32_16x16x32_bf16 v[96:99], a[48:51], a[12:15], v[96:99]\n\t"
    "ds_read_b64_tr_b16 a[82:83], v202 offset:20480\n\t"
    "buffer_load_dwordx4 v192, s[36:39], s56 offen offset:0 lds\n\t"
    "v_mfma_f32_16x16x32_bf16 v[128:131], a[48:51], a[16:19], v[128:131]\n\t"
    "v_mfma_f32_16x16x32_bf16 v[160:163], a[48:51], a[20:23], v[160:163]\n\t"
    "ds_read_b128 a[28:31], v201 offset:2048\n\t"
    "v_mfma_f32_16x16x32_bf16 v[4:7], a[52:55], a[0:3], v[4:7]\n\t"
    "ds_read_b64_tr_b16 a[84:85], v203 offset:16384\n\t"
    "v_mfma_f32_16x16x32_bf16 v[36:39], a[52:55], a[4:7], v[36:39]\n\t"
    "buffer_load_dwordx4 v193, s[36:39], s56 offen offset:1024 lds\n\t"
    "v_mfma_f32_16x16x32_bf16 v[68:71], a[52:55], a[8:11], v[68:71]\n\t"
    "ds_read_b64_tr_b16 a[86:87], v203 offset:20480\n\t"
    "v_mfma_f32_16x16x32_bf16 v[100:103], a[52:55], a[12:15], v[100:103]\n\t"
    "v_mfma_f32_16x16x32_bf16 v[132:135], a[52:55], a[16:19], v[132:135]\n\t"
    "ds_read_b128 a[32:35], v201 offset:4096\n\t"
    "v_mfma_f32_16x16x32_bf16 v[164:167], a[52:55], a[20:23], v[164:167]\n\t"
    "ds_read_b64_tr_b16 a[88:89], v202 offset:16640\n\t"
    "buffer_load_dwordx4 v194, s[36:39], s56 offen offset:2048 lds\n\t"
    "v_mfma_f32_16x16x32_bf16 v[8:11], a[56:59], a[0:3], v[8:11]\n\t"
    "v_mfma_f32_16x16x32_bf16 v[40:43], a[56:59], a[4:7], v[40:43]\n\t"
    "ds_read_b64_tr_b16 a[90:91], v202 offset:20736\n\t"
    "v_mfma_f32_16x16x32_bf16 v[72:75], a[56:59], a[8:11], v[72:75]\n\t"
    "buffer_load_dwordx4 v195, s[36:39], s56 offen offset:3072 lds\n\t"
    "v_mfma_f32_16x16x32_bf16 v[104:107], a[56:59], a[12:15], v[104:107]\n\t"
    "ds_read_b128 a[36:39], v201 offset:6144\n\t"
    "v_mfma_f32_16x16x32_bf16 v[136:139], a[56:59], a[16:19], v[136:139]\n\t"
    "ds_read_b64_tr_b16 a[92:93], v203 offset:16640\n\t"
    "v_mfma_f32_16x16x32_bf16 v[168:171], a[56:59], a[20:23], v[168:171]\n\t"
    "v_mfma_f32_16x16x32_bf16 v[12:15], a[60:63], a[0:3], v[12:15]\n\t"
    "ds_read_b64_tr_b16 a[94:95], v203 offset:20736\n\t"
    "s_addk_i32 m0, 4096\n\t"
    "v_mfma_f32_16x16x32_bf16 v[44:47], a[60:63], a[4:7], v[44:47]\n\t"
    "v_mfma_f32_16x16x32_bf16 v[76:79], a[60:63], a[8:11], v[76:79]\n\t"
    "ds_read_b128 a[40:43], v201 offset:8192\n\t"
    "v_mfma_f32_16x16x32_bf16 v[108:111], a[60:63], a[12:15], v[108:111]\n\t"
    "ds_read_b64_tr_b16 a[96:97], v202 offset:16896\n\t"
    "v_mfma_f32_16x16x32_bf16 v[140:143], a[60:63], a[16:19], v[140:143]\n\t"
    "buffer_load_dwordx4 v192, s[36:39], s57 offen offset:0 lds\n\t"
    "v_mfma_f32_16x16x32_bf16 v[172:175], a[60:63], a[20:23], v[172:175]\n\t"
    "ds_read_b64_tr_b16 a[98:99], v202 offset:20992\n\t"
    "v_mfma_f32_16x16x32_bf16 v[16:19], a[64:67], a[0:3], v[16:19]\n\t"
    "v_mfma_f32_16x16x32_bf16 v[48:51], a[64:67], a[4:7], v[48:51]\n\t"
    "ds_read_b128 a[44:47], v201 offset:10240\n\t"
    "buffer_load_dwordx4 v193, s[36:39], s57 offen offset:1024 lds\n\t"
    "v_mfma_f32_16x16x32_bf16 v[80:83], a[64:67], a[8:11], v[80:83]\n\t"
    "ds_read_b64_tr_b16 a[100:101], v203 offset:16896\n\t"
    "v_mfma_f32_16x16x32_bf16 v[112:115], a[64:67], a[12:15], v[112:115]\n\t"
    "v_mfma_f32_16x16x32_bf16 v[144:147], a[64:67], a[16:19], v[144:147]\n\t"
    "ds_read_b64_tr_b16 a[102:103], v203 offset:20992\n\t"
    "v_mfma_f32_16x16x32_bf16 v[176:179], a[64:67], a[20:23], v[176:179]\n\t"
    "s_addk_i32 s56, 128\n\t"
    "v_mfma_f32_16x16x32_bf16 v[20:23], a[68:71], a[0:3], v[20:23]\n\t"
    "ds_read_b64_tr_b16 a[104:105], v202 offset:17152\n\t"
    "v_mfma_f32_16x16x32_bf16 v[52:55], a[68:71], a[4:7], v[52:55]\n\t"
    "ds_read_b64_tr_b16 a[106:107], v202 offset:21248\n\t"
    "v_mfma_f32_16x16x32_bf16 v[84:87], a[68:71], a[8:11], v[84:87]\n\t"
    "v_mfma_f32_16x16x32_bf16 v[116:119], a[68:71], a[12:15], v[116:119]\n\t"
    "ds_read_b64_tr_b16 a[108:109], v203 offset:17152\n\t"
    "s_addk_i32 s57, 128\n\t"
    "v_mfma_f32_16x16x32_bf16 v[148:151], a[68:71], a[16:19], v[148:151]\n\t"
    "v_mfma_f32_16x16x32_bf16 v[180:183], a[68:71], a[20:23], v[180:183]\n\t"
    "ds_read_b64_tr_b16 a[110:111], v203 offset:21248\n\t"
    "v_mfma_f32_16x16x32_bf16 v[24:27], a[72:75], a[0:3], v[24:27]\n\t"
    "buffer_load_dwordx4 a[184:187], v206, s[44:47], s62 offen offset:2048 nt\n\t"
    "v_mfma_f32_16x16x32_bf16 v[56:59], a[72:75], a[4:7], v[56:59]\n\t"
    "v_mfma_f32_16x16x32_bf16 v[88:91], a[72:75], a[8:11], v[88:91]\n\t"
    "v_mfma_f32_16x16x32_bf16 v[120:123], a[72:75], a[12:15], v[120:123]\n\t"
    "v_mfma_f32_16x16x32_bf16 v[152:155], a[72:75], a[16:19], v[152:155]\n\t"
    "buffer_load_dwordx4 a[188:191], v207, s[44:47], s62 offen offset:3072 nt\n\t"
    "v_mfma_f32_16x16x32_bf16 v[184:187], a[72:75], a[20:23], v[184:187]\n\t"
    "v_mfma_f32_16x16x32_bf16 v[28:31], a[76:79], a[0:3], v[28:31]\n\t"
    "v_mfma_f32_16x16x32_bf16 v[60:63], a[76:79], a[4:7], v[60:63]\n\t"
    "v_mfma_f32_16x16x32_bf16 v[92:95], a[76:79], a[8:11], v[92:95]\n\t"
    "buffer_load_dwordx4 a[192:195], v208, s[48:51], s62 offen offset:0 nt\n\t"
    "v_mfma_f32_16x16x32_bf16 v[124:127], a[76:79], a[12:15], v[124:127]\n\t"
    "v_mfma_f32_16x16x32_bf16 v[156:159], a[76:79], a[16:19], v[156:159]\n\t"
    "v_mfma_f32_16x16x32_bf16 v[188:191], a[76:79], a[20:23], v[188:191]\n\t"
    "s_waitcnt vmcnt(13) lgkmcnt(0)\n\t"
    "s_barrier\n\t"
    "v_mfma_f32_16x16x32_bf16 v[0:3], a[80:83], a[24:27], v[0:3]\n\t"
    "ds_read_b128 a[0:3], v200 offset:24576\n\t"
    "s_add_u32 m0, s61, 0\n\t"
    "v_mfma_f32_16x16x32_bf16 v[32:35], a[80:83], a[28:31], v[32:35]\n\t"
    "ds_read_b64_tr_b16 a[48:49], v202 offset:32768\n\t"
    "v_mfma_f32_16x16x32_bf16 v[64:67], a[80:83], a[32:35], v[64:67]\n\t"
    "v_mfma_f32_16x16x32_bf16 v[96:99], a[80:83], a[36:39], v[96:99]\n\t"
    "ds_read_b64_tr_b16 a[50:51], v202 offset:36864\n\t"
    "v_mfma_f32_16x16x32_bf16 v[128:131], a[80:83], a[40:43], v[128:131]\n\t"
    "buffer_load_dwordx4 v196, s[40:43], s58 offen offset:0 lds\n\t"
    "v_mfma_f32_16x16x32_bf16 v[160:163], a[80:83], a[44:47], v[160:163]\n\t"
    "ds_read_b128 a[4:7], v200 offset:26624\n\t"
    "v_mfma_f32_16x16x32_bf16 v[4:7], a[84:87], a[24:27], v[4:7]\n\t"
    "ds_read_b64_tr_b16 a[52:53], v203 offset:32768\n\t"
    "v_mfma_f32_16x16x32_bf16 v[36:39], a[84:87], a[28:31], v[36:39]\n\t"
    "v_mfma_f32_16x16x32_bf16 v[68:71], a[84:87], a[32:35], v[68:71]\n\t"
    "ds_read_b64_tr_b16 a[54:55], v203 offset:36864\n\t"
    "buffer_load_dwordx4 v197, s[40:43], s58 offen offset:1024 lds\n\t"
    "v_mfma_f32_16x16x32_bf16 v[100:103], a[84:87], a[36:39], v[100:103]\n\t"
    "v_mfma_f32_16x16x32_bf16 v[132:135], a[84:87], a[40:43], v[132:135]\n\t"
    "ds_read_b128 a[8:11], v200 offset:28672\n\t"
    "v_mfma_f32_16x16x32_bf16 v[164:167], a[84:87], a[44:47], v[164:167]\n\t"
    "ds_read_b64_tr_b16 a[56:57], v202 offset:33024\n\t"
    "v_mfma_f32_16x16x32_bf16 v[8:11], a[88:91], a[24:27], v[8:11]\n\t"
    "buffer_load_dwordx4 v198, s[40:43], s58 offen offset:2048 lds\n\t"
    "v_mfma_f32_16x16x32_bf16 v[40:43], a[88:91], a[28:31], v[40:43]\n\t"
    "ds_read_b64_tr_b16 a[58:59], v202 offset:37120\n\t"
    "v_mfma_f32_16x16x32_bf16 v[72:75], a[88:91], a[32:35], v[72:75]\n\t"
    "v_mfma_f32_16x16x32_bf16 v[104:107], a[88:91], a[36:39], v[104:107]\n\t"
    "ds_read_b128 a[12:15], v200 offset:30720\n\t"
    "v_mfma_f32_16x16x32_bf16 v[136:139], a[88:91], a[40:43], v[136:139]\n\t"
    "ds_read_b64_tr_b16 a[60:61], v203 offset:33024\n\t"
    "buffer_load_dwordx4 v199, s[40:43], s58 offen offset:3072 lds\n\t"
    "v_mfma_f32_16x16x32_bf16 v[168:171], a[88:91], a[44:47], v[168:171]\n\t"
    "v_mfma_f32_16x16x32_bf16 v[12:15], a[92:95], a[24:27], v[12:15]\n\t"
    "ds_read_b64_tr_b16 a[62:63], v203 offset:37120\n\t"
    "v_mfma_f32_16x16x32_bf16 v[44:47], a[92:95], a[28:31], v[44:47]\n\t"
    "v_mfma_f32_16x16x32_bf16 v[76:79], a[92:95], a[32:35], v[76:79]\n\t"
    "ds_read_b128 a[16:19], v200 offset:32768\n\t"
    "s_add_u32 s58, s58, 262144\n\t"
    "v_mfma_f32_16x16x32_bf16 v[108:111], a[92:95], a[36:39], v[108:111]\n\t"
    "ds_read_b64_tr_b16 a[64:65], v202 offset:33280\n\t"
    "v_mfma_f32_16x16x32_bf16 v[140:143], a[92:95], a[40:43], v[140:143]\n\t"
    "v_mfma_f32_16x16x32_bf16 v[172:175], a[92:95], a[44:47], v[172:175]\n\t"
    "ds_read_b64_tr_b16 a[66:67], v202 offset:37376\n\t"
    "v_mfma_f32_16x16x32_bf16 v[16:19], a[96:99], a[24:27], v[16:19]\n\t"
    "s_add_u32 m0, s61, 16384\n\t"
    "v_mfma_f32_16x16x32_bf16 v[48:51], a[96:99], a[28:31], v[48:51]\n\t"
    "ds_read_b128 a[20:23], v200 offset:34816\n\t"
    "v_mfma_f32_16x16x32_bf16 v[80:83], a[96:99], a[32:35], v[80:83]\n\t"
    "ds_read_b64_tr_b16 a[68:69], v203 offset:33280\n\t"
    "v_mfma_f32_16x16x32_bf16 v[112:115], a[96:99], a[36:39], v[112:115]\n\t"
    "v_mfma_f32_16x16x32_bf16 v[144:147], a[96:99], a[40:43], v[144:147]\n\t"
    "ds_read_b64_tr_b16 a[70:71], v203 offset:37376\n\t"
    "buffer_load_dwordx4 v196, s[40:43], s58 offen offset:0 lds\n\t"
    "v_mfma_f32_16x16x32_bf16 v[176:179], a[96:99], a[44:47], v[176:179]\n\t"
    "v_mfma_f32_16x16x32_bf16 v[20:23], a[100:103], a[24:27], v[20:23]\n\t"
    "ds_read_b64_tr_b16 a[72:73], v202 offset:33536\n\t"
    "v_mfma_f32_16x16x32_bf16 v[52:55], a[100:103], a[28:31], v[52:55]\n\t"
    "ds_read_b64_tr_b16 a[74:75], v202 offset:37632\n\t"
    "v_mfma_f32_16x16x32_bf16 v[84:87], a[100:103], a[32:35], v[84:87]\n\t"
    "buffer_load_dwordx4 v197, s[40:43], s58 offen offset:1024 lds\n\t"
    "v_mfma_f32_16x16x32_bf16 v[116:119], a[100:103], a[36:39], v[116:119]\n\t"
    "ds_read_b64_tr_b16 a[76:77], v203 offset:33536\n\t"
    "v_mfma_f32_16x16x32_bf16 v[148:151], a[100:103], a[40:43], v[148:151]\n\t"
    "v_mfma_f32_16x16x32_bf16 v[180:183], a[100:103], a[44:47], v[180:183]\n\t"
    "ds_read_b64_tr_b16 a[78:79], v203 offset:37632\n\t"
    "v_mfma_f32_16x16x32_bf16 v[24:27], a[104:107], a[24:27], v[24:27]\n\t"
    "buffer_load_dwordx4 v198, s[40:43], s58 offen offset:2048 lds\n\t"
    "v_mfma_f32_16x16x32_bf16 v[56:59], a[104:107], a[28:31], v[56:59]\n\t"
    "v_mfma_f32_16x16x32_bf16 v[88:91], a[104:107], a[32:35], v[88:91]\n\t"
    "v_mfma_f32_16x16x32_bf16 v[120:123], a[104:107], a[36:39], v[120:123]\n\t"
    "v_mfma_f32_16x16x32_bf16 v[152:155], a[104:107], a[40:43], v[152:155]\n\t"
    "buffer_load_dwordx4 v199, s[40:43], s58 offen offset:3072 lds\n\t"
    "v_mfma_f32_16x16x32_bf16 v[184:187], a[104:107], a[44:47], v[184:187]\n\t"
    "v_mfma_f32_16x16x32_bf16 v[28:31], a[108:111], a[24:27], v[28:31]\n\t"
    "v_mfma_f32_16x16x32_bf16 v[60:63], a[108:111], a[28:31], v[60:63]\n\t"
    "v_mfma_f32_16x16x32_bf16 v[92:95], a[108:111], a[32:35], v[92:95]\n\t"
    "s_add_u32 s58, s58, 262144\n\t"
    "v_mfma_f32_16x16x32_bf16 v[124:127], a[108:111], a[36:39], v[124:127]\n\t"
    "v_mfma_f32_16x16x32_bf16 v[156:159], a[108:111], a[40:43], v[156:159]\n\t"
    "v_mfma_f32_16x16x32_bf16 v[188:191], a[108:111], a[44:47], v[188:191]\n\t"
    "s_waitcnt lgkmcnt(0)\n\t"
    "v_mfma_f32_16x16x32_bf16 v[0:3], a[48:51], a[0:3], v[0:3]\n\t"
    "ds_read_b128 a[24:27], v201 offset:24576\n\t"
    "s_add_u32 m0, s60, 0\n\t"
    "v_mfma_f32_16x16x32_bf16 v[32:35], a[48:51], a[4:7], v[32:35]\n\t"
    "ds_read_b64_tr_b16 a[80:81], v202 offset:49152\n\t"
    "v_mfma_f32_16x16x32_bf16 v[64:67], a[48:51], a[8:11], v[64:67]\n\t"
    "v_mfma_f32_16x16x32_bf16 v[96:99], a[48:51], a[12:15], v[96:99]\n\t"
    "ds_read_b64_tr_b16 a[82:83], v202 offset:53248\n\t"
    "buffer_load_dwordx4 v192, s[36:39], s56 offen offset:0 lds\n\t"
    "v_mfma_f32_16x16x32_bf16 v[128:131], a[48:51], a[16:19], v[128:131]\n\t"
    "v_mfma_f32_16x16x32_bf16 v[160:163], a[48:51], a[20:23], v[160:163]\n\t"
    "ds_read_b128 a[28:31], v201 offset:26624\n\t"
    "v_mfma_f32_16x16x32_bf16 v[4:7], a[52:55], a[0:3], v[4:7]\n\t"
    "ds_read_b64_tr_b16 a[84:85], v203 offset:49152\n\t"
    "v_mfma_f32_16x16x32_bf16 v[36:39], a[52:55], a[4:7], v[36:39]\n\t"
    "buffer_load_dwordx4 v193, s[36:39], s56 offen offset:1024 lds\n\t"
    "v_mfma_f32_16x16x32_bf16 v[68:71], a[52:55], a[8:11], v[68:71]\n\t"
    "ds_read_b64_tr_b16 a[86:87], v203 offset:53248\n\t"
    "v_mfma_f32_16x16x32_bf16 v[100:103], a[52:55], a[12:15], v[100:103]\n\t"
    "v_mfma_f32_16x16x32_bf16 v[132:135], a[52:55], a[16:19], v[132:135]\n\t"
    "ds_read_b128 a[32:35], v201 offset:28672\n\t"
    "v_mfma_f32_16x16x32_bf16 v[164:167], a[52:55], a[20:23], v[164:167]\n\t"
    "ds_read_b64_tr_b16 a[88:89], v202 offset:49408\n\t"
    "buffer_load_dwordx4 v194, s[36:39], s56 offen offset:2048 lds\n\t"
    "v_mfma_f32_16x16x32_bf16 v[8:11], a[56:59], a[0:3], v[8:11]\n\t"
    "v_mfma_f32_16x16x32_bf16 v[40:43], a[56:59], a[4:7], v[40:43]\n\t"
    "ds_read_b64_tr_b16 a[90:91], v202 offset:53504\n\t"
    "v_mfma_f32_16x16x32_bf16 v[72:75], a[56:59], a[8:11], v[72:75]\n\t"
    "buffer_load_dwordx4 v195, s[36:39], s56 offen offset:3072 lds\n\t"
    "v_mfma_f32_16x16x32_bf16 v[104:107], a[56:59], a[12:15], v[104:107]\n\t"
    "ds_read_b128 a[36:39], v201 offset:30720\n\t"
    "v_mfma_f32_16x16x32_bf16 v[136:139], a[56:59], a[16:19], v[136:139]\n\t"
    "ds_read_b64_tr_b16 a[92:93], v203 offset:49408\n\t"
    "v_mfma_f32_16x16x32_bf16 v[168:171], a[56:59], a[20:23], v[168:171]\n\t"
    "v_mfma_f32_16x16x32_bf16 v[12:15], a[60:63], a[0:3], v[12:15]\n\t"
    "ds_read_b64_tr_b16 a[94:95], v203 offset:53504\n\t"
    "s_addk_i32 m0, 4096\n\t"
    "v_mfma_f32_16x16x32_bf16 v[44:47], a[60:63], a[4:7], v[44:47]\n\t"
    "v_mfma_f32_16x16x32_bf16 v[76:79], a[60:63], a[8:11], v[76:79]\n\t"
    "ds_read_b128 a[40:43], v201 offset:32768\n\t"
    "v_mfma_f32_16x16x32_bf16 v[108:111], a[60:63], a[12:15], v[108:111]\n\t"
    "ds_read_b64_tr_b16 a[96:97], v202 offset:49664\n\t"
    "v_mfma_f32_16x16x32_bf16 v[140:143], a[60:63], a[16:19], v[140:143]\n\t"
    "buffer_load_dwordx4 v192, s[36:39], s57 offen offset:0 lds\n\t"
    "v_mfma_f32_16x16x32_bf16 v[172:175], a[60:63], a[20:23], v[172:175]\n\t"
    "ds_read_b64_tr_b16 a[98:99], v202 offset:53760\n\t"
    "v_mfma_f32_16x16x32_bf16 v[16:19], a[64:67], a[0:3], v[16:19]\n\t"
    "v_mfma_f32_16x16x32_bf16 v[48:51], a[64:67], a[4:7], v[48:51]\n\t"
    "ds_read_b128 a[44:47], v201 offset:34816\n\t"
    "buffer_load_dwordx4 v193, s[36:39], s57 offen offset:1024 lds\n\t"
    "v_mfma_f32_16x16x32_bf16 v[80:83], a[64:67], a[8:11], v[80:83]\n\t"
    "ds_read_b64_tr_b16 a[100:101], v203 offset:49664\n\t"
    "v_mfma_f32_16x16x32_bf16 v[112:115], a[64:67], a[12:15], v[112:115]\n\t"
    "v_mfma_f32_16x16x32_bf16 v[144:147], a[64:67], a[16:19], v[144:147]\n\t"
    "ds_read_b64_tr_b16 a[102:103], v203 offset:53760\n\t"
    "v_mfma_f32_16x16x32_bf16 v[176:179], a[64:67], a[20:23], v[176:179]\n\t"
    "s_addk_i32 s56, 128\n\t"
    "v_mfma_f32_16x16x32_bf16 v[20:23], a[68:71], a[0:3], v[20:23]\n\t"
    "ds_read_b64_tr_b16 a[104:105], v202 offset:49920\n\t"
    "v_mfma_f32_16x16x32_bf16 v[52:55], a[68:71], a[4:7], v[52:55]\n\t"
    "ds_read_b64_tr_b16 a[106:107], v202 offset:54016\n\t"
    "v_mfma_f32_16x16x32_bf16 v[84:87], a[68:71], a[8:11], v[84:87]\n\t"
    "v_mfma_f32_16x16x32_bf16 v[116:119], a[68:71], a[12:15], v[116:119]\n\t"
    "ds_read_b64_tr_b16 a[108:109], v203 offset:49920\n\t"
    "s_addk_i32 s57, 128\n\t"
    "v_mfma_f32_16x16x32_bf16 v[148:151], a[68:71], a[16:19], v[148:151]\n\t"
    "v_mfma_f32_16x16x32_bf16 v[180:183], a[68:71], a[20:23], v[180:183]\n\t"
    "ds_read_b64_tr_b16 a[110:111], v203 offset:54016\n\t"
    "v_mfma_f32_16x16x32_bf16 v[24:27], a[72:75], a[0:3], v[24:27]\n\t"
    "buffer_load_dwordx4 a[196:199], v209, s[48:51], s62 offen offset:1024 nt\n\t"
    "v_mfma_f32_16x16x32_bf16 v[56:59], a[72:75], a[4:7], v[56:59]\n\t"
    "v_mfma_f32_16x16x32_bf16 v[88:91], a[72:75], a[8:11], v[88:91]\n\t"
    "v_mfma_f32_16x16x32_bf16 v[120:123], a[72:75], a[12:15], v[120:123]\n\t"
    "v_mfma_f32_16x16x32_bf16 v[152:155], a[72:75], a[16:19], v[152:155]\n\t"
    "buffer_load_dwordx4 a[200:203], v221, s[48:51], s62 offen offset:2048 nt\n\t"
    "v_mfma_f32_16x16x32_bf16 v[184:187], a[72:75], a[20:23], v[184:187]\n\t"
    "v_mfma_f32_16x16x32_bf16 v[28:31], a[76:79], a[0:3], v[28:31]\n\t"
    "v_mfma_f32_16x16x32_bf16 v[60:63], a[76:79], a[4:7], v[60:63]\n\t"
    "v_mfma_f32_16x16x32_bf16 v[92:95], a[76:79], a[8:11], v[92:95]\n\t"
    "buffer_load_dwordx4 a[204:207], v246, s[48:51], s62 offen offset:3072 nt\n\t"
    "s_add_u32 s62, s62, 8192\n\t"
    "v_mfma_f32_16x16x32_bf16 v[124:127], a[76:79], a[12:15], v[124:127]\n\t"
    "v_mfma_f32_16x16x32_bf16 v[156:159], a[76:79], a[16:19], v[156:159]\n\t"
    "v_mfma_f32_16x16x32_bf16 v[188:191], a[76:79], a[20:23], v[188:191]\n\t"
    "s_waitcnt vmcnt(13) lgkmcnt(0)\n\t"
    "s_barrier\n\t"
    "v_mfma_f32_16x16x32_bf16 v[0:3], a[80:83], a[24:27], v[0:3]\n\t"
    "ds_read_b128 a[0:3], v200 offset:49152\n\t"
    "s_add_u32 m0, s61, 32768\n\t"
    "v_mfma_f32_16x16x32_bf16 v[32:35], a[80:83], a[28:31], v[32:35]\n\t"
    "ds_read_b64_tr_b16 a[48:49], v248 offset:0\n\t"
    "v_mfma_f32_16x16x32_bf16 v[64:67], a[80:83], a[32:35], v[64:67]\n\t"
    "v_mfma_f32_16x16x32_bf16 v[96:99], a[80:83], a[36:39], v[96:99]\n\t"
    "ds_read_b64_tr_b16 a[50:51], v248 offset:4096\n\t"
    "v_mfma_f32_16x16x32_bf16 v[128:131], a[80:83], a[40:43], v[128:131]\n\t"
    "buffer_load_dwordx4 v196, s[40:43], s58 offen offset:0 lds\n\t"
    "v_mfma_f32_16x16x32_bf16 v[160:163], a[80:83], a[44:47], v[160:163]\n\t"
    "ds_read_b128 a[4:7], v200 offset:51200\n\t"
    "v_mfma_f32_16x16x32_bf16 v[4:7], a[84:87], a[24:27], v[4:7]\n\t"
    "ds_read_b64_tr_b16 a[52:53], v249 offset:0\n\t"
    "v_mfma_f32_16x16x32_bf16 v[36:39], a[84:87], a[28:31], v[36:39]\n\t"
    "v_mfma_f32_16x16x32_bf16 v[68:71], a[84:87], a[32:35], v[68:71]\n\t"
    "ds_read_b64_tr_b16 a[54:55], v249 offset:4096\n\t"
    "buffer_load_dwordx4 v197, s[40:43], s58 offen offset:1024 lds\n\t"
    "v_mfma_f32_16x16x32_bf16 v[100:103], a[84:87], a[36:39], v[100:103]\n\t"
    "v_mfma_f32_16x16x32_bf16 v[132:135], a[84:87], a[40:43], v[132:135]\n\t"
    "ds_read_b128 a[8:11], v200 offset:53248\n\t"
    "v_mfma_f32_16x16x32_bf16 v[164:167], a[84:87], a[44:47], v[164:167]\n\t"
    "ds_read_b64_tr_b16 a[56:57], v248 offset:256\n\t"
    "v_mfma_f32_16x16x32_bf16 v[8:11], a[88:91], a[24:27], v[8:11]\n\t"
    "buffer_load_dwordx4 v198, s[40:43], s58 offen offset:2048 lds\n\t"
    "v_mfma_f32_16x16x32_bf16 v[40:43], a[88:91], a[28:31], v[40:43]\n\t"
    "ds_read_b64_tr_b16 a[58:59], v248 offset:4352\n\t"
    "v_mfma_f32_16x16x32_bf16 v[72:75], a[88:91], a[32:35], v[72:75]\n\t"
    "v_mfma_f32_16x16x32_bf16 v[104:107], a[88:91], a[36:39], v[104:107]\n\t"
    "ds_read_b128 a[12:15], v200 offset:55296\n\t"
    "v_mfma_f32_16x16x32_bf16 v[136:139], a[88:91], a[40:43], v[136:139]\n\t"
    "ds_read_b64_tr_b16 a[60:61], v249 offset:256\n\t"
    "buffer_load_dwordx4 v199, s[40:43], s58 offen offset:3072 lds\n\t"
    "v_mfma_f32_16x16x32_bf16 v[168:171], a[88:91], a[44:47], v[168:171]\n\t"
    "v_mfma_f32_16x16x32_bf16 v[12:15], a[92:95], a[24:27], v[12:15]\n\t"
    "ds_read_b64_tr_b16 a[62:63], v249 offset:4352\n\t"
    "v_mfma_f32_16x16x32_bf16 v[44:47], a[92:95], a[28:31], v[44:47]\n\t"
    "v_mfma_f32_16x16x32_bf16 v[76:79], a[92:95], a[32:35], v[76:79]\n\t"
    "ds_read_b128 a[16:19], v200 offset:57344\n\t"
    "s_add_u32 s58, s58, 262144\n\t"
    "v_mfma_f32_16x16x32_bf16 v[108:111], a[92:95], a[36:39], v[108:111]\n\t"
    "ds_read_b64_tr_b16 a[64:65], v248 offset:512\n\t"
    "v_mfma_f32_16x16x32_bf16 v[140:143], a[92:95], a[40:43], v[140:143]\n\t"
    "v_mfma_f32_16x16x32_bf16 v[172:175], a[92:95], a[44:47], v[172:175]\n\t"
    "ds_read_b64_tr_b16 a[66:67], v248 offset:4608\n\t"
    "v_mfma_f32_16x16x32_bf16 v[16:19], a[96:99], a[24:27], v[16:19]\n\t"
    "s_add_u32 m0, s61, 49152\n\t"
    "v_mfma_f32_16x16x32_bf16 v[48:51], a[96:99], a[28:31], v[48:51]\n\t"
    "ds_read_b128 a[20:23], v200 offset:59392\n\t"
    "v_mfma_f32_16x16x32_bf16 v[80:83], a[96:99], a[32:35], v[80:83]\n\t"
    "ds_read_b64_tr_b16 a[68:69], v249 offset:512\n\t"
    "v_mfma_f32_16x16x32_bf16 v[112:115], a[96:99], a[36:39], v[112:115]\n\t"
    "v_mfma_f32_16x16x32_bf16 v[144:147], a[96:99], a[40:43], v[144:147]\n\t"
    "ds_read_b64_tr_b16 a[70:71], v249 offset:4608\n\t"
    "buffer_load_dwordx4 v196, s[40:43], s58 offen offset:0 lds\n\t"
    "v_mfma_f32_16x16x32_bf16 v[176:179], a[96:99], a[44:47], v[176:179]\n\t"
    "v_mfma_f32_16x16x32_bf16 v[20:23], a[100:103], a[24:27], v[20:23]\n\t"
    "ds_read_b64_tr_b16 a[72:73], v248 offset:768\n\t"
    "v_mfma_f32_16x16x32_bf16 v[52:55], a[100:103], a[28:31], v[52:55]\n\t"
    "ds_read_b64_tr_b16 a[74:75], v248 offset:4864\n\t"
    "v_mfma_f32_16x16x32_bf16 v[84:87], a[100:103], a[32:35], v[84:87]\n\t"
    "buffer_load_dwordx4 v197, s[40:43], s58 offen offset:1024 lds\n\t"
    "v_mfma_f32_16x16x32_bf16 v[116:119], a[100:103], a[36:39], v[116:119]\n\t"
    "ds_read_b64_tr_b16 a[76:77], v249 offset:768\n\t"
    "v_mfma_f32_16x16x32_bf16 v[148:151], a[100:103], a[40:43], v[148:151]\n\t"
    "v_mfma_f32_16x16x32_bf16 v[180:183], a[100:103], a[44:47], v[180:183]\n\t"
    "ds_read_b64_tr_b16 a[78:79], v249 offset:4864\n\t"
    "v_mfma_f32_16x16x32_bf16 v[24:27], a[104:107], a[24:27], v[24:27]\n\t"
    "buffer_load_dwordx4 v198, s[40:43], s58 offen offset:2048 lds\n\t"
    "v_mfma_f32_16x16x32_bf16 v[56:59], a[104:107], a[28:31], v[56:59]\n\t"
    "v_mfma_f32_16x16x32_bf16 v[88:91], a[104:107], a[32:35], v[88:91]\n\t"
    "v_mfma_f32_16x16x32_bf16 v[120:123], a[104:107], a[36:39], v[120:123]\n\t"
    "v_mfma_f32_16x16x32_bf16 v[152:155], a[104:107], a[40:43], v[152:155]\n\t"
    "buffer_load_dwordx4 v199, s[40:43], s58 offen offset:3072 lds\n\t"
    "v_mfma_f32_16x16x32_bf16 v[184:187], a[104:107], a[44:47], v[184:187]\n\t"
    "v_mfma_f32_16x16x32_bf16 v[28:31], a[108:111], a[24:27], v[28:31]\n\t"
    "v_mfma_f32_16x16x32_bf16 v[60:63], a[108:111], a[28:31], v[60:63]\n\t"
    "v_mfma_f32_16x16x32_bf16 v[92:95], a[108:111], a[32:35], v[92:95]\n\t"
    "s_add_u32 s58, s58, 262144\n\t"
    "v_mfma_f32_16x16x32_bf16 v[124:127], a[108:111], a[36:39], v[124:127]\n\t"
    "v_mfma_f32_16x16x32_bf16 v[156:159], a[108:111], a[40:43], v[156:159]\n\t"
    "v_mfma_f32_16x16x32_bf16 v[188:191], a[108:111], a[44:47], v[188:191]\n\t"
    "s_waitcnt lgkmcnt(0)\n\t"
    "v_mfma_f32_16x16x32_bf16 v[0:3], a[48:51], a[0:3], v[0:3]\n\t"
    "ds_read_b128 a[24:27], v201 offset:49152\n\t"
    "s_add_u32 m0, s60, 24576\n\t"
    "v_mfma_f32_16x16x32_bf16 v[32:35], a[48:51], a[4:7], v[32:35]\n\t"
    "ds_read_b64_tr_b16 a[80:81], v202 offset:0\n\t"
    "v_mfma_f32_16x16x32_bf16 v[64:67], a[48:51], a[8:11], v[64:67]\n\t"
    "v_mfma_f32_16x16x32_bf16 v[96:99], a[48:51], a[12:15], v[96:99]\n\t"
    "ds_read_b64_tr_b16 a[82:83], v202 offset:4096\n\t"
    "buffer_load_dwordx4 v192, s[36:39], s56 offen offset:0 lds\n\t"
    "v_mfma_f32_16x16x32_bf16 v[128:131], a[48:51], a[16:19], v[128:131]\n\t"
    "v_mfma_f32_16x16x32_bf16 v[160:163], a[48:51], a[20:23], v[160:163]\n\t"
    "ds_read_b128 a[28:31], v201 offset:51200\n\t"
    "v_mfma_f32_16x16x32_bf16 v[4:7], a[52:55], a[0:3], v[4:7]\n\t"
    "ds_read_b64_tr_b16 a[84:85], v203 offset:0\n\t"
    "v_mfma_f32_16x16x32_bf16 v[36:39], a[52:55], a[4:7], v[36:39]\n\t"
    "buffer_load_dwordx4 v193, s[36:39], s56 offen offset:1024 lds\n\t"
    "v_mfma_f32_16x16x32_bf16 v[68:71], a[52:55], a[8:11], v[68:71]\n\t"
    "ds_read_b64_tr_b16 a[86:87], v203 offset:4096\n\t"
    "v_mfma_f32_16x16x32_bf16 v[100:103], a[52:55], a[12:15], v[100:103]\n\t"
    "v_mfma_f32_16x16x32_bf16 v[132:135], a[52:55], a[16:19], v[132:135]\n\t"
    "ds_read_b128 a[32:35], v201 offset:53248\n\t"
    "v_mfma_f32_16x16x32_bf16 v[164:167], a[52:55], a[20:23], v[164:167]\n\t"
    "ds_read_b64_tr_b16 a[88:89], v202 offset:256\n\t"
    "buffer_load_dwordx4 v194, s[36:39], s56 offen offset:2048 lds\n\t"
    "v_mfma_f32_16x16x32_bf16 v[8:11], a[56:59], a[0:3], v[8:11]\n\t"
    "v_mfma_f32_16x16x32_bf16 v[40:43], a[56:59], a[4:7], v[40:43]\n\t"
    "ds_read_b64_tr_b16 a[90:91], v202 offset:4352\n\t"
    "v_mfma_f32_16x16x32_bf16 v[72:75], a[56:59], a[8:11], v[72:75]\n\t"
    "buffer_load_dwordx4 v195, s[36:39], s56 offen offset:3072 lds\n\t"
    "v_mfma_f32_16x16x32_bf16 v[104:107], a[56:59], a[12:15], v[104:107]\n\t"
    "ds_read_b128 a[36:39], v201 offset:55296\n\t"
    "v_mfma_f32_16x16x32_bf16 v[136:139], a[56:59], a[16:19], v[136:139]\n\t"
    "ds_read_b64_tr_b16 a[92:93], v203 offset:256\n\t"
    "v_mfma_f32_16x16x32_bf16 v[168:171], a[56:59], a[20:23], v[168:171]\n\t"
    "v_mfma_f32_16x16x32_bf16 v[12:15], a[60:63], a[0:3], v[12:15]\n\t"
    "ds_read_b64_tr_b16 a[94:95], v203 offset:4352\n\t"
    "s_addk_i32 m0, 4096\n\t"
    "v_mfma_f32_16x16x32_bf16 v[44:47], a[60:63], a[4:7], v[44:47]\n\t"
    "v_mfma_f32_16x16x32_bf16 v[76:79], a[60:63], a[8:11], v[76:79]\n\t"
    "ds_read_b128 a[40:43], v201 offset:57344\n\t"
    "v_mfma_f32_16x16x32_bf16 v[108:111], a[60:63], a[12:15], v[108:111]\n\t"
    "ds_read_b64_tr_b16 a[96:97], v202 offset:512\n\t"
    "v_mfma_f32_16x16x32_bf16 v[140:143], a[60:63], a[16:19], v[140:143]\n\t"
    "buffer_load_dwordx4 v192, s[36:39], s57 offen offset:0 lds\n\t"
    "v_mfma_f32_16x16x32_bf16 v[172:175], a[60:63], a[20:23], v[172:175]\n\t"
    "ds_read_b64_tr_b16 a[98:99], v202 offset:4608\n\t"
    "v_mfma_f32_16x16x32_bf16 v[16:19], a[64:67], a[0:3], v[16:19]\n\t"
    "v_mfma_f32_16x16x32_bf16 v[48:51], a[64:67], a[4:7], v[48:51]\n\t"
    "ds_read_b128 a[44:47], v201 offset:59392\n\t"
    "buffer_load_dwordx4 v193, s[36:39], s57 offen offset:1024 lds\n\t"
    "v_mfma_f32_16x16x32_bf16 v[80:83], a[64:67], a[8:11], v[80:83]\n\t"
    "ds_read_b64_tr_b16 a[100:101], v203 offset:512\n\t"
    "v_mfma_f32_16x16x32_bf16 v[112:115], a[64:67], a[12:15], v[112:115]\n\t"
    "v_mfma_f32_16x16x32_bf16 v[144:147], a[64:67], a[16:19], v[144:147]\n\t"
    "ds_read_b64_tr_b16 a[102:103], v203 offset:4608\n\t"
    "v_mfma_f32_16x16x32_bf16 v[176:179], a[64:67], a[20:23], v[176:179]\n\t"
    "s_addk_i32 s56, 128\n\t"
    "v_mfma_f32_16x16x32_bf16 v[20:23], a[68:71], a[0:3], v[20:23]\n\t"
    "ds_read_b64_tr_b16 a[104:105], v202 offset:768\n\t"
    "v_mfma_f32_16x16x32_bf16 v[52:55], a[68:71], a[4:7], v[52:55]\n\t"
    "ds_read_b64_tr_b16 a[106:107], v202 offset:4864\n\t"
    "v_mfma_f32_16x16x32_bf16 v[84:87], a[68:71], a[8:11], v[84:87]\n\t"
    "v_mfma_f32_16x16x32_bf16 v[116:119], a[68:71], a[12:15], v[116:119]\n\t"
    "ds_read_b64_tr_b16 a[108:109], v203 offset:768\n\t"
    "s_addk_i32 s57, 128\n\t"
    "v_mfma_f32_16x16x32_bf16 v[148:151], a[68:71], a[16:19], v[148:151]\n\t"
    "v_mfma_f32_16x16x32_bf16 v[180:183], a[68:71], a[20:23], v[180:183]\n\t"
    "ds_read_b64_tr_b16 a[110:111], v203 offset:4864\n\t"
    "v_mfma_f32_16x16x32_bf16 v[24:27], a[72:75], a[0:3], v[24:27]\n\t"
    "buffer_load_dwordx4 a[208:211], v204, s[44:47], s62 offen offset:0 nt\n\t"
    "v_mfma_f32_16x16x32_bf16 v[56:59], a[72:75], a[4:7], v[56:59]\n\t"
    "v_mfma_f32_16x16x32_bf16 v[88:91], a[72:75], a[8:11], v[88:91]\n\t"
    "v_mfma_f32_16x16x32_bf16 v[120:123], a[72:75], a[12:15], v[120:123]\n\t"
    "v_mfma_f32_16x16x32_bf16 v[152:155], a[72:75], a[16:19], v[152:155]\n\t"
    "buffer_load_dwordx4 a[212:215], v205, s[44:47], s62 offen offset:1024 nt\n\t"
    "v_mfma_f32_16x16x32_bf16 v[184:187], a[72:75], a[20:23], v[184:187]\n\t"
    "v_mfma_f32_16x16x32_bf16 v[28:31], a[76:79], a[0:3], v[28:31]\n\t"
    "v_mfma_f32_16x16x32_bf16 v[60:63], a[76:79], a[4:7], v[60:63]\n\t"
    "v_mfma_f32_16x16x32_bf16 v[92:95], a[76:79], a[8:11], v[92:95]\n\t"
    "s_getpc_b64 s[68:69]\n\t"
    ".LCT3_%=:\n\t"
    "v_min_u32 v251, .LEND_%=-.LCT3_%=-4, v223\n\t"
    "global_load_dword a255, v251, s[68:69]\n\t"
    "v_mfma_f32_16x16x32_bf16 v[124:127], a[76:79], a[12:15], v[124:127]\n\t"
    "v_mfma_f32_16x16x32_bf16 v[156:159], a[76:79], a[16:19], v[156:159]\n\t"
    "v_mfma_f32_16x16x32_bf16 v[188:191], a[76:79], a[20:23], v[188:191]\n\t"
    "s_waitcnt vmcnt(13) lgkmcnt(0)\n\t"
    "s_barrier\n\t"
    "v_mfma_f32_16x16x32_bf16 v[0:3], a[80:83], a[24:27], v[0:3]\n\t"
    "ds_read_b128 a[0:3], v200 offset:0\n\t"
    "s_add_u32 m0, s61, 65536\n\t"
    "v_mfma_f32_16x16x32_bf16 v[32:35], a[80:83], a[28:31], v[32:35]\n\t"
    "ds_read_b64_tr_b16 a[48:49], v202 offset:16384\n\t"
    "v_mfma_f32_16x16x32_bf16 v[64:67], a[80:83], a[32:35], v[64:67]\n\t"
    "v_mfma_f32_16x16x32_bf16 v[96:99], a[80:83], a[36:39], v[96:99]\n\t"
    "ds_read_b64_tr_b16 a[50:51], v202 offset:20480\n\t"
    "buffer_load_dwordx4 v196, s[40:43], s58 offen offset:0 lds\n\t"
    "v_mfma_f32_16x16x32_bf16 v[128:131], a[80:83], a[40:43], v[128:131]\n\t"
    "v_mfma_f32_16x16x32_bf16 v[160:163], a[80:83], a[44:47], v[160:163]\n\t"
    "ds_read_b128 a[4:7], v200 offset:2048\n\t"
    "v_mfma_f32_16x16x32_bf16 v[4:7], a[84:87], a[24:27], v[4:7]\n\t"
    "ds_read_b64_tr_b16 a[52:53], v203 offset:16384\n\t"
    "v_mfma_f32_16x16x32_bf16 v[36:39], a[84:87], a[28:31], v[36:39]\n\t"
    "buffer_load_dwordx4 v197, s[40:43], s58 offen offset:1024 lds\n\t"
    "v_mfma_f32_16x16x32_bf16 v[68:71], a[84:87], a[32:35], v[68:71]\n\t"
    "ds_read_b64_tr_b16 a[54:55], v203 offset:20480\n\t"
    "v_mfma_f32_16x16x32_bf16 v[100:103], a[84:87], a[36:39], v[100:103]\n\t"
    "v_mfma_f32_16x16x32_bf16 v[132:135], a[84:87], a[40:43], v[132:135]\n\t"
    "ds_read_b128 a[8:11], v200 offset:4096\n\t"
    "v_mfma_f32_16x16x32_bf16 v[164:167], a[84:87], a[44:47], v[164:167]\n\t"
    "ds_read_b64_tr_b16 a[56:57], v202 offset:16640\n\t"
    "buffer_load_dwordx4 v198, s[40:43], s58 offen offset:2048 lds\n\t"
    "v_mfma_f32_16x16x32_bf16 v[8:11], a[88:91], a[24:27], v[8:11]\n\t"
    "v_mfma_f32_16x16x32_bf16 v[40:43], a[88:91], a[28:31], v[40:43]\n\t"
    "ds_read_b64_tr_b16 a[58:59], v202 offset:20736\n\t"
    "v_mfma_f32_16x16x32_bf16 v[72:75], a[88:91], a[32:35], v[72:75]\n\t"
    "buffer_load_dwordx4 v199, s[40:43], s58 offen offset:3072 lds\n\t"
    "v_mfma_f32_16x16x32_bf16 v[104:107], a[88:91], a[36:39], v[104:107]\n\t"
    "ds_read_b128 a[12:15], v200 offset:6144\n\t"
    "v_mfma_f32_16x16x32_bf16 v[136:139], a[88:91], a[40:43], v[136:139]\n\t"
    "ds_read_b64_tr_b16 a[60:61], v203 offset:16640\n\t"
    "v_mfma_f32_16x16x32_bf16 v[168:171], a[88:91], a[44:47], v[168:171]\n\t"
    "v_mfma_f32_16x16x32_bf16 v[12:15], a[92:95], a[24:27], v[12:15]\n\t"
    "ds_read_b64_tr_b16 a[62:63], v203 offset:20736\n\t"
    "s_add_u32 s58, s58, 262144\n\t"
    "v_mfma_f32_16x16x32_bf16 v[44:47], a[92:95], a[28:31], v[44:47]\n\t"
    "v_mfma_f32_16x16x32_bf16 v[76:79], a[92:95], a[32:35], v[76:79]\n\t"
    "ds_read_b128 a[16:19], v200 offset:8192\n\t"
    "v_mfma_f32_16x16x32_bf16 v[108:111], a[92:95], a[36:39], v[108:111]\n\t"
    "ds_read_b64_tr_b16 a[64:65], v202 offset:16896\n\t"
    "v_mfma_f32_16x16x32_bf16 v[140:143], a[92:95], a[40:43], v[140:143]\n\t"
    "s_add_u32 m0, s61, 0\n\t"
    "v_mfma_f32_16x16x32_bf16 v[172:175], a[92:95], a[44:47], v[172:175]\n\t"
    "ds_read_b64_tr_b16 a[66:67], v202 offset:20992\n\t"
    "v_mfma_f32_16x16x32_bf16 v[16:19], a[96:99], a[24:27], v[16:19]\n\t"
    "v_mfma_f32_16x16x32_bf16 v[48:51], a[96:99], a[28:31], v[48:51]\n\t"
    "ds_read_b128 a[20:23], v200 offset:10240\n\t"
    "buffer_load_dwordx4 v196, s[40:43], s58 offen offset:0 lds\n\t"
    "v_mfma_f32_16x16x32_bf16 v[80:83], a[96:99], a[32:35], v[80:83]\n\t"
    "ds_read_b64_tr_b16 a[68:69], v203 offset:16896\n\t"
    "v_mfma_f32_16x16x32_bf16 v[112:115], a[96:99], a[36:39], v[112:115]\n\t"
    "v_mfma_f32_16x16x32_bf16 v[144:147], a[96:99], a[40:43], v[144:147]\n\t"
    "ds_read_b64_tr_b16 a[70:71], v203 offset:20992\n\t"
    "v_mfma_f32_16x16x32_bf16 v[176:179], a[96:99], a[44:47], v[176:179]\n\t"
    "buffer_load_dwordx4 v197, s[40:43], s58 offen offset:1024 lds\n\t"
    "v_mfma_f32_16x16x32_bf16 v[20:23], a[100:103], a[24:27], v[20:23]\n\t"
    "ds_read_b64_tr_b16 a[72:73], v202 offset:17152\n\t"
    "v_mfma_f32_16x16x32_bf16 v[52:55], a[100:103], a[28:31], v[52:55]\n\t"
    "ds_read_b64_tr_b16 a[74:75], v202 offset:21248\n\t"
    "v_mfma_f32_16x16x32_bf16 v[84:87], a[100:103], a[32:35], v[84:87]\n\t"
    "v_mfma_f32_16x16x32_bf16 v[116:119], a[100:103], a[36:39], v[116:119]\n\t"
    "ds_read_b64_tr_b16 a[76:77], v203 offset:17152\n\t"
    "buffer_load_dwordx4 v198, s[40:43], s58 offen offset:2048 lds\n\t"
    "v_mfma_f32_16x16x32_bf16 v[148:151], a[100:103], a[40:43], v[148:151]\n\t"
    "v_mfma_f32_16x16x32_bf16 v[180:183], a[100:103], a[44:47], v[180:183]\n\t"
    "ds_read_b64_tr_b16 a[78:79], v203 offset:21248\n\t"
    "v_mfma_f32_16x16x32_bf16 v[24:27], a[104:107], a[24:27], v[24:27]\n\t"
    "buffer_load_dwordx4 v199, s[40:43], s58 offen offset:3072 lds\n\t"
    "v_mfma_f32_16x16x32_bf16 v[56:59], a[104:107], a[28:31], v[56:59]\n\t"
    "v_mfma_f32_16x16x32_bf16 v[88:91], a[104:107], a[32:35], v[88:91]\n\t"
    "v_mfma_f32_16x16x32_bf16 v[120:123], a[104:107], a[36:39], v[120:123]\n\t"
    "v_mfma_f32_16x16x32_bf16 v[152:155], a[104:107], a[40:43], v[152:155]\n\t"
    "s_add_u32 s58, s58, 262144\n\t"
    "v_mfma_f32_16x16x32_bf16 v[184:187], a[104:107], a[44:47], v[184:187]\n\t"
    "v_mfma_f32_16x16x32_bf16 v[28:31], a[108:111], a[24:27], v[28:31]\n\t"
    "v_mfma_f32_16x16x32_bf16 v[60:63], a[108:111], a[28:31], v[60:63]\n\t"
    "v_mfma_f32_16x16x32_bf16 v[92:95], a[108:111], a[32:35], v[92:95]\n\t"
    "s_add_u32 m0, s60, 49152\n\t"
    "v_mfma_f32_16x16x32_bf16 v[124:127], a[108:111], a[36:39], v[124:127]\n\t"
    "v_mfma_f32_16x16x32_bf16 v[156:159], a[108:111], a[40:43], v[156:159]\n\t"
    "v_mfma_f32_16x16x32_bf16 v[188:191], a[108:111], a[44:47], v[188:191]\n\t"
    "s_waitcnt lgkmcnt(0)\n\t"
    "v_mfma_f32_16x16x32_bf16 v[0:3], a[48:51], a[0:3], v[0:3]\n\t"
    "ds_read_b128 a[24:27], v201 offset:0\n\t"
    "buffer_load_dwordx4 v192, s[36:39], s56 offen offset:0 lds\n\t"
    "v_mfma_f32_16x16x32_bf16 v[32:35], a[48:51], a[4:7], v[32:35]\n\t"
    "ds_read_b64_tr_b16 a[80:81], v202 offset:32768\n\t"
    "v_mfma_f32_16x16x32_bf16 v[64:67], a[48:51], a[8:11], v[64:67]\n\t"
    "v_mfma_f32_16x16x32_bf16 v[96:99], a[48:51], a[12:15], v[96:99]\n\t"
    "ds_read_b64_tr_b16 a[82:83], v202 offset:36864\n\t"
    "buffer_load_dwordx4 v193, s[36:39], s56 offen offset:1024 lds\n\t"
    "v_mfma_f32_16x16x32_bf16 v[128:131], a[48:51], a[16:19], v[128:131]\n\t"
    "v_mfma_f32_16x16x32_bf16 v[160:163], a[48:51], a[20:23], v[160:163]\n\t"
    "ds_read_b128 a[28:31], v201 offset:2048\n\t"
    "v_mfma_f32_16x16x32_bf16 v[4:7], a[52:55], a[0:3], v[4:7]\n\t"
    "ds_read_b64_tr_b16 a[84:85], v203 offset:32768\n\t"
    "buffer_load_dwordx4 v194, s[36:39], s56 offen offset:2048 lds\n\t"
    "v_mfma_f32_16x16x32_bf16 v[36:39], a[52:55], a[4:7], v[36:39]\n\t"
    "v_mfma_f32_16x16x32_bf16 v[68:71], a[52:55], a[8:11], v[68:71]\n\t"
    "ds_read_b64_tr_b16 a[86:87], v203 offset:36864\n\t"
    "v_mfma_f32_16x16x32_bf16 v[100:103], a[52:55], a[12:15], v[100:103]\n\t"
    "v_mfma_f32_16x16x32_bf16 v[132:135], a[52:55], a[16:19], v[132:135]\n\t"
    "ds_read_b128 a[32:35], v201 offset:4096\n\t"
    "buffer_load_dwordx4 v195, s[36:39], s56 offen offset:3072 lds\n\t"
    "v_mfma_f32_16x16x32_bf16 v[164:167], a[52:55], a[20:23], v[164:167]\n\t"
    "ds_read_b64_tr_b16 a[88:89], v202 offset:33024\n\t"
    "v_mfma_f32_16x16x32_bf16 v[8:11], a[56:59], a[0:3], v[8:11]\n\t"
    "v_mfma_f32_16x16x32_bf16 v[40:43], a[56:59], a[4:7], v[40:43]\n\t"
    "ds_read_b64_tr_b16 a[90:91], v202 offset:37120\n\t"
    "s_addk_i32 m0, 4096\n\t"
    "v_mfma_f32_16x16x32_bf16 v[72:75], a[56:59], a[8:11], v[72:75]\n\t"
    "v_mfma_f32_16x16x32_bf16 v[104:107], a[56:59], a[12:15], v[104:107]\n\t"
    "ds_read_b128 a[36:39], v201 offset:6144\n\t"
    "v_mfma_f32_16x16x32_bf16 v[136:139], a[56:59], a[16:19], v[136:139]\n\t"
    "ds_read_b64_tr_b16 a[92:93], v203 offset:33024\n\t"
    "v_mfma_f32_16x16x32_bf16 v[168:171], a[56:59], a[20:23], v[168:171]\n\t"
    "buffer_load_dwordx4 v192, s[36:39], s57 offen offset:0 lds\n\t"
    "v_mfma_f32_16x16x32_bf16 v[12:15], a[60:63], a[0:3], v[12:15]\n\t"
    "ds_read_b64_tr_b16 a[94:95], v203 offset:37120\n\t"
    "v_mfma_f32_16x16x32_bf16 v[44:47], a[60:63], a[4:7], v[44:47]\n\t"
    "v_mfma_f32_16x16x32_bf16 v[76:79], a[60:63], a[8:11], v[76:79]\n\t"
    "ds_read_b128 a[40:43], v201 offset:8192\n\t"
    "buffer_load_dwordx4 v193, s[36:39], s57 offen offset:1024 lds\n\t"
    "v_mfma_f32_16x16x32_bf16 v[108:111], a[60:63], a[12:15], v[108:111]\n\t"
    "ds_read_b64_tr_b16 a[96:97], v202 offset:33280\n\t"
    "v_mfma_f32_16x16x32_bf16 v[140:143], a[60:63], a[16:19], v[140:143]\n\t"
    "v_mfma_f32_16x16x32_bf16 v[172:175], a[60:63], a[20:23], v[172:175]\n\t"
    "ds_read_b64_tr_b16 a[98:99], v202 offset:37376\n\t"
    "v_mfma_f32_16x16x32_bf16 v[16:19], a[64:67], a[0:3], v[16:19]\n\t"
    "s_addk_i32 s56, 128\n\t"
    "v_mfma_f32_16x16x32_bf16 v[48:51], a[64:67], a[4:7], v[48:51]\n\t"
    "ds_read_b128 a[44:47], v201 offset:10240\n\t"
    "v_mfma_f32_16x16x32_bf16 v[80:83], a[64:67], a[8:11], v[80:83]\n\t"
    "ds_read_b64_tr_b16 a[100:101], v203 offset:33280\n\t"
    "v_mfma_f32_16x16x32_bf16 v[112:115], a[64:67], a[12:15], v[112:115]\n\t"
    "s_addk_i32 s57, 128\n\t"
    "v_mfma_f32_16x16x32_bf16 v[144:147], a[64:67], a[16:19], v[144:147]\n\t"
    "ds_read_b64_tr_b16 a[102:103], v203 offset:37376\n\t"
    "v_mfma_f32_16x16x32_bf16 v[176:179], a[64:67], a[20:23], v[176:179]\n\t"
    "v_mfma_f32_16x16x32_bf16 v[20:23], a[68:71], a[0:3], v[20:23]\n\t"
    "ds_read_b64_tr_b16 a[104:105], v202 offset:33536\n\t"
    "s_bitset0_b32 s56, 11\n\t"
    "v_mfma_f32_16x16x32_bf16 v[52:55], a[68:71], a[4:7], v[52:55]\n\t"
    "ds_read_b64_tr_b16 a[106:107], v202 offset:37632\n\t"
    "v_mfma_f32_16x16x32_bf16 v[84:87], a[68:71], a[8:11], v[84:87]\n\t"
    "v_mfma_f32_16x16x32_bf16 v[116:119], a[68:71], a[12:15], v[116:119]\n\t"
    "ds_read_b64_tr_b16 a[108:109], v203 offset:33536\n\t"
    "v_mfma_f32_16x16x32_bf16 v[148:151], a[68:71], a[16:19], v[148:151]\n\t"
    "s_bitset0_b32 s57, 11\n\t"
    "v_mfma_f32_16x16x32_bf16 v[180:183], a[68:71], a[20:23], v[180:183]\n\t"
    "ds_read_b64_tr_b16 a[110:111], v203 offset:37632\n\t"
    "v_mfma_f32_16x16x32_bf16 v[24:27], a[72:75], a[0:3], v[24:27]\n\t"
    "v_mfma_f32_16x16x32_bf16 v[56:59], a[72:75], a[4:7], v[56:59]\n\t"
    "buffer_load_dwordx4 a[216:219], v206, s[44:47], s62 offen offset:2048 nt\n\t"
    "v_mfma_f32_16x16x32_bf16 v[88:91], a[72:75], a[8:11], v[88:91]\n\t"
    "v_mfma_f32_16x16x32_bf16 v[120:123], a[72:75], a[12:15], v[120:123]\n\t"
    "v_mfma_f32_16x16x32_bf16 v[152:155], a[72:75], a[16:19], v[152:155]\n\t"
    "v_mfma_f32_16x16x32_bf16 v[184:187], a[72:75], a[20:23], v[184:187]\n\t"
    "buffer_load_dwordx4 a[220:223], v207, s[44:47], s62 offen offset:3072 nt\n\t"
    "v_mfma_f32_16x16x32_bf16 v[28:31], a[76:79], a[0:3], v[28:31]\n\t"
    "v_mfma_f32_16x16x32_bf16 v[60:63], a[76:79], a[4:7], v[60:63]\n\t"
    "v_mfma_f32_16x16x32_bf16 v[92:95], a[76:79], a[8:11], v[92:95]\n\t"
    "buffer_load_dwordx4 a[224:227], v208, s[48:51], s62 offen offset:0 nt\n\t"
    "v_mfma_f32_16x16x32_bf16 v[124:127], a[76:79], a[12:15], v[124:127]\n\t"
    "v_mfma_f32_16x16x32_bf16 v[156:159], a[76:79], a[16:19], v[156:159]\n\t"
    "v_mfma_f32_16x16x32_bf16 v[188:191], a[76:79], a[20:23], v[188:191]\n\t"
    "s_waitcnt vmcnt(13) lgkmcnt(0)\n\t"
    "s_barrier\n\t"
    "v_mfma_f32_16x16x32_bf16 v[0:3], a[80:83], a[24:27], v[0:3]\n\t"
    "ds_read_b128 a[0:3], v200 offset:24576\n\t"
    "s_add_u32 m0, s61, 16384\n\t"
    "v_mfma_f32_16x16x32_bf16 v[32:35], a[80:83], a[28:31], v[32:35]\n\t"
    "ds_read_b64_tr_b16 a[48:49], v202 offset:49152\n\t"
    "v_mfma_f32_16x16x32_bf16 v[64:67], a[80:83], a[32:35], v[64:67]\n\t"
    "v_mfma_f32_16x16x32_bf16 v[96:99], a[80:83], a[36:39], v[96:99]\n\t"
    "ds_read_b64_tr_b16 a[50:51], v202 offset:53248\n\t"
    "v_mfma_f32_16x16x32_bf16 v[128:131], a[80:83], a[40:43], v[128:131]\n\t"
    "v_mfma_f32_16x16x32_bf16 v[160:163], a[80:83], a[44:47], v[160:163]\n\t"
    "ds_read_b128 a[4:7], v200 offset:26624\n\t"
    "v_mfma_f32_16x16x32_bf16 v[4:7], a[84:87], a[24:27], v[4:7]\n\t"
    "ds_read_b64_tr_b16 a[52:53], v203 offset:49152\n\t"
    "v_mfma_f32_16x16x32_bf16 v[36:39], a[84:87], a[28:31], v[36:39]\n\t"
    "v_mfma_f32_16x16x32_bf16 v[68:71], a[84:87], a[32:35], v[68:71]\n\t"
    "ds_read_b64_tr_b16 a[54:55], v203 offset:53248\n\t"
    "v_mfma_f32_16x16x32_bf16 v[100:103], a[84:87], a[36:39], v[100:103]\n\t"
    "buffer_load_dwordx4 v196, s[40:43], s58 offen offset:0 lds\n\t"
    "v_mfma_f32_16x16x32_bf16 v[132:135], a[84:87], a[40:43], v[132:135]\n\t"
    "ds_read_b128 a[8:11], v200 offset:28672\n\t"
    "v_mfma_f32_16x16x32_bf16 v[164:167], a[84:87], a[44:47], v[164:167]\n\t"
    "ds_read_b64_tr_b16 a[56:57], v202 offset:49408\n\t"
    "v_mfma_f32_16x16x32_bf16 v[8:11], a[88:91], a[24:27], v[8:11]\n\t"
    "v_mfma_f32_16x16x32_bf16 v[40:43], a[88:91], a[28:31], v[40:43]\n\t"
    "ds_read_b64_tr_b16 a[58:59], v202 offset:53504\n\t"
    "v_mfma_f32_16x16x32_bf16 v[72:75], a[88:91], a[32:35], v[72:75]\n\t"
    "v_mfma_f32_16x16x32_bf16 v[104:107], a[88:91], a[36:39], v[104:107]\n\t"
    "ds_read_b128 a[12:15], v200 offset:30720\n\t"
    "v_mfma_f32_16x16x32_bf16 v[136:139], a[88:91], a[40:43], v[136:139]\n\t"
    "ds_read_b64_tr_b16 a[60:61], v203 offset:49408\n\t"
    "v_mfma_f32_16x16x32_bf16 v[168:171], a[88:91], a[44:47], v[168:171]\n\t"
    "v_mfma_f32_16x16x32_bf16 v[12:15], a[92:95], a[24:27], v[12:15]\n\t"
    "ds_read_b64_tr_b16 a[62:63], v203 offset:53504\n\t"
    "v_mfma_f32_16x16x32_bf16 v[44:47], a[92:95], a[28:31], v[44:47]\n\t"
    "buffer_load_dwordx4 v197, s[40:43], s58 offen offset:1024 lds\n\t"
    "v_mfma_f32_16x16x32_bf16 v[76:79], a[92:95], a[32:35], v[76:79]\n\t"
    "ds_read_b128 a[16:19], v200 offset:32768\n\t"
    "v_mfma_f32_16x16x32_bf16 v[108:111], a[92:95], a[36:39], v[108:111]\n\t"
    "ds_read_b64_tr_b16 a[64:65], v202 offset:49664\n\t"
    "v_mfma_f32_16x16x32_bf16 v[140:143], a[92:95], a[40:43], v[140:143]\n\t"
    "v_mfma_f32_16x16x32_bf16 v[172:175], a[92:95], a[44:47], v[172:175]\n\t"
    "ds_read_b64_tr_b16 a[66:67], v202 offset:53760\n\t"
    "v_mfma_f32_16x16x32_bf16 v[16:19], a[96:99], a[24:27], v[16:19]\n\t"
    "v_mfma_f32_16x16x32_bf16 v[48:51], a[96:99], a[28:31], v[48:51]\n\t"
    "ds_read_b128 a[20:23], v200 offset:34816\n\t"
    "v_mfma_f32_16x16x32_bf16 v[80:83], a[96:99], a[32:35], v[80:83]\n\t"
    "ds_read_b64_tr_b16 a[68:69], v203 offset:49664\n\t"
    "v_mfma_f32_16x16x32_bf16 v[112:115], a[96:99], a[36:39], v[112:115]\n\t"
    "v_mfma_f32_16x16x32_bf16 v[144:147], a[96:99], a[40:43], v[144:147]\n\t"
    "ds_read_b64_tr_b16 a[70:71], v203 offset:53760\n\t"
    "buffer_load_dwordx4 v198, s[40:43], s58 offen offset:2048 lds\n\t"
    "v_mfma_f32_16x16x32_bf16 v[176:179], a[96:99], a[44:47], v[176:179]\n\t"
    "v_mfma_f32_16x16x32_bf16 v[20:23], a[100:103], a[24:27], v[20:23]\n\t"
    "ds_read_b64_tr_b16 a[72:73], v202 offset:49920\n\t"
    "v_mfma_f32_16x16x32_bf16 v[52:55], a[100:103], a[28:31], v[52:55]\n\t"
    "ds_read_b64_tr_b16 a[74:75], v202 offset:54016\n\t"
    "v_mfma_f32_16x16x32_bf16 v[84:87], a[100:103], a[32:35], v[84:87]\n\t"
    "v_mfma_f32_16x16x32_bf16 v[116:119], a[100:103], a[36:39], v[116:119]\n\t"
    "ds_read_b64_tr_b16 a[76:77], v203 offset:49920\n\t"
    "v_mfma_f32_16x16x32_bf16 v[148:151], a[100:103], a[40:43], v[148:151]\n\t"
    "v_mfma_f32_16x16x32_bf16 v[180:183], a[100:103], a[44:47], v[180:183]\n\t"
    "ds_read_b64_tr_b16 a[78:79], v203 offset:54016\n\t"
    "v_mfma_f32_16x16x32_bf16 v[24:27], a[104:107], a[24:27], v[24:27]\n\t"
    "v_mfma_f32_16x16x32_bf16 v[56:59], a[104:107], a[28:31], v[56:59]\n\t"
    "v_mfma_f32_16x16x32_bf16 v[88:91], a[104:107], a[32:35], v[88:91]\n\t"
    "buffer_load_dwordx4 v199, s[40:43], s58 offen offset:3072 lds\n\t"
    "v_mfma_f32_16x16x32_bf16 v[120:123], a[104:107], a[36:39], v[120:123]\n\t"
    "v_mfma_f32_16x16x32_bf16 v[152:155], a[104:107], a[40:43], v[152:155]\n\t"
    "v_mfma_f32_16x16x32_bf16 v[184:187], a[104:107], a[44:47], v[184:187]\n\t"
    "v_mfma_f32_16x16x32_bf16 v[28:31], a[108:111], a[24:27], v[28:31]\n\t"
    "v_mfma_f32_16x16x32_bf16 v[60:63], a[108:111], a[28:31], v[60:63]\n\t"
    "v_mfma_f32_16x16x32_bf16 v[92:95], a[108:111], a[32:35], v[92:95]\n\t"
    "v_mfma_f32_16x16x32_bf16 v[124:127], a[108:111], a[36:39], v[124:127]\n\t"
    "v_mfma_f32_16x16x32_bf16 v[156:159], a[108:111], a[40:43], v[156:159]\n\t"
    "v_mfma_f32_16x16x32_bf16 v[188:191], a[108:111], a[44:47], v[188:191]\n\t"
    "s_waitcnt lgkmcnt(0)\n\t"
    "v_mfma_f32_16x16x32_bf16 v[0:3], a[48:51], a[0:3], v[0:3]\n\t"
    "ds_read_b128 a[24:27], v201 offset:24576\n\t"
    "s_add_u32 s58, s58, 262144\n\t"
    "v_mfma_f32_16x16x32_bf16 v[32:35], a[48:51], a[4:7], v[32:35]\n\t"
    "ds_read_b64_tr_b16 a[80:81], v248 offset:0\n\t"
    "v_mfma_f32_16x16x32_bf16 v[64:67], a[48:51], a[8:11], v[64:67]\n\t"
    "v_mfma_f32_16x16x32_bf16 v[96:99], a[48:51], a[12:15], v[96:99]\n\t"
    "ds_read_b64_tr_b16 a[82:83], v248 offset:4096\n\t"
    "v_mfma_f32_16x16x32_bf16 v[128:131], a[48:51], a[16:19], v[128:131]\n\t"
    "v_mfma_f32_16x16x32_bf16 v[160:163], a[48:51], a[20:23], v[160:163]\n\t"
    "ds_read_b128 a[28:31], v201 offset:26624\n\t"
    "v_mfma_f32_16x16x32_bf16 v[4:7], a[52:55], a[0:3], v[4:7]\n\t"
    "ds_read_b64_tr_b16 a[84:85], v249 offset:0\n\t"
    "v_mfma_f32_16x16x32_bf16 v[36:39], a[52:55], a[4:7], v[36:39]\n\t"
    "v_mfma_f32_16x16x32_bf16 v[68:71], a[52:55], a[8:11], v[68:71]\n\t"
    "ds_read_b64_tr_b16 a[86:87], v249 offset:4096\n\t"
    "v_mfma_f32_16x16x32_bf16 v[100:103], a[52:55], a[12:15], v[100:103]\n\t"
    "s_and_b32 s58, s58, 8388607\n\t"
    "v_mfma_f32_16x16x32_bf16 v[132:135], a[52:55], a[16:19], v[132:135]\n\t"
    "ds_read_b128 a[32:35], v201 offset:28672\n\t"
    "v_mfma_f32_16x16x32_bf16 v[164:167], a[52:55], a[20:23], v[164:167]\n\t"
    "ds_read_b64_tr_b16 a[88:89], v248 offset:256\n\t"
    "v_mfma_f32_16x16x32_bf16 v[8:11], a[56:59], a[0:3], v[8:11]\n\t"
    "v_mfma_f32_16x16x32_bf16 v[40:43], a[56:59], a[4:7], v[40:43]\n\t"
    "ds_read_b64_tr_b16 a[90:91], v248 offset:4352\n\t"
    "v_mfma_f32_16x16x32_bf16 v[72:75], a[56:59], a[8:11], v[72:75]\n\t"
    "v_mfma_f32_16x16x32_bf16 v[104:107], a[56:59], a[12:15], v[104:107]\n\t"
    "ds_read_b128 a[36:39], v201 offset:30720\n\t"
    "v_mfma_f32_16x16x32_bf16 v[136:139], a[56:59], a[16:19], v[136:139]\n\t"
    "ds_read_b64_tr_b16 a[92:93], v249 offset:256\n\t"
    "v_mfma_f32_16x16x32_bf16 v[168:171], a[56:59], a[20:23], v[168:171]\n\t"
    "v_mfma_f32_16x16x32_bf16 v[12:15], a[60:63], a[0:3], v[12:15]\n\t"
    "ds_read_b64_tr_b16 a[94:95], v249 offset:4352\n\t"
    "v_mfma_f32_16x16x32_bf16 v[44:47], a[60:63], a[4:7], v[44:47]\n\t"
    "buffer_load_dwordx4 a[228:231], v209, s[48:51], s62 offen offset:1024 nt\n\t"
    "v_mfma_f32_16x16x32_bf16 v[76:79], a[60:63], a[8:11], v[76:79]\n\t"
    "ds_read_b128 a[40:43], v201 offset:32768\n\t"
    "v_mfma_f32_16x16x32_bf16 v[108:111], a[60:63], a[12:15], v[108:111]\n\t"
    "ds_read_b64_tr_b16 a[96:97], v248 offset:512\n\t"
    "v_mfma_f32_16x16x32_bf16 v[140:143], a[60:63], a[16:19], v[140:143]\n\t"
    "v_mfma_f32_16x16x32_bf16 v[172:175], a[60:63], a[20:23], v[172:175]\n\t"
    "ds_read_b64_tr_b16 a[98:99], v248 offset:4608\n\t"
    "v_mfma_f32_16x16x32_bf16 v[16:19], a[64:67], a[0:3], v[16:19]\n\t"
    "v_mfma_f32_16x16x32_bf16 v[48:51], a[64:67], a[4:7], v[48:51]\n\t"
    "ds_read_b128 a[44:47], v201 offset:34816\n\t"
    "v_mfma_f32_16x16x32_bf16 v[80:83], a[64:67], a[8:11], v[80:83]\n\t"
    "ds_read_b64_tr_b16 a[100:101], v249 offset:512\n\t"
    "v_mfma_f32_16x16x32_bf16 v[112:115], a[64:67], a[12:15], v[112:115]\n\t"
    "v_mfma_f32_16x16x32_bf16 v[144:147], a[64:67], a[16:19], v[144:147]\n\t"
    "ds_read_b64_tr_b16 a[102:103], v249 offset:4608\n\t"
    "buffer_load_dwordx4 a[232:235], v221, s[48:51], s62 offen offset:2048 nt\n\t"
    "v_mfma_f32_16x16x32_bf16 v[176:179], a[64:67], a[20:23], v[176:179]\n\t"
    "v_mfma_f32_16x16x32_bf16 v[20:23], a[68:71], a[0:3], v[20:23]\n\t"
    "ds_read_b64_tr_b16 a[104:105], v248 offset:768\n\t"
    "v_mfma_f32_16x16x32_bf16 v[52:55], a[68:71], a[4:7], v[52:55]\n\t"
    "ds_read_b64_tr_b16 a[106:107], v248 offset:4864\n\t"
    "v_mfma_f32_16x16x32_bf16 v[84:87], a[68:71], a[8:11], v[84:87]\n\t"
    "v_mfma_f32_16x16x32_bf16 v[116:119], a[68:71], a[12:15], v[116:119]\n\t"
    "ds_read_b64_tr_b16 a[108:109], v249 offset:768\n\t"
    "v_mfma_f32_16x16x32_bf16 v[148:151], a[68:71], a[16:19], v[148:151]\n\t"
    "v_mfma_f32_16x16x32_bf16 v[180:183], a[68:71], a[20:23], v[180:183]\n\t"
    "ds_read_b64_tr_b16 a[110:111], v249 offset:4864\n\t"
    "v_mfma_f32_16x16x32_bf16 v[24:27], a[72:75], a[0:3], v[24:27]\n\t"
    "v_mfma_f32_16x16x32_bf16 v[56:59], a[72:75], a[4:7], v[56:59]\n\t"
    "v_mfma_f32_16x16x32_bf16 v[88:91], a[72:75], a[8:11], v[88:91]\n\t"
    "buffer_load_dwordx4 a[236:239], v246, s[48:51], s62 offen offset:3072 nt\n\t"
    "s_add_u32 s62, s62, 8192\n\t"
    "v_mfma_f32_16x16x32_bf16 v[120:123], a[72:75], a[12:15], v[120:123]\n\t"
    "v_mfma_f32_16x16x32_bf16 v[152:155], a[72:75], a[16:19], v[152:155]\n\t"
    "v_mfma_f32_16x16x32_bf16 v[184:187], a[72:75], a[20:23], v[184:187]\n\t"
    "v_mfma_f32_16x16x32_bf16 v[28:31], a[76:79], a[0:3], v[28:31]\n\t"
    "v_mfma_f32_16x16x32_bf16 v[60:63], a[76:79], a[4:7], v[60:63]\n\t"
    "v_mfma_f32_16x16x32_bf16 v[92:95], a[76:79], a[8:11], v[92:95]\n\t"
    "v_mfma_f32_16x16x32_bf16 v[124:127], a[76:79], a[12:15], v[124:127]\n\t"
    "v_mfma_f32_16x16x32_bf16 v[156:159], a[76:79], a[16:19], v[156:159]\n\t"
    "v_mfma_f32_16x16x32_bf16 v[188:191], a[76:79], a[20:23], v[188:191]\n\t"
    "s_waitcnt vmcnt(3) lgkmcnt(0)\n\t"
    "s_barrier\n\t"
    "v_mfma_f32_16x16x32_bf16 v[0:3], a[80:83], a[24:27], v[0:3]\n\t"
    "ds_read_b128 a[0:3], v200 offset:49152\n\t"
    "ds_write_b128 v254, a[112:115] offset:32768\n\t"
    "v_mfma_f32_16x16x32_bf16 v[32:35], a[80:83], a[28:31], v[32:35]\n\t"
    "ds_read_b64_tr_b16 a[48:49], v202 offset:0\n\t"
    "v_mfma_f32_16x16x32_bf16 v[64:67], a[80:83], a[32:35], v[64:67]\n\t"
    "v_mfma_f32_16x16x32_bf16 v[96:99], a[80:83], a[36:39], v[96:99]\n\t"
    "ds_read_b64_tr_b16 a[50:51], v202 offset:4096\n\t"
    "v_mfma_f32_16x16x32_bf16 v[128:131], a[80:83], a[40:43], v[128:131]\n\t"
    "ds_write_b128 v254, a[116:119] offset:33792\n\t"
    "v_mfma_f32_16x16x32_bf16 v[160:163], a[80:83], a[44:47], v[160:163]\n\t"
    "ds_read_b128 a[4:7], v200 offset:51200\n\t"
    "v_mfma_f32_16x16x32_bf16 v[4:7], a[84:87], a[24:27], v[4:7]\n\t"
    "ds_read_b64_tr_b16 a[52:53], v203 offset:0\n\t"
    "v_mfma_f32_16x16x32_bf16 v[36:39], a[84:87], a[28:31], v[36:39]\n\t"
    "v_mfma_f32_16x16x32_bf16 v[68:71], a[84:87], a[32:35], v[68:71]\n\t"
    "ds_read_b64_tr_b16 a[54:55], v203 offset:4096\n\t"
    "ds_write_b128 v254, a[120:123] offset:34816\n\t"
    "v_mfma_f32_16x16x32_bf16 v[100:103], a[84:87], a[36:39], v[100:103]\n\t"
    "v_mfma_f32_16x16x32_bf16 v[132:135], a[84:87], a[40:43], v[132:135]\n\t"
    "ds_read_b128 a[8:11], v200 offset:53248\n\t"
    "v_mfma_f32_16x16x32_bf16 v[164:167], a[84:87], a[44:47], v[164:167]\n\t"
    "ds_read_b64_tr_b16 a[56:57], v202 offset:256\n\t"
    "v_mfma_f32_16x16x32_bf16 v[8:11], a[88:91], a[24:27], v[8:11]\n\t"
    "ds_write_b128 v254, a[124:127] offset:35840\n\t"
    "v_mfma_f32_16x16x32_bf16 v[40:43], a[88:91], a[28:31], v[40:43]\n\t"
    "ds_read_b64_tr_b16 a[58:59], v202 offset:4352\n\t"
    "v_mfma_f32_16x16x32_bf16 v[72:75], a[88:91], a[32:35], v[72:75]\n\t"
    "v_mfma_f32_16x16x32_bf16 v[104:107], a[88:91], a[36:39], v[104:107]\n\t"
    "ds_read_b128 a[12:15], v200 offset:55296\n\t"
    "v_mfma_f32_16x16x32_bf16 v[136:139], a[88:91], a[40:43], v[136:139]\n\t"
    "ds_read_b64_tr_b16 a[60:61], v203 offset:256\n\t"
    "ds_write_b128 v254, a[128:131] offset:40960\n\t"
    "v_mfma_f32_16x16x32_bf16 v[168:171], a[88:91], a[44:47], v[168:171]\n\t"
    "v_mfma_f32_16x16x32_bf16 v[12:15], a[92:95], a[24:27], v[12:15]\n\t"
    "ds_read_b64_tr_b16 a[62:63], v203 offset:4352\n\t"
    "v_mfma_f32_16x16x32_bf16 v[44:47], a[92:95], a[28:31], v[44:47]\n\t"
    "v_mfma_f32_16x16x32_bf16 v[76:79], a[92:95], a[32:35], v[76:79]\n\t"
    "ds_read_b128 a[16:19], v200 offset:57344\n\t"
    "ds_write_b128 v254, a[132:135] offset:41984\n\t"
    "v_mfma_f32_16x16x32_bf16 v[108:111], a[92:95], a[36:39], v[108:111]\n\t"
    "ds_read_b64_tr_b16 a[64:65], v202 offset:512\n\t"
    "v_mfma_f32_16x16x32_bf16 v[140:143], a[92:95], a[40:43], v[140:143]\n\t"
    "v_mfma_f32_16x16x32_bf16 v[172:175], a[92:95], a[44:47], v[172:175]\n\t"
    "ds_read_b64_tr_b16 a[66:67], v202 offset:4608\n\t"
    "v_mfma_f32_16x16x32_bf16 v[16:19], a[96:99], a[24:27], v[16:19]\n\t"
    "ds_write_b128 v254, a[136:139] offset:43008\n\t"
    "v_mfma_f32_16x16x32_bf16 v[48:51], a[96:99], a[28:31], v[48:51]\n\t"
    "ds_read_b128 a[20:23], v200 offset:59392\n\t"
    "v_mfma_f32_16x16x32_bf16 v[80:83], a[96:99], a[32:35], v[80:83]\n\t"
    "ds_read_b64_tr_b16 a[68:69], v203 offset:512\n\t"
    "v_mfma_f32_16x16x32_bf16 v[112:115], a[96:99], a[36:39], v[112:115]\n\t"
    "v_mfma_f32_16x16x32_bf16 v[144:147], a[96:99], a[40:43], v[144:147]\n\t"
    "ds_read_b64_tr_b16 a[70:71], v203 offset:4608\n\t"
    "ds_write_b128 v254, a[140:143] offset:44032\n\t"
    "v_mfma_f32_16x16x32_bf16 v[176:179], a[96:99], a[44:47], v[176:179]\n\t"
    "v_mfma_f32_16x16x32_bf16 v[20:23], a[100:103], a[24:27], v[20:23]\n\t"
    "ds_read_b64_tr_b16 a[72:73], v202 offset:768\n\t"
    "v_mfma_f32_16x16x32_bf16 v[52:55], a[100:103], a[28:31], v[52:55]\n\t"
    "ds_read_b64_tr_b16 a[74:75], v202 offset:4864\n\t"
    "v_mfma_f32_16x16x32_bf16 v[84:87], a[100:103], a[32:35], v[84:87]\n\t"
    "ds_write_b128 v255, a[144:147] offset:0\n\t"
    "v_mfma_f32_16x16x32_bf16 v[116:119], a[100:103], a[36:39], v[116:119]\n\t"
    "ds_read_b64_tr_b16 a[76:77], v203 offset:768\n\t"
    "v_mfma_f32_16x16x32_bf16 v[148:151], a[100:103], a[40:43], v[148:151]\n\t"
    "v_mfma_f32_16x16x32_bf16 v[180:183], a[100:103], a[44:47], v[180:183]\n\t"
    "ds_read_b64_tr_b16 a[78:79], v203 offset:4864\n\t"
    "v_mfma_f32_16x16x32_bf16 v[24:27], a[104:107], a[24:27], v[24:27]\n\t"
    "ds_write_b128 v255, a[148:151] offset:1024\n\t"
    "v_mfma_f32_16x16x32_bf16 v[56:59], a[104:107], a[28:31], v[56:59]\n\t"
    "v_mfma_f32_16x16x32_bf16 v[88:91], a[104:107], a[32:35], v[88:91]\n\t"
    "v_mfma_f32_16x16x32_bf16 v[120:123], a[104:107], a[36:39], v[120:123]\n\t"
    "v_mfma_f32_16x16x32_bf16 v[152:155], a[104:107], a[40:43], v[152:155]\n\t"
    "ds_write_b128 v255, a[152:155] offset:2048\n\t"
    "v_mfma_f32_16x16x32_bf16 v[184:187], a[104:107], a[44:47], v[184:187]\n\t"
    "v_mfma_f32_16x16x32_bf16 v[28:31], a[108:111], a[24:27], v[28:31]\n\t"
    "v_mfma_f32_16x16x32_bf16 v[60:63], a[108:111], a[28:31], v[60:63]\n\t"
    "v_mfma_f32_16x16x32_bf16 v[92:95], a[108:111], a[32:35], v[92:95]\n\t"
    "ds_write_b128 v255, a[156:159] offset:3072\n\t"
    "v_mfma_f32_16x16x32_bf16 v[124:127], a[108:111], a[36:39], v[124:127]\n\t"
    "v_mfma_f32_16x16x32_bf16 v[156:159], a[108:111], a[40:43], v[156:159]\n\t"
    "v_mfma_f32_16x16x32_bf16 v[188:191], a[108:111], a[44:47], v[188:191]\n\t"
    "s_waitcnt lgkmcnt(0)\n\t"
    "v_mfma_f32_16x16x32_bf16 v[0:3], a[48:51], a[0:3], v[0:3]\n\t"
    "ds_read_b128 a[24:27], v201 offset:49152\n\t"
    "ds_write_b128 v255, a[160:163] offset:8192\n\t"
    "v_mfma_f32_16x16x32_bf16 v[32:35], a[48:51], a[4:7], v[32:35]\n\t"
    "ds_read_b64_tr_b16 a[80:81], v202 offset:16384\n\t"
    "v_mfma_f32_16x16x32_bf16 v[64:67], a[48:51], a[8:11], v[64:67]\n\t"
    "v_mfma_f32_16x16x32_bf16 v[96:99], a[48:51], a[12:15], v[96:99]\n\t"
    "ds_read_b64_tr_b16 a[82:83], v202 offset:20480\n\t"
    "v_mfma_f32_16x16x32_bf16 v[128:131], a[48:51], a[16:19], v[128:131]\n\t"
    "ds_write_b128 v255, a[164:167] offset:9216\n\t"
    "v_mfma_f32_16x16x32_bf16 v[160:163], a[48:51], a[20:23], v[160:163]\n\t"
    "ds_read_b128 a[28:31], v201 offset:51200\n\t"
    "v_mfma_f32_16x16x32_bf16 v[4:7], a[52:55], a[0:3], v[4:7]\n\t"
    "ds_read_b64_tr_b16 a[84:85], v203 offset:16384\n\t"
    "v_mfma_f32_16x16x32_bf16 v[36:39], a[52:55], a[4:7], v[36:39]\n\t"
    "v_mfma_f32_16x16x32_bf16 v[68:71], a[52:55], a[8:11], v[68:71]\n\t"
    "ds_read_b64_tr_b16 a[86:87], v203 offset:20480\n\t"
    "ds_write_b128 v255, a[168:171] offset:10240\n\t"
    "v_mfma_f32_16x16x32_bf16 v[100:103], a[52:55], a[12:15], v[100:103]\n\t"
    "v_mfma_f32_16x16x32_bf16 v[132:135], a[52:55], a[16:19], v[132:135]\n\t"
    "ds_read_b128 a[32:35], v201 offset:53248\n\t"
    "v_mfma_f32_16x16x32_bf16 v[164:167], a[52:55], a[20:23], v[164:167]\n\t"
    "ds_read_b64_tr_b16 a[88:89], v202 offset:16640\n\t"
    "v_mfma_f32_16x16x32_bf16 v[8:11], a[56:59], a[0:3], v[8:11]\n\t"
    "ds_write_b128 v255, a[172:175] offset:11264\n\t"
    "v_mfma_f32_16x16x32_bf16 v[40:43], a[56:59], a[4:7], v[40:43]\n\t"
    "ds_read_b64_tr_b16 a[90:91], v202 offset:20736\n\t"
    "v_mfma_f32_16x16x32_bf16 v[72:75], a[56:59], a[8:11], v[72:75]\n\t"
    "v_mfma_f32_16x16x32_bf16 v[104:107], a[56:59], a[12:15], v[104:107]\n\t"
    "ds_read_b128 a[36:39], v201 offset:55296\n\t"
    "v_mfma_f32_16x16x32_bf16 v[136:139], a[56:59], a[16:19], v[136:139]\n\t"
    "ds_read_b64_tr_b16 a[92:93], v203 offset:16640\n\t"
    "ds_write_b128 v255, a[176:179] offset:32768\n\t"
    "v_mfma_f32_16x16x32_bf16 v[168:171], a[56:59], a[20:23], v[168:171]\n\t"
    "v_mfma_f32_16x16x32_bf16 v[12:15], a[60:63], a[0:3], v[12:15]\n\t"
    "ds_read_b64_tr_b16 a[94:95], v203 offset:20736\n\t"
    "v_mfma_f32_16x16x32_bf16 v[44:47], a[60:63], a[4:7], v[44:47]\n\t"
    "v_mfma_f32_16x16x32_bf16 v[76:79], a[60:63], a[8:11], v[76:79]\n\t"
    "ds_read_b128 a[40:43], v201 offset:57344\n\t"
    "ds_write_b128 v255, a[180:183] offset:33792\n\t"
    "v_mfma_f32_16x16x32_bf16 v[108:111], a[60:63], a[12:15], v[108:111]\n\t"
    "ds_read_b64_tr_b16 a[96:97], v202 offset:16896\n\t"
    "v_mfma_f32_16x16x32_bf16 v[140:143], a[60:63], a[16:19], v[140:143]\n\t"
    "v_mfma_f32_16x16x32_bf16 v[172:175], a[60:63], a[20:23], v[172:175]\n\t"
    "ds_read_b64_tr_b16 a[98:99], v202 offset:20992\n\t"
    "v_mfma_f32_16x16x32_bf16 v[16:19], a[64:67], a[0:3], v[16:19]\n\t"
    "ds_write_b128 v255, a[184:187] offset:34816\n\t"
    "v_mfma_f32_16x16x32_bf16 v[48:51], a[64:67], a[4:7], v[48:51]\n\t"
    "ds_read_b128 a[44:47], v201 offset:59392\n\t"
    "v_mfma_f32_16x16x32_bf16 v[80:83], a[64:67], a[8:11], v[80:83]\n\t"
    "ds_read_b64_tr_b16 a[100:101], v203 offset:16896\n\t"
    "v_mfma_f32_16x16x32_bf16 v[112:115], a[64:67], a[12:15], v[112:115]\n\t"
    "v_mfma_f32_16x16x32_bf16 v[144:147], a[64:67], a[16:19], v[144:147]\n\t"
    "ds_read_b64_tr_b16 a[102:103], v203 offset:20992\n\t"
    "ds_write_b128 v255, a[188:191] offset:35840\n\t"
    "v_mfma_f32_16x16x32_bf16 v[176:179], a[64:67], a[20:23], v[176:179]\n\t"
    "v_mfma_f32_16x16x32_bf16 v[20:23], a[68:71], a[0:3], v[20:23]\n\t"
    "ds_read_b64_tr_b16 a[104:105], v202 offset:17152\n\t"
    "v_mfma_f32_16x16x32_bf16 v[52:55], a[68:71], a[4:7], v[52:55]\n\t"
    "ds_read_b64_tr_b16 a[106:107], v202 offset:21248\n\t"
    "v_mfma_f32_16x16x32_bf16 v[84:87], a[68:71], a[8:11], v[84:87]\n\t"
    "ds_write_b128 v255, a[192:195] offset:40960\n\t"
    "v_mfma_f32_16x16x32_bf16 v[116:119], a[68:71], a[12:15], v[116:119]\n\t"
    "ds_read_b64_tr_b16 a[108:109], v203 offset:17152\n\t"
    "v_mfma_f32_16x16x32_bf16 v[148:151], a[68:71], a[16:19], v[148:151]\n\t"
    "v_mfma_f32_16x16x32_bf16 v[180:183], a[68:71], a[20:23], v[180:183]\n\t"
    "ds_read_b64_tr_b16 a[110:111], v203 offset:21248\n\t"
    "v_mfma_f32_16x16x32_bf16 v[24:27], a[72:75], a[0:3], v[24:27]\n\t"
    "ds_write_b128 v255, a[196:199] offset:41984\n\t"
    "v_mfma_f32_16x16x32_bf16 v[56:59], a[72:75], a[4:7], v[56:59]\n\t"
    "v_mfma_f32_16x16x32_bf16 v[88:91], a[72:75], a[8:11], v[88:91]\n\t"
    "v_mfma_f32_16x16x32_bf16 v[120:123], a[72:75], a[12:15], v[120:123]\n\t"
    "v_mfma_f32_16x16x32_bf16 v[152:155], a[72:75], a[16:19], v[152:155]\n\t"
    "ds_write_b128 v255, a[200:203] offset:43008\n\t"
    "v_mfma_f32_16x16x32_bf16 v[184:187], a[72:75], a[20:23], v[184:187]\n\t"
    "v_mfma_f32_16x16x32_bf16 v[28:31], a[76:79], a[0:3], v[28:31]\n\t"
    "v_mfma_f32_16x16x32_bf16 v[60:63], a[76:79], a[4:7], v[60:63]\n\t"
    "v_mfma_f32_16x16x32_bf16 v[92:95], a[76:79], a[8:11], v[92:95]\n\t"
    "ds_write_b128 v255, a[204:207] offset:44032\n\t"
    "v_mfma_f32_16x16x32_bf16 v[124:127], a[76:79], a[12:15], v[124:127]\n\t"
    "v_mfma_f32_16x16x32_bf16 v[156:159], a[76:79], a[16:19], v[156:159]\n\t"
    "v_mfma_f32_16x16x32_bf16 v[188:191], a[76:79], a[20:23], v[188:191]\n\t"
    "s_waitcnt lgkmcnt(0)\n\t"
    "s_barrier\n\t"
    "v_mfma_f32_16x16x32_bf16 v[0:3], a[80:83], a[24:27], v[0:3]\n\t"
    "ds_write_b128 v231, a[208:211] offset:0\n\t"
    "v_mfma_f32_16x16x32_bf16 v[32:35], a[80:83], a[28:31], v[32:35]\n\t"
    "v_mfma_f32_16x16x32_bf16 v[64:67], a[80:83], a[32:35], v[64:67]\n\t"
    "v_mfma_f32_16x16x32_bf16 v[96:99], a[80:83], a[36:39], v[96:99]\n\t"
    "v_mfma_f32_16x16x32_bf16 v[128:131], a[80:83], a[40:43], v[128:131]\n\t"
    "ds_write_b128 v231, a[212:215] offset:1024\n\t"
    "v_mfma_f32_16x16x32_bf16 v[160:163], a[80:83], a[44:47], v[160:163]\n\t"
    "v_mfma_f32_16x16x32_bf16 v[4:7], a[84:87], a[24:27], v[4:7]\n\t"
    "v_mfma_f32_16x16x32_bf16 v[36:39], a[84:87], a[28:31], v[36:39]\n\t"
    "v_mfma_f32_16x16x32_bf16 v[68:71], a[84:87], a[32:35], v[68:71]\n\t"
    "ds_write_b128 v231, a[216:219] offset:2048\n\t"
    "v_mfma_f32_16x16x32_bf16 v[100:103], a[84:87], a[36:39], v[100:103]\n\t"
    "v_mfma_f32_16x16x32_bf16 v[132:135], a[84:87], a[40:43], v[132:135]\n\t"
    "v_mfma_f32_16x16x32_bf16 v[164:167], a[84:87], a[44:47], v[164:167]\n\t"
    "v_mfma_f32_16x16x32_bf16 v[8:11], a[88:91], a[24:27], v[8:11]\n\t"
    "v_mfma_f32_16x16x32_bf16 v[40:43], a[88:91], a[28:31], v[40:43]\n\t"
    "ds_write_b128 v231, a[220:223] offset:3072\n\t"
    "v_mfma_f32_16x16x32_bf16 v[72:75], a[88:91], a[32:35], v[72:75]\n\t"
    "v_mfma_f32_16x16x32_bf16 v[104:107], a[88:91], a[36:39], v[104:107]\n\t"
    "v_mfma_f32_16x16x32_bf16 v[136:139], a[88:91], a[40:43], v[136:139]\n\t"
    "v_mfma_f32_16x16x32_bf16 v[168:171], a[88:91], a[44:47], v[168:171]\n\t"
    "ds_write_b128 v231, a[224:227] offset:8192\n\t"
    "v_mfma_f32_16x16x32_bf16 v[12:15], a[92:95], a[24:27], v[12:15]\n\t"
    "v_mfma_f32_16x16x32_bf16 v[44:47], a[92:95], a[28:31], v[44:47]\n\t"
    "v_mfma_f32_16x16x32_bf16 v[76:79], a[92:95], a[32:35], v[76:79]\n\t"
    "v_mfma_f32_16x16x32_bf16 v[108:111], a[92:95], a[36:39], v[108:111]\n\t"
    "s_waitcnt vmcnt(2)\n\t"
    "ds_write_b128 v231, a[228:231] offset:9216\n\t"
    "v_mfma_f32_16x16x32_bf16 v[140:143], a[92:95], a[40:43], v[140:143]\n\t"
    "v_mfma_f32_16x16x32_bf16 v[172:175], a[92:95], a[44:47], v[172:175]\n\t"
    "v_mfma_f32_16x16x32_bf16 v[16:19], a[96:99], a[24:27], v[16:19]\n\t"
    "v_mfma_f32_16x16x32_bf16 v[48:51], a[96:99], a[28:31], v[48:51]\n\t"
    "v_mfma_f32_16x16x32_bf16 v[80:83], a[96:99], a[32:35], v[80:83]\n\t"
    "s_waitcnt vmcnt(1)\n\t"
    "ds_write_b128 v231, a[232:235] offset:10240\n\t"
    "v_mfma_f32_16x16x32_bf16 v[112:115], a[96:99], a[36:39], v[112:115]\n\t"
    "v_mfma_f32_16x16x32_bf16 v[144:147], a[96:99], a[40:43], v[144:147]\n\t"
    "v_mfma_f32_16x16x32_bf16 v[176:179], a[96:99], a[44:47], v[176:179]\n\t"
    "v_mfma_f32_16x16x32_bf16 v[20:23], a[100:103], a[24:27], v[20:23]\n\t"
    "s_waitcnt vmcnt(0)\n\t"
    "ds_write_b128 v231, a[236:239] offset:11264\n\t"
    "v_mfma_f32_16x16x32_bf16 v[52:55], a[100:103], a[28:31], v[52:55]\n\t"
    "v_mfma_f32_16x16x32_bf16 v[84:87], a[100:103], a[32:35], v[84:87]\n\t"
    "v_mfma_f32_16x16x32_bf16 v[116:119], a[100:103], a[36:39], v[116:119]\n\t"
    "v_mfma_f32_16x16x32_bf16 v[148:151], a[100:103], a[40:43], v[148:151]\n\t"
    "s_add_u32 m0, s63, 0\n\t"
    "v_mfma_f32_16x16x32_bf16 v[180:183], a[100:103], a[44:47], v[180:183]\n\t"
    "v_mfma_f32_16x16x32_bf16 v[24:27], a[104:107], a[24:27], v[24:27]\n\t"
    "v_mfma_f32_16x16x32_bf16 v[56:59], a[104:107], a[28:31], v[56:59]\n\t"
    "v_mfma_f32_16x16x32_bf16 v[88:91], a[104:107], a[32:35], v[88:91]\n\t"
    "v_mfma_f32_16x16x32_bf16 v[120:123], a[104:107], a[36:39], v[120:123]\n\t"
    "buffer_load_dwordx4 v204, s[44:47], s62 offen offset:0 nt lds\n\t"
    "v_mfma_f32_16x16x32_bf16 v[152:155], a[104:107], a[40:43], v[152:155]\n\t"
    "v_mfma_f32_16x16x32_bf16 v[184:187], a[104:107], a[44:47], v[184:187]\n\t"
    "v_mfma_f32_16x16x32_bf16 v[28:31], a[108:111], a[24:27], v[28:31]\n\t"
    "v_mfma_f32_16x16x32_bf16 v[60:63], a[108:111], a[28:31], v[60:63]\n\t"
    "buffer_load_dwordx4 v205, s[44:47], s62 offen offset:1024 nt lds\n\t"
    "v_mfma_f32_16x16x32_bf16 v[92:95], a[108:111], a[32:35], v[92:95]\n\t"
    "v_mfma_f32_16x16x32_bf16 v[124:127], a[108:111], a[36:39], v[124:127]\n\t"
    "v_mfma_f32_16x16x32_bf16 v[156:159], a[108:111], a[40:43], v[156:159]\n\t"
    "v_mfma_f32_16x16x32_bf16 v[188:191], a[108:111], a[44:47], v[188:191]\n\t"
    "v_pk_mul_f32 v[0:1], v[0:1], s[66:67]\n\t"
    "v_pk_mul_f32 v[2:3], v[2:3], s[66:67]\n\t"
    "v_pk_mul_f32 v[4:5], v[4:5], s[66:67]\n\t"
    "v_pk_mul_f32 v[6:7], v[6:7], s[66:67]\n\t"
    "v_cvt_pk_bf16_f32 v0, v0, v1\n\t"
    "v_cvt_pk_bf16_f32 v1, v2, v3\n\t"
    "v_cvt_pk_bf16_f32 v2, v4, v5\n\t"
    "v_cvt_pk_bf16_f32 v3, v6, v7\n\t"
    "v_accvgpr_write_b32 a160, 0\n\t"
    "v_accvgpr_write_b32 a161, 0\n\t"
    "buffer_load_dwordx4 v206, s[44:47], s62 offen offset:2048 nt lds\n\t"
    "v_accvgpr_write_b32 a162, 0\n\t"
    "v_pk_mul_f32 v[8:9], v[8:9], s[66:67]\n\t"
    "v_pk_mul_f32 v[10:11], v[10:11], s[66:67]\n\t"
    "v_pk_mul_f32 v[12:13], v[12:13], s[66:67]\n\t"
    "v_pk_mul_f32 v[14:15], v[14:15], s[66:67]\n\t"
    "v_permlane32_swap_b32 v0, v2\n\t"
    "v_permlane32_swap_b32 v1, v3\n\t"
    "v_cvt_pk_bf16_f32 v4, v8, v9\n\t"
    "v_cvt_pk_bf16_f32 v5, v10, v11\n\t"
    "v_cvt_pk_bf16_f32 v6, v12, v13\n\t"
    "v_cvt_pk_bf16_f32 v7, v14, v15\n\t"
    "v_permlane16_swap_b32 v0, v2\n\t"
    "v_permlane16_swap_b32 v1, v3\n\t"
    "v_accvgpr_write_b32 a163, 0\n\t"
    "v_accvgpr_write_b32 a164, 0\n\t"
    "v_accvgpr_write_b32 a165, 0\n\t"
    "v_pk_mul_f32 v[16:17], v[16:17], s[66:67]\n\t"
    "v_pk_mul_f32 v[18:19], v[18:19], s[66:67]\n\t"
    "v_pk_mul_f32 v[20:21], v[20:21], s[66:67]\n\t"
    "v_pk_mul_f32 v[22:23], v[22:23], s[66:67]\n\t"
    "v_permlane32_swap_b32 v4, v6\n\t"
    "v_permlane32_swap_b32 v5, v7\n\t"
    "v_cvt_pk_bf16_f32 v8, v16, v17\n\t"
    "v_cvt_pk_bf16_f32 v9, v18, v19\n\t"
    "v_cvt_pk_bf16_f32 v10, v20, v21\n\t"
    "v_cvt_pk_bf16_f32 v11, v22, v23\n\t"
    "v_permlane16_swap_b32 v4, v6\n\t"
    "v_permlane16_swap_b32 v5, v7\n\t"
    "v_accvgpr_write_b32 a166, 0\n\t"
    "v_accvgpr_write_b32 a167, 0\n\t"
    "v_accvgpr_write_b32 a168, 0\n\t"
    "v_pk_mul_f32 v[24:25], v[24:25], s[66:67]\n\t"
    "v_pk_mul_f32 v[26:27], v[26:27], s[66:67]\n\t"
    "v_pk_mul_f32 v[28:29], v[28:29], s[66:67]\n\t"
    "v_pk_mul_f32 v[30:31], v[30:31], s[66:67]\n\t"
    "v_permlane32_swap_b32 v8, v10\n\t"
    "v_permlane32_swap_b32 v9, v11\n\t"
    "v_cvt_pk_bf16_f32 v12, v24, v25\n\t"
    "v_cvt_pk_bf16_f32 v13, v26, v27\n\t"
    "v_cvt_pk_bf16_f32 v14, v28, v29\n\t"
    "v_cvt_pk_bf16_f32 v15, v30, v31\n\t"
    "v_permlane16_swap_b32 v8, v10\n\t"
    "v_permlane16_swap_b32 v9, v11\n\t"
    "v_accvgpr_write_b32 a169, 0\n\t"
    "v_accvgpr_write_b32 a170, 0\n\t"
    "v_accvgpr_write_b32 a171, 0\n\t"
    "v_pk_mul_f32 v[32:33], v[32:33], s[66:67]\n\t"
    "v_pk_mul_f32 v[34:35], v[34:35], s[66:67]\n\t"
    "buffer_load_dwordx4 v207, s[44:47], s62 offen offset:3072 nt lds\n\t"
    "v_pk_mul_f32 v[36:37], v[36:37], s[66:67]\n\t"
    "v_pk_mul_f32 v[38:39], v[38:39], s[66:67]\n\t"
    "v_permlane32_swap_b32 v12, v14\n\t"
    "v_permlane32_swap_b32 v13, v15\n\t"
    "v_cvt_pk_bf16_f32 v16, v32, v33\n\t"
    "v_cvt_pk_bf16_f32 v17, v34, v35\n\t"
    "v_cvt_pk_bf16_f32 v18, v36, v37\n\t"
    "v_cvt_pk_bf16_f32 v19, v38, v39\n\t"
    "v_permlane16_swap_b32 v12, v14\n\t"
    "v_permlane16_swap_b32 v13, v15\n\t"
    "v_accvgpr_write_b32 a172, 0\n\t"
    "v_accvgpr_write_b32 a173, 0\n\t"
    "v_accvgpr_write_b32 a174, 0\n\t"
    "v_pk_mul_f32 v[40:41], v[40:41], s[66:67]\n\t"
    "v_pk_mul_f32 v[42:43], v[42:43], s[66:67]\n\t"
    "v_pk_mul_f32 v[44:45], v[44:45], s[66:67]\n\t"
    "v_pk_mul_f32 v[46:47], v[46:47], s[66:67]\n\t"
    "v_permlane32_swap_b32 v16, v18\n\t"
    "v_permlane32_swap_b32 v17, v19\n\t"
    "v_cvt_pk_bf16_f32 v20, v40, v41\n\t"
    "v_cvt_pk_bf16_f32 v21, v42, v43\n\t"
    "v_cvt_pk_bf16_f32 v22, v44, v45\n\t"
    "v_cvt_pk_bf16_f32 v23, v46, v47\n\t"
    "v_permlane16_swap_b32 v16, v18\n\t"
    "v_permlane16_swap_b32 v17, v19\n\t"
    "v_accvgpr_write_b32 a175, 0\n\t"
    "v_accvgpr_write_b32 a176, 0\n\t"
    "v_accvgpr_write_b32 a177, 0\n\t"
    "v_pk_mul_f32 v[48:49], v[48:49], s[66:67]\n\t"
    "v_pk_mul_f32 v[50:51], v[50:51], s[66:67]\n\t"
    "v_pk_mul_f32 v[52:53], v[52:53], s[66:67]\n\t"
    "v_pk_mul_f32 v[54:55], v[54:55], s[66:67]\n\t"
    "v_permlane32_swap_b32 v20, v22\n\t"
    "v_permlane32_swap_b32 v21, v23\n\t"
    "v_cvt_pk_bf16_f32 v24, v48, v49\n\t"
    "v_cvt_pk_bf16_f32 v25, v50, v51\n\t"
    "v_cvt_pk_bf16_f32 v26, v52, v53\n\t"
    "v_cvt_pk_bf16_f32 v27, v54, v55\n\t"
    "v_permlane16_swap_b32 v20, v22\n\t"
    "v_permlane16_swap_b32 v21, v23\n\t"
    "v_accvgpr_write_b32 a178, 0\n\t"
    "v_accvgpr_write_b32 a179, 0\n\t"
    "v_accvgpr_write_b32 a180, 0\n\t"
    "v_pk_mul_f32 v[56:57], v[56:57], s[66:67]\n\t"
    "v_pk_mul_f32 v[58:59], v[58:59], s[66:67]\n\t"
    "v_pk_mul_f32 v[60:61], v[60:61], s[66:67]\n\t"
    "v_pk_mul_f32 v[62:63], v[62:63], s[66:67]\n\t"
    "v_permlane32_swap_b32 v24, v26\n\t"
    "v_permlane32_swap_b32 v25, v27\n\t"
    "s_add_u32 m0, s63, 8192\n\t"
    "v_cvt_pk_bf16_f32 v28, v56, v57\n\t"
    "v_cvt_pk_bf16_f32 v29, v58, v59\n\t"
    "v_cvt_pk_bf16_f32 v30, v60, v61\n\t"
    "v_cvt_pk_bf16_f32 v31, v62, v63\n\t"
    "v_permlane16_swap_b32 v24, v26\n\t"
    "v_permlane16_swap_b32 v25, v27\n\t"
    "v_accvgpr_write_b32 a181, 0\n\t"
    "v_accvgpr_write_b32 a182, 0\n\t"
    "v_accvgpr_write_b32 a183, 0\n\t"
    "v_pk_mul_f32 v[64:65], v[64:65], s[66:67]\n\t"
    "v_pk_mul_f32 v[66:67], v[66:67], s[66:67]\n\t"
    "v_pk_mul_f32 v[68:69], v[68:69], s[66:67]\n\t"
    "v_pk_mul_f32 v[70:71], v[70:71], s[66:67]\n\t"
    "v_permlane32_swap_b32 v28, v30\n\t"
    "v_permlane32_swap_b32 v29, v31\n\t"
    "v_cvt_pk_bf16_f32 v32, v64, v65\n\t"
    "v_cvt_pk_bf16_f32 v33, v66, v67\n\t"
    "v_cvt_pk_bf16_f32 v34, v68, v69\n\t"
    "v_cvt_pk_bf16_f32 v35, v70, v71\n\t"
    "v_permlane16_swap_b32 v28, v30\n\t"
    "v_permlane16_swap_b32 v29, v31\n\t"
    "v_accvgpr_write_b32 a184, 0\n\t"
    "v_accvgpr_write_b32 a185, 0\n\t"
    "v_accvgpr_write_b32 a186, 0\n\t"
    "v_pk_mul_f32 v[72:73], v[72:73], s[66:67]\n\t"
    "v_pk_mul_f32 v[74:75], v[74:75], s[66:67]\n\t"
    "v_pk_mul_f32 v[76:77], v[76:77], s[66:67]\n\t"
    "v_pk_mul_f32 v[78:79], v[78:79], s[66:67]\n\t"
    "v_permlane32_swap_b32 v32, v34\n\t"
    "v_permlane32_swap_b32 v33, v35\n\t"
    "v_cvt_pk_bf16_f32 v36, v72, v73\n\t"
    "v_cvt_pk_bf16_f32 v37, v74, v75\n\t"
    "v_cvt_pk_bf16_f32 v38, v76, v77\n\t"
    "v_cvt_pk_bf16_f32 v39, v78, v79\n\t"
    "v_permlane16_swap_b32 v32, v34\n\t"
    "v_permlane16_swap_b32 v33, v35\n\t"
    "v_accvgpr_write_b32 a187, 0\n\t"
    "v_accvgpr_write_b32 a188, 0\n\t"
    "v_accvgpr_write_b32 a189, 0\n\t"
    "v_pk_mul_f32 v[80:81], v[80:81], s[66:67]\n\t"
    "v_pk_mul_f32 v[82:83], v[82:83], s[66:67]\n\t"
    "v_pk_mul_f32 v[84:85], v[84:85], s[66:67]\n\t"
    "v_pk_mul_f32 v[86:87], v[86:87], s[66:67]\n\t"
    "v_permlane32_swap_b32 v36, v38\n\t"
    "v_permlane32_swap_b32 v37, v39\n\t"
    "v_cvt_pk_bf16_f32 v40, v80, v81\n\t"
    "v_cvt_pk_bf16_f32 v41, v82, v83\n\t"
    "v_cvt_pk_bf16_f32 v42, v84, v85\n\t"
    "buffer_load_dwordx4 v208, s[48:51], s62 offen offset:0 nt lds\n\t"
    "v_cvt_pk_bf16_f32 v43, v86, v87\n\t"
    "v_permlane16_swap_b32 v36, v38\n\t"
    "v_permlane16_swap_b32 v37, v39\n\t"
    "v_accvgpr_write_b32 a190, 0\n\t"
    "v_accvgpr_write_b32 a191, 0\n\t"
    "v_accvgpr_write_b32 a212, 0\n\t"
    "v_pk_mul_f32 v[88:89], v[88:89], s[66:67]\n\t"
    "v_pk_mul_f32 v[90:91], v[90:91], s[66:67]\n\t"
    "v_pk_mul_f32 v[92:93], v[92:93], s[66:67]\n\t"
    "v_pk_mul_f32 v[94:95], v[94:95], s[66:67]\n\t"
    "v_permlane32_swap_b32 v40, v42\n\t"
    "v_permlane32_swap_b32 v41, v43\n\t"
    "v_cvt_pk_bf16_f32 v44, v88, v89\n\t"
    "v_cvt_pk_bf16_f32 v45, v90, v91\n\t"
    "v_cvt_pk_bf16_f32 v46, v92, v93\n\t"
    "v_cvt_pk_bf16_f32 v47, v94, v95\n\t"
    "v_permlane16_swap_b32 v40, v42\n\t"
    "v_permlane16_swap_b32 v41, v43\n\t"
    "v_accvgpr_write_b32 a213, 0\n\t"
    "v_accvgpr_write_b32 a214, 0\n\t"
    "v_accvgpr_write_b32 a215, 0\n\t"
    "v_pk_mul_f32 v[96:97], v[96:97], s[66:67]\n\t"
    "v_pk_mul_f32 v[98:99], v[98:99], s[66:67]\n\t"
    "v_pk_mul_f32 v[100:101], v[100:101], s[66:67]\n\t"
    "v_pk_mul_f32 v[102:103], v[102:103], s[66:67]\n\t"
    "v_permlane32_swap_b32 v44, v46\n\t"
    "v_permlane32_swap_b32 v45, v47\n\t"
    "v_cvt_pk_bf16_f32 v48, v96, v97\n\t"
    "v_cvt_pk_bf16_f32 v49, v98, v99\n\t"
    "v_cvt_pk_bf16_f32 v50, v100, v101\n\t"
    "v_cvt_pk_bf16_f32 v51, v102, v103\n\t"
    "v_permlane16_swap_b32 v44, v46\n\t"
    "v_permlane16_swap_b32 v45, v47\n\t"
    "v_pk_mul_f32 v[104:105], v[104:105], s[66:67]\n\t"
    "v_pk_mul_f32 v[106:107], v[106:107], s[66:67]\n\t"
    "v_pk_mul_f32 v[108:109], v[108:109], s[66:67]\n\t"
    "v_pk_mul_f32 v[110:111], v[110:111], s[66:67]\n\t"
    "v_permlane32_swap_b32 v48, v50\n\t"
    "v_permlane32_swap_b32 v49, v51\n\t"
    "v_cvt_pk_bf16_f32 v52, v104, v105\n\t"
    "v_cvt_pk_bf16_f32 v53, v106, v107\n\t"
    "v_cvt_pk_bf16_f32 v54, v108, v109\n\t"
    "v_cvt_pk_bf16_f32 v55, v110, v111\n\t"
    "v_permlane16_swap_b32 v48, v50\n\t"
    "v_permlane16_swap_b32 v49, v51\n\t"
    "v_pk_mul_f32 v[112:113], v[112:113], s[66:67]\n\t"
    "v_pk_mul_f32 v[114:115], v[114:115], s[66:67]\n\t"
    "v_pk_mul_f32 v[116:117], v[116:117], s[66:67]\n\t"
    "v_pk_mul_f32 v[118:119], v[118:119], s[66:67]\n\t"
    "buffer_load_dwordx4 v209, s[48:51], s62 offen offset:1024 nt lds\n\t"
    "v_permlane32_swap_b32 v52, v54\n\t"
    "v_permlane32_swap_b32 v53, v55\n\t"
    "v_cvt_pk_bf16_f32 v56, v112, v113\n\t"
    "v_cvt_pk_bf16_f32 v57, v114, v115\n\t"
    "v_cvt_pk_bf16_f32 v58, v116, v117\n\t"
    "v_cvt_pk_bf16_f32 v59, v118, v119\n\t"
    "v_permlane16_swap_b32 v52, v54\n\t"
    "v_permlane16_swap_b32 v53, v55\n\t"
    "v_pk_mul_f32 v[120:121], v[120:121], s[66:67]\n\t"
    "v_pk_mul_f32 v[122:123], v[122:123], s[66:67]\n\t"
    "v_pk_mul_f32 v[124:125], v[124:125], s[66:67]\n\t"
    "v_pk_mul_f32 v[126:127], v[126:127], s[66:67]\n\t"
    "v_permlane32_swap_b32 v56, v58\n\t"
    "v_permlane32_swap_b32 v57, v59\n\t"
    "v_cvt_pk_bf16_f32 v60, v120, v121\n\t"
    "v_cvt_pk_bf16_f32 v61, v122, v123\n\t"
    "v_cvt_pk_bf16_f32 v62, v124, v125\n\t"
    "v_cvt_pk_bf16_f32 v63, v126, v127\n\t"
    "v_permlane16_swap_b32 v56, v58\n\t"
    "v_permlane16_swap_b32 v57, v59\n\t"
    "v_pk_mul_f32 v[128:129], v[128:129], s[66:67]\n\t"
    "v_pk_mul_f32 v[130:131], v[130:131], s[66:67]\n\t"
    "v_pk_mul_f32 v[132:133], v[132:133], s[66:67]\n\t"
    "v_pk_mul_f32 v[134:135], v[134:135], s[66:67]\n\t"
    "v_permlane32_swap_b32 v60, v62\n\t"
    "v_permlane32_swap_b32 v61, v63\n\t"
    "v_cvt_pk_bf16_f32 v64, v128, v129\n\t"
    "v_cvt_pk_bf16_f32 v65, v130, v131\n\t"
    "v_cvt_pk_bf16_f32 v66, v132, v133\n\t"
    "v_cvt_pk_bf16_f32 v67, v134, v135\n\t"
    "v_permlane16_swap_b32 v60, v62\n\t"
    "v_permlane16_swap_b32 v61, v63\n\t"
    "s_waitcnt lgkmcnt(0)\n\t"
    "s_barrier\n\t"
    "ds_read_b128 v[96:99], v224 offset:0\n\t"
    "ds_read_b128 v[112:115], v224 offset:4096\n\t"
    "v_pk_mul_f32 v[136:137], v[136:137], s[66:67]\n\t"
    "v_pk_mul_f32 v[138:139], v[138:139], s[66:67]\n\t"
    "v_pk_mul_f32 v[140:141], v[140:141], s[66:67]\n\t"
    "v_pk_mul_f32 v[142:143], v[142:143], s[66:67]\n\t"
    "v_permlane32_swap_b32 v64, v66\n\t"
    "v_permlane32_swap_b32 v65, v67\n\t"
    "v_cvt_pk_bf16_f32 v68, v136, v137\n\t"
    "v_cvt_pk_bf16_f32 v69, v138, v139\n\t"
    "v_cvt_pk_bf16_f32 v70, v140, v141\n\t"
    "v_cvt_pk_bf16_f32 v71, v142, v143\n\t"
    "v_permlane16_swap_b32 v64, v66\n\t"
    "v_permlane16_swap_b32 v65, v67\n\t"
    "buffer_load_dwordx4 v221, s[48:51], s62 offen offset:2048 nt lds\n\t"
    "ds_read_b128 v[100:103], v225 offset:0\n\t"
    "ds_read_b128 v[116:119], v225 offset:4096\n\t"
    "ds_read_b128 v[104:107], v224 offset:512\n\t"
    "ds_read_b128 v[120:123], v224 offset:4608\n\t"
    "v_pk_mul_f32 v[144:145], v[144:145], s[66:67]\n\t"
    "v_pk_mul_f32 v[146:147], v[146:147], s[66:67]\n\t"
    "v_pk_mul_f32 v[148:149], v[148:149], s[66:67]\n\t"
    "v_pk_mul_f32 v[150:151], v[150:151], s[66:67]\n\t"
    "v_permlane32_swap_b32 v68, v70\n\t"
    "v_permlane32_swap_b32 v69, v71\n\t"
    "v_cvt_pk_bf16_f32 v72, v144, v145\n\t"
    "v_cvt_pk_bf16_f32 v73, v146, v147\n\t"
    "v_cvt_pk_bf16_f32 v74, v148, v149\n\t"
    "v_cvt_pk_bf16_f32 v75, v150, v151\n\t"
    "v_permlane16_swap_b32 v68, v70\n\t"
    "v_permlane16_swap_b32 v69, v71\n\t"
    "ds_read_b128 v[108:111], v225 offset:512\n\t"
    "ds_read_b128 v[124:127], v225 offset:4608\n\t"
    "ds_read_b64_tr_b16 a[216:217], v228 offset:0\n\t"
    "ds_read_b64_tr_b16 a[218:219], v228 offset:4096\n\t"
    "v_pk_mul_f32 v[152:153], v[152:153], s[66:67]\n\t"
    "v_pk_mul_f32 v[154:155], v[154:155], s[66:67]\n\t"
    "v_pk_mul_f32 v[156:157], v[156:157], s[66:67]\n\t"
    "v_pk_mul_f32 v[158:159], v[158:159], s[66:67]\n\t"
    "v_permlane32_swap_b32 v72, v74\n\t"
    "v_permlane32_swap_b32 v73, v75\n\t"
    "v_cvt_pk_bf16_f32 v76, v152, v153\n\t"
    "v_cvt_pk_bf16_f32 v77, v154, v155\n\t"
    "v_cvt_pk_bf16_f32 v78, v156, v157\n\t"
    "v_cvt_pk_bf16_f32 v79, v158, v159\n\t"
    "v_permlane16_swap_b32 v72, v74\n\t"
    "v_permlane16_swap_b32 v73, v75\n\t"
    "ds_read_b64_tr_b16 a[220:221], v229 offset:0\n\t"
    "ds_read_b64_tr_b16 a[222:223], v229 offset:4096\n\t"
    "ds_read_b64_tr_b16 a[224:225], v228 offset:256\n\t"
    "ds_read_b64_tr_b16 a[226:227], v228 offset:4352\n\t"
    "v_pk_mul_f32 v[160:161], v[160:161], s[66:67]\n\t"
    "v_pk_mul_f32 v[162:163], v[162:163], s[66:67]\n\t"
    "v_pk_mul_f32 v[164:165], v[164:165], s[66:67]\n\t"
    "v_pk_mul_f32 v[166:167], v[166:167], s[66:67]\n\t"
    "v_permlane32_swap_b32 v76, v78\n\t"
    "v_permlane32_swap_b32 v77, v79\n\t"
    "v_cvt_pk_bf16_f32 v80, v160, v161\n\t"
    "v_cvt_pk_bf16_f32 v81, v162, v163\n\t"
    "v_cvt_pk_bf16_f32 v82, v164, v165\n\t"
    "v_cvt_pk_bf16_f32 v83, v166, v167\n\t"
    "v_permlane16_swap_b32 v76, v78\n\t"
    "v_permlane16_swap_b32 v77, v79\n\t"
    "ds_read_b64_tr_b16 a[228:229], v229 offset:256\n\t"
    "buffer_load_dwordx4 v246, s[48:51], s62 offen offset:3072 nt lds\n\t"
    "ds_read_b64_tr_b16 a[230:231], v229 offset:4352\n\t"
    "ds_read_b64_tr_b16 a[232:233], v228 offset:512\n\t"
    "ds_read_b64_tr_b16 a[234:235], v228 offset:4608\n\t"
    "v_pk_mul_f32 v[168:169], v[168:169], s[66:67]\n\t"
    "v_pk_mul_f32 v[170:171], v[170:171], s[66:67]\n\t"
    "v_pk_mul_f32 v[172:173], v[172:173], s[66:67]\n\t"
    "v_pk_mul_f32 v[174:175], v[174:175], s[66:67]\n\t"
    "v_permlane32_swap_b32 v80, v82\n\t"
    "v_permlane32_swap_b32 v81, v83\n\t"
    "v_cvt_pk_bf16_f32 v84, v168, v169\n\t"
    "v_cvt_pk_bf16_f32 v85, v170, v171\n\t"
    "v_cvt_pk_bf16_f32 v86, v172, v173\n\t"
    "v_cvt_pk_bf16_f32 v87, v174, v175\n\t"
    "v_permlane16_swap_b32 v80, v82\n\t"
    "v_permlane16_swap_b32 v81, v83\n\t"
    "ds_read_b64_tr_b16 a[236:237], v229 offset:512\n\t"
    "ds_read_b64_tr_b16 a[238:239], v229 offset:4608\n\t"
    "ds_read_b64_tr_b16 a[240:241], v228 offset:768\n\t"
    "ds_read_b64_tr_b16 a[242:243], v228 offset:4864\n\t"
    "v_pk_mul_f32 v[176:177], v[176:177], s[66:67]\n\t"
    "v_pk_mul_f32 v[178:179], v[178:179], s[66:67]\n\t"
    "v_pk_mul_f32 v[180:181], v[180:181], s[66:67]\n\t"
    "v_pk_mul_f32 v[182:183], v[182:183], s[66:67]\n\t"
    "v_permlane32_swap_b32 v84, v86\n\t"
    "v_permlane32_swap_b32 v85, v87\n\t"
    "v_cvt_pk_bf16_f32 v88, v176, v177\n\t"
    "v_cvt_pk_bf16_f32 v89, v178, v179\n\t"
    "v_cvt_pk_bf16_f32 v90, v180, v181\n\t"
    "v_cvt_pk_bf16_f32 v91, v182, v183\n\t"
    "v_permlane16_swap_b32 v84, v86\n\t"
    "v_permlane16_swap_b32 v85, v87\n\t"
    "ds_read_b64_tr_b16 a[244:245], v229 offset:768\n\t"
    "ds_read_b64_tr_b16 a[246:247], v229 offset:4864\n\t"
    "v_pk_mul_f32 v[184:185], v[184:185], s[66:67]\n\t"
    "v_pk_mul_f32 v[186:187], v[186:187], s[66:67]\n\t"
    "v_pk_mul_f32 v[188:189], v[188:189], s[66:67]\n\t"
    "v_pk_mul_f32 v[190:191], v[190:191], s[66:67]\n\t"
    "v_permlane32_swap_b32 v88, v90\n\t"
    "v_permlane32_swap_b32 v89, v91\n\t"
    "v_cvt_pk_bf16_f32 v92, v184, v185\n\t"
    "v_cvt_pk_bf16_f32 v93, v186, v187\n\t"
    "v_cvt_pk_bf16_f32 v94, v188, v189\n\t"
    "v_cvt_pk_bf16_f32 v95, v190, v191\n\t"
    "v_permlane16_swap_b32 v88, v90\n\t"
    "v_permlane16_swap_b32 v89, v91\n\t"
    "v_permlane32_swap_b32 v92, v94\n\t"
    "v_permlane32_swap_b32 v93, v95\n\t"
    "s_nop 0\n\t"
    "v_permlane16_swap_b32 v92, v94\n\t"
    "v_permlane16_swap_b32 v93, v95\n\t"
    "v_mov_b32 v253, 0x3f803f80\n\t"
    "v_accvgpr_write_b32 a248, v253\n\t"
    "v_accvgpr_write_b32 a249, v253\n\t"
    "v_accvgpr_write_b32 a250, v253\n\t"
    "v_accvgpr_write_b32 a251, v253\n\t"
    "s_add_u32 s72, s71, s63\n\t"
    "s_add_u32 s71, s71, 32768\n\t"
    "s_cmp_eq_u32 s71, 163840\n\t"
    "s_cselect_b32 s71, 0, s71\n\t"
    "s_waitcnt lgkmcnt(14)\n\t"
    "v_mfma_f32_16x16x32_bf16 v[128:131], v[96:99], v[0:3], 0\n\t"
    "v_mfma_f32_16x16x32_bf16 v[132:135], v[112:115], v[0:3], 0\n\t"
    "v_mfma_f32_16x16x32_bf16 v[128:131], v[100:103], v[4:7], v[128:131]\n\t"
    "v_add_u32 v224, s71, v210\n\t"
    "v_mfma_f32_16x16x32_bf16 v[132:135], v[116:119], v[4:7], v[132:135]\n\t"
    "v_add_u32 v225, s71, v211\n\t"
    "v_mfma_f32_16x16x32_bf16 v[128:131], v[104:107], v[8:11], v[128:131]\n\t"
    "v_mfma_f32_16x16x32_bf16 v[132:135], v[120:123], v[8:11], v[132:135]\n\t"
    "v_add_u32 v226, s71, v212\n\t"
    "v_mfma_f32_16x16x32_bf16 v[128:131], v[108:111], v[12:15], v[128:131]\n\t"
    "v_add_u32 v227, s71, v213\n\t"
    "v_mfma_f32_16x16x32_bf16 v[132:135], v[124:127], v[12:15], v[132:135]\n\t"
    "v_mfma_f32_16x16x32_bf16 v[136:139], v[96:99], v[16:19], 0\n\t"
    "v_mfma_f32_16x16x32_bf16 v[140:143], v[112:115], v[16:19], 0\n\t"
    "v_mfma_f32_16x16x32_bf16 v[136:139], v[100:103], v[20:23], v[136:139]\n\t"
    "v_mfma_f32_16x16x32_bf16 v[140:143], v[116:119], v[20:23], v[140:143]\n\t"
    "v_mfma_f32_16x16x32_bf16 v[136:139], v[104:107], v[24:27], v[136:139]\n\t"
    "s_nop 0\n\t"
    "v_max3_f32 v252, v128, v129, v130\n\t"
    "s_nop 0\n\t"
    "v_max3_f32 v252, v252, v131, v132\n\t"
    "v_max3_f32 v252, v252, v133, v134\n\t"
    "v_mfma_f32_16x16x32_bf16 v[140:143], v[120:123], v[24:27], v[140:143]\n\t"
    "v_max_f32 v252, v252, v135\n\t"
    "v_mov_b32 v253, v252\n\t"
    "s_nop 1\n\t"
    "v_permlane32_swap_b32 v252, v253\n\t"
    "v_mfma_f32_16x16x32_bf16 v[136:139], v[108:111], v[28:31], v[136:139]\n\t"
    "v_max_f32 v252, v252, v253\n\t"
    "v_mov_b32 v253, v252\n\t"
    "s_nop 1\n\t"
    "v_permlane16_swap_b32 v252, v253\n\t"
    "v_mfma_f32_16x16x32_bf16 v[140:143], v[124:127], v[28:31], v[140:143]\n\t"
    "v_max_f32 v252, v252, v253\n\t"
    "v_sub_f32 v128, v128, v252\n\t"
    "v_mfma_f32_16x16x32_bf16 v[176:179], v[96:99], v[32:35], 0\n\t"
    "v_sub_f32 v129, v129, v252\n\t"
    "v_sub_f32 v130, v130, v252\n\t"
    "v_sub_f32 v131, v131, v252\n\t"
    "v_mfma_f32_16x16x32_bf16 v[180:183], v[112:115], v[32:35], 0\n\t"
    "v_sub_f32 v132, v132, v252\n\t"
    "v_sub_f32 v133, v133, v252\n\t"
    "v_sub_f32 v134, v134, v252\n\t"
    "v_mfma_f32_16x16x32_bf16 v[176:179], v[100:103], v[36:39], v[176:179]\n\t"
    "v_sub_f32 v135, v135, v252\n\t"
    "v_sub_f32 v152, 0, v252\n\t"
    "v_sub_f32 v153, 0, v252\n\t"
    "v_mfma_f32_16x16x32_bf16 v[180:183], v[116:119], v[36:39], v[180:183]\n\t"
    "v_sub_f32 v154, 0, v252\n\t"
    "v_sub_f32 v155, 0, v252\n\t"
    "v_mfma_f32_16x16x32_bf16 v[176:179], v[104:107], v[40:43], v[176:179]\n\t"
    "v_max3_f32 v252, v136, v137, v138\n\t"
    "v_max3_f32 v252, v252, v139, v140\n\t"
    "v_max3_f32 v252, v252, v141, v142\n\t"
    "v_max_f32 v252, v252, v143\n\t"
    "v_mov_b32 v253, v252\n\t"
    "s_nop 1\n\t"
    "v_permlane32_swap_b32 v252, v253\n\t"
    "v_mfma_f32_16x16x32_bf16 v[180:183], v[120:123], v[40:43], v[180:183]\n\t"
    "v_max_f32 v252, v252, v253\n\t"
    "v_mov_b32 v253, v252\n\t"
    "s_nop 1\n\t"
    "v_permlane16_swap_b32 v252, v253\n\t"
    "v_max_f32 v252, v252, v253\n\t"
    "v_sub_f32 v136, v136, v252\n\t"
    "v_mfma_f32_16x16x32_bf16 v[176:179], v[108:111], v[44:47], v[176:179]\n\t"
    "v_sub_f32 v137, v137, v252\n\t"
    "v_sub_f32 v138, v138, v252\n\t"
    "v_sub_f32 v139, v139, v252\n\t"
    "v_sub_f32 v140, v140, v252\n\t"
    "v_sub_f32 v141, v141, v252\n\t"
    "v_sub_f32 v142, v142, v252\n\t"
    "v_mfma_f32_16x16x32_bf16 v[180:183], v[124:127], v[44:47], v[180:183]\n\t"
    "v_sub_f32 v143, v143, v252\n\t"
    "v_sub_f32 v156, 0, v252\n\t"
    "v_sub_f32 v157, 0, v252\n\t"
    "v_sub_f32 v158, 0, v252\n\t"
    "v_sub_f32 v159, 0, v252\n\t"
    "v_max3_f32 v252, v176, v177, v178\n\t"
    "s_nop 1\n\t"
    "v_max3_f32 v252, v252, v179, v180\n\t"
    "v_max3_f32 v252, v252, v181, v182\n\t"
    "v_max_f32 v252, v252, v183\n\t"
    "v_mov_b32 v253, v252\n\t"
    "s_nop 1\n\t"
    "v_permlane32_swap_b32 v252, v253\n\t"
    "v_max_f32 v252, v252, v253\n\t"
    "v_mov_b32 v253, v252\n\t"
    "s_nop 1\n\t"
    "v_permlane16_swap_b32 v252, v253\n\t"
    "v_max_f32 v252, v252, v253\n\t"
    "v_sub_f32 v176, v176, v252\n\t"
    "v_sub_f32 v177, v177, v252\n\t"
    "v_sub_f32 v178, v178, v252\n\t"
    "v_sub_f32 v179, v179, v252\n\t"
    "v_sub_f32 v180, v180, v252\n\t"
    "v_sub_f32 v181, v181, v252\n\t"
    "v_sub_f32 v182, v182, v252\n\t"
    "v_sub_f32 v183, v183, v252\n\t"
    "v_sub_f32 v160, 0, v252\n\t"
    "v_sub_f32 v161, 0, v252\n\t"
    "v_sub_f32 v162, 0, v252\n\t"
    "v_sub_f32 v163, 0, v252\n\t"
    "v_mfma_f32_16x16x32_bf16 v[184:187], v[96:99], v[48:51], 0\n\t"
    "v_exp_f32 v128, v128\n\t"
    "v_mfma_f32_16x16x32_bf16 v[188:191], v[112:115], v[48:51], 0\n\t"
    "v_exp_f32 v129, v129\n\t"
    "v_mfma_f32_16x16x32_bf16 v[184:187], v[100:103], v[52:55], v[184:187]\n\t"
    "v_exp_f32 v130, v130\n\t"
    "v_mfma_f32_16x16x32_bf16 v[188:191], v[116:119], v[52:55], v[188:191]\n\t"
    "v_cvt_pk_bf16_f32 v144, v128, v129\n\t"
    "v_mfma_f32_16x16x32_bf16 v[184:187], v[104:107], v[56:59], v[184:187]\n\t"
    "v_exp_f32 v131, v131\n\t"
    "v_mfma_f32_16x16x32_bf16 v[188:191], v[120:123], v[56:59], v[188:191]\n\t"
    "v_exp_f32 v132, v132\n\t"
    "v_mfma_f32_16x16x32_bf16 v[184:187], v[108:111], v[60:63], v[184:187]\n\t"
    "v_exp_f32 v133, v133\n\t"
    "v_mfma_f32_16x16x32_bf16 v[188:191], v[124:127], v[60:63], v[188:191]\n\t"
    "v_exp_f32 v134, v134\n\t"
    "v_exp_f32 v135, v135\n\t"
    "v_cvt_pk_bf16_f32 v145, v130, v131\n\t"
    "s_nop 2\n\t"
    "v_max3_f32 v252, v184, v185, v186\n\t"
    "s_nop 0\n\t"
    "v_max3_f32 v252, v252, v187, v188\n\t"
    "v_max3_f32 v252, v252, v189, v190\n\t"
    "v_cvt_pk_bf16_f32 v146, v132, v133\n\t"
    "v_max_f32 v252, v252, v191\n\t"
    "v_cvt_pk_bf16_f32 v147, v134, v135\n\t"
    "v_mov_b32 v253, v252\n\t"
    "s_nop 1\n\t"
    "v_permlane32_swap_b32 v252, v253\n\t"
    "v_max_f32 v252, v252, v253\n\t"
    "v_mov_b32 v253, v252\n\t"
    "s_nop 1\n\t"
    "v_permlane16_swap_b32 v252, v253\n\t"
    "v_max_f32 v252, v252, v253\n\t"
    "v_sub_f32 v184, v184, v252\n\t"
    "v_sub_f32 v185, v185, v252\n\t"
    "v_sub_f32 v186, v186, v252\n\t"
    "v_sub_f32 v187, v187, v252\n\t"
    "v_sub_f32 v188, v188, v252\n\t"
    "v_sub_f32 v189, v189, v252\n\t"
    "v_sub_f32 v190, v190, v252\n\t"
    "v_sub_f32 v191, v191, v252\n\t"
    "v_sub_f32 v164, 0, v252\n\t"
    "v_sub_f32 v165, 0, v252\n\t"
    "v_sub_f32 v166, 0, v252\n\t"
    "v_sub_f32 v167, 0, v252\n\t"
    "v_mfma_f32_16x16x32_bf16 v[192:195], v[96:99], v[64:67], 0\n\t"
    "v_mfma_f32_16x16x32_bf16 v[196:199], v[112:115], v[64:67], 0\n\t"
    "v_mfma_f32_16x16x32_bf16 v[192:195], v[100:103], v[68:71], v[192:195]\n\t"
    "v_mfma_f32_16x16x32_bf16 v[196:199], v[116:119], v[68:71], v[196:199]\n\t"
    "v_mfma_f32_16x16x32_bf16 v[192:195], v[104:107], v[72:75], v[192:195]\n\t"
    "v_exp_f32 v136, v136\n\t"
    "v_mfma_f32_16x16x32_bf16 v[196:199], v[120:123], v[72:75], v[196:199]\n\t"
    "v_exp_f32 v137, v137\n\t"
    "v_mfma_f32_16x16x32_bf16 v[192:195], v[108:111], v[76:79], v[192:195]\n\t"
    "v_exp_f32 v138, v138\n\t"
    "v_mfma_f32_16x16x32_bf16 v[196:199], v[124:127], v[76:79], v[196:199]\n\t"
    "v_exp_f32 v139, v139\n\t"
    "v_mfma_f32_16x16x32_bf16 a[0:3], a[216:219], v[144:147], 0\n\t"
    "v_exp_f32 v140, v140\n\t"
    "s_waitcnt lgkmcnt(12)\n\t"
    "v_mfma_f32_16x16x32_bf16 a[4:7], a[220:223], v[144:147], 0\n\t"
    "v_exp_f32 v141, v141\n\t"
    "s_waitcnt lgkmcnt(10)\n\t"
    "v_mfma_f32_16x16x32_bf16 a[8:11], a[224:227], v[144:147], 0\n\t"
    "v_exp_f32 v142, v142\n\t"
    "s_waitcnt lgkmcnt(8)\n\t"
    "v_mfma_f32_16x16x32_bf16 a[12:15], a[228:231], v[144:147], 0\n\t"
    "v_exp_f32 v143, v143\n\t"
    "s_waitcnt lgkmcnt(0)\n\t"
    "v_mfma_f32_16x16x32_bf16 a[16:19], a[232:235], v[144:147], 0\n\t"
    "v_cvt_pk_bf16_f32 v148, v136, v137\n\t"
    "v_cvt_pk_bf16_f32 v149, v138, v139\n\t"
    "v_mfma_f32_16x16x32_bf16 a[20:23], a[236:239], v[144:147], 0\n\t"
    "v_cvt_pk_bf16_f32 v150, v140, v141\n\t"
    "v_cvt_pk_bf16_f32 v151, v142, v143\n\t"
    "v_mfma_f32_16x16x32_bf16 a[24:27], a[240:243], v[144:147], 0\n\t"
    "v_max3_f32 v252, v192, v193, v194\n\t"
    "v_max3_f32 v252, v252, v195, v196\n\t"
    "v_mfma_f32_16x16x32_bf16 a[28:31], a[244:247], v[144:147], 0\n\t"
    "v_max3_f32 v252, v252, v197, v198\n\t"
    "v_max_f32 v252, v252, v199\n\t"
    "v_mfma_f32_16x16x32_bf16 a[192:195], a[248:251], v[144:147], 0\n\t"
    "s_waitcnt vmcnt(24)\n\t"
    "s_barrier\n\t"
    "v_mov_b32 v253, v252\n\t"
    "s_nop 1\n\t"
    "v_permlane32_swap_b32 v252, v253\n\t"
    "v_max_f32 v252, v252, v253\n\t"
    "v_mov_b32 v253, v252\n\t"
    "s_nop 1\n\t"
    "v_permlane16_swap_b32 v252, v253\n\t"
    "v_max_f32 v252, v252, v253\n\t"
    "v_sub_f32 v192, v192, v252\n\t"
    "v_sub_f32 v193, v193, v252\n\t"
    "v_sub_f32 v194, v194, v252\n\t"
    "v_sub_f32 v195, v195, v252\n\t"
    "v_sub_f32 v196, v196, v252\n\t"
    "v_sub_f32 v197, v197, v252\n\t"
    "v_sub_f32 v198, v198, v252\n\t"
    "v_sub_f32 v199, v199, v252\n\t"
    "v_sub_f32 v168, 0, v252\n\t"
    "v_sub_f32 v169, 0, v252\n\t"
    "v_sub_f32 v170, 0, v252\n\t"
    "v_sub_f32 v171, 0, v252\n\t"
    "v_mfma_f32_16x16x32_bf16 v[200:203], v[96:99], v[80:83], 0\n\t"
    "v_exp_f32 v176, v176\n\t"
    "v_mfma_f32_16x16x32_bf16 v[248:251], v[112:115], v[80:83], 0\n\t"
    "ds_read_b128 v[96:99], v224 offset:0\n\t"
    "ds_read_b128 v[112:115], v224 offset:4096\n\t"
    "v_mfma_f32_16x16x32_bf16 v[200:203], v[100:103], v[84:87], v[200:203]\n\t"
    "v_exp_f32 v177, v177\n\t"
    "v_mfma_f32_16x16x32_bf16 v[248:251], v[116:119], v[84:87], v[248:251]\n\t"
    "ds_read_b128 v[100:103], v225 offset:0\n\t"
    "ds_read_b128 v[116:119], v225 offset:4096\n\t"
    "v_mfma_f32_16x16x32_bf16 v[200:203], v[104:107], v[88:91], v[200:203]\n\t"
    "v_exp_f32 v178, v178\n\t"
    "v_mfma_f32_16x16x32_bf16 v[248:251], v[120:123], v[88:91], v[248:251]\n\t"
    "ds_read_b128 v[104:107], v224 offset:512\n\t"
    "ds_read_b128 v[120:123], v224 offset:4608\n\t"
    "v_mfma_f32_16x16x32_bf16 v[200:203], v[108:111], v[92:95], v[200:203]\n\t"
    "v_exp_f32 v179, v179\n\t"
    "v_mfma_f32_16x16x32_bf16 v[248:251], v[124:127], v[92:95], v[248:251]\n\t"
    "ds_read_b128 v[108:111], v225 offset:512\n\t"
    "ds_read_b128 v[124:127], v225 offset:4608\n\t"
    "v_mfma_f32_16x16x32_bf16 a[32:35], a[216:219], v[148:151], 0\n\t"
    "s_add_u32 s62, s62, 8192\n\t"
    "s_memrealtime s[92:93]\n\t"
    "v_mfma_f32_16x16x32_bf16 a[36:39], a[220:223], v[148:151], 0\n\t"
    "v_exp_f32 v180, v180\n\t"
    "v_mfma_f32_16x16x32_bf16 a[40:43], a[224:227], v[148:151], 0\n\t"
    "v_exp_f32 v181, v181\n\t"
    "v_mfma_f32_16x16x32_bf16 a[44:47], a[228:231], v[148:151], 0\n\t"
    "v_exp_f32 v182, v182\n\t"
    "v_mfma_f32_16x16x32_bf16 a[48:51], a[232:235], v[148:151], 0\n\t"
    "v_max3_f32 v252, v200, v201, v202\n\t"
    "v_max3_f32 v252, v252, v203, v248\n\t"
    "v_mfma_f32_16x16x32_bf16 a[52:55], a[236:239], v[148:151], 0\n\t"
    "v_max3_f32 v252, v252, v249, v250\n\t"
    "v_max_f32 v252, v252, v251\n\t"
    "v_mfma_f32_16x16x32_bf16 a[56:59], a[240:243], v[148:151], 0\n\t"
    "v_mov_b32 v253, v252\n\t"
    "s_mov_b32 m0, s72\n\t"
    "v_mfma_f32_16x16x32_bf16 a[60:63], a[244:247], v[148:151], 0\n\t"
    "v_exp_f32 v183, v183\n\t"
    "v_mfma_f32_16x16x32_bf16 a[196:199], a[248:251], v[148:151], 0\n\t"
    "buffer_load_dwordx4 v204, s[44:47], s62 offen offset:0 nt lds\n\t"
    "v_permlane32_swap_b32 v252, v253\n\t"
    "v_max_f32 v252, v252, v253\n\t"
    "v_mov_b32 v253, v252\n\t"
    "s_nop 1\n\t"
    "v_permlane16_swap_b32 v252, v253\n\t"
    "v_max_f32 v252, v252, v253\n\t"
    "v_sub_f32 v200, v200, v252\n\t"
    "v_sub_f32 v201, v201, v252\n\t"
    "v_sub_f32 v202, v202, v252\n\t"
    "v_sub_f32 v203, v203, v252\n\t"
    "v_sub_f32 v248, v248, v252\n\t"
    "v_sub_f32 v249, v249, v252\n\t"
    "v_sub_f32 v250, v250, v252\n\t"
    "v_sub_f32 v251, v251, v252\n\t"
    "v_sub_f32 v172, 0, v252\n\t"
    "v_sub_f32 v173, 0, v252\n\t"
    "v_sub_f32 v174, 0, v252\n\t"
    "v_sub_f32 v175, 0, v252\n\t"
    "s_waitcnt lgkmcnt(0)\n\t"
    "v_mfma_f32_16x16x32_bf16 v[128:131], v[96:99], v[0:3], v[152:155]\n\t"
    "v_cvt_pk_bf16_f32 v144, v176, v177\n\t"
    "v_cvt_pk_bf16_f32 v145, v178, v179\n\t"
    "v_mfma_f32_16x16x32_bf16 v[132:135], v[112:115], v[0:3], v[152:155]\n\t"
    "v_cvt_pk_bf16_f32 v146, v180, v181\n\t"
    "v_cvt_pk_bf16_f32 v147, v182, v183\n\t"
    "v_mfma_f32_16x16x32_bf16 v[128:131], v[100:103], v[4:7], v[128:131]\n\t"
    "v_exp_f32 v184, v184\n\t"
    "v_mfma_f32_16x16x32_bf16 v[132:135], v[116:119], v[4:7], v[132:135]\n\t"
    "v_exp_f32 v185, v185\n\t"
    "v_mfma_f32_16x16x32_bf16 v[128:131], v[104:107], v[8:11], v[128:131]\n\t"
    "v_cvt_pk_bf16_f32 v148, v184, v185\n\t"
    "v_add_u32 v228, s71, v214\n\t"
    "v_mfma_f32_16x16x32_bf16 v[132:135], v[120:123], v[8:11], v[132:135]\n\t"
    "v_exp_f32 v186, v186\n\t"
    "v_mfma_f32_16x16x32_bf16 v[128:131], v[108:111], v[12:15], v[128:131]\n\t"
    "v_exp_f32 v187, v187\n\t"
    "v_mfma_f32_16x16x32_bf16 v[132:135], v[124:127], v[12:15], v[132:135]\n\t"
    "v_cvt_pk_bf16_f32 v149, v186, v187\n\t"
    "v_add_u32 v229, s71, v215\n\t"
    "v_mfma_f32_16x16x32_bf16 a[64:67], a[216:219], v[144:147], 0\n\t"
    "v_exp_f32 v188, v188\n\t"
    "v_mfma_f32_16x16x32_bf16 a[68:71], a[220:223], v[144:147], 0\n\t"
    "v_exp_f32 v189, v189\n\t"
    "v_mfma_f32_16x16x32_bf16 a[72:75], a[224:227], v[144:147], 0\n\t"
    "v_cvt_pk_bf16_f32 v150, v188, v189\n\t"
    "buffer_load_dwordx4 v205, s[44:47], s62 offen offset:1024 nt lds\n\t"
    "v_mfma_f32_16x16x32_bf16 a[76:79], a[228:231], v[144:147], 0\n\t"
    "s_lshr_b32 s34, s62, 13\n\t"
    "s_sub_u32 s74, s92, s32\n\t"
    "v_mfma_f32_16x16x32_bf16 a[80:83], a[232:235], v[144:147], 0\n\t"
    "v_exp_f32 v190, v190\n\t"
    "v_mfma_f32_16x16x32_bf16 a[84:87], a[236:239], v[144:147], 0\n\t"
    "v_max3_f32 v252, v128, v129, v130\n\t"
    "v_max3_f32 v252, v252, v131, v132\n\t"
    "v_mfma_f32_16x16x32_bf16 a[88:91], a[240:243], v[144:147], 0\n\t"
    "v_exp_f32 v191, v191\n\t"
    "v_mfma_f32_16x16x32_bf16 a[92:95], a[244:247], v[144:147], 0\n\t"
    "v_max3_f32 v252, v252, v133, v134\n\t"
    "v_max_f32 v252, v252, v135\n\t"
    "v_mfma_f32_16x16x32_bf16 a[200:203], a[248:251], v[144:147], 0\n\t"
    "v_cvt_pk_bf16_f32 v151, v190, v191\n\t"
    "v_readlane_b32 s33, v247, s34\n\t"
    "v_mfma_f32_16x16x32_bf16 v[136:139], v[96:99], v[16:19], v[156:159]\n\t"
    "v_exp_f32 v192, v192\n\t"
    "v_mfma_f32_16x16x32_bf16 v[140:143], v[112:115], v[16:19], v[156:159]\n\t"
    "s_add_u32 s72, s71, s63\n\t"
    "s_add_u32 s71, s71, 32768\n\t"
    "v_mfma_f32_16x16x32_bf16 v[136:139], v[100:103], v[20:23], v[136:139]\n\t"
    "v_exp_f32 v193, v193\n\t"
    "v_mfma_f32_16x16x32_bf16 v[140:143], v[116:119], v[20:23], v[140:143]\n\t"
    "v_cvt_pk_bf16_f32 v144, v192, v193\n\t"
    "buffer_load_dwordx4 v206, s[44:47], s62 offen offset:2048 nt lds\n\t"
    "v_mfma_f32_16x16x32_bf16 v[136:139], v[104:107], v[24:27], v[136:139]\n\t"
    "v_exp_f32 v194, v194\n\t"
    "v_mfma_f32_16x16x32_bf16 v[140:143], v[120:123], v[24:27], v[140:143]\n\t"
    "s_cmp_eq_u32 s71, 163840\n\t"
    "s_cselect_b32 s71, 0, s71\n\t"
    "v_mfma_f32_16x16x32_bf16 v[136:139], v[108:111], v[28:31], v[136:139]\n\t"
    "v_exp_f32 v195, v195\n\t"
    "v_mfma_f32_16x16x32_bf16 v[140:143], v[124:127], v[28:31], v[140:143]\n\t"
    "v_cvt_pk_bf16_f32 v145, v194, v195\n\t"
    "v_add_u32 v224, s71, v210\n\t"
    "v_mfma_f32_16x16x32_bf16 a[96:99], a[216:219], v[148:151], 0\n\t"
    "v_exp_f32 v196, v196\n\t"
    "v_mfma_f32_16x16x32_bf16 a[100:103], a[220:223], v[148:151], 0\n\t"
    "v_add_u32 v225, s71, v211\n\t"
    "v_mfma_f32_16x16x32_bf16 a[104:107], a[224:227], v[148:151], 0\n\t"
    "v_exp_f32 v197, v197\n\t"
    "v_mfma_f32_16x16x32_bf16 a[108:111], a[228:231], v[148:151], 0\n\t"
    "v_cvt_pk_bf16_f32 v146, v196, v197\n\t"
    "buffer_load_dwordx4 v207, s[44:47], s62 offen offset:3072 nt lds\n\t"
    "v_mfma_f32_16x16x32_bf16 a[112:115], a[232:235], v[148:151], 0\n\t"
    "v_exp_f32 v198, v198\n\t"
    "v_mfma_f32_16x16x32_bf16 a[116:119], a[236:239], v[148:151], 0\n\t"
    "v_max3_f32 v252, v252, v136, v137\n\t"
    "v_max3_f32 v252, v252, v138, v139\n\t"
    "v_mfma_f32_16x16x32_bf16 a[120:123], a[240:243], v[148:151], 0\n\t"
    "v_exp_f32 v199, v199\n\t"
    "v_mfma_f32_16x16x32_bf16 a[124:127], a[244:247], v[148:151], 0\n\t"
    "v_max3_f32 v252, v252, v140, v141\n\t"
    "v_max3_f32 v252, v252, v142, v143\n\t"
    "v_mfma_f32_16x16x32_bf16 a[204:207], a[248:251], v[148:151], 0\n\t"
    "v_cvt_pk_bf16_f32 v147, v198, v199\n\t"
    "s_addk_i32 m0, 8192\n\t"
    "v_mfma_f32_16x16x32_bf16 v[176:179], v[96:99], v[32:35], v[160:163]\n\t"
    "v_exp_f32 v200, v200\n\t"
    "v_mfma_f32_16x16x32_bf16 v[180:183], v[112:115], v[32:35], v[160:163]\n\t"
    "v_exp_f32 v201, v201\n\t"
    "v_mfma_f32_16x16x32_bf16 v[176:179], v[100:103], v[36:39], v[176:179]\n\t"
    "v_cvt_pk_bf16_f32 v148, v200, v201\n\t"
    "v_mfma_f32_16x16x32_bf16 v[180:183], v[116:119], v[36:39], v[180:183]\n\t"
    "v_exp_f32 v202, v202\n\t"
    "v_mfma_f32_16x16x32_bf16 v[176:179], v[104:107], v[40:43], v[176:179]\n\t"
    "v_exp_f32 v203, v203\n\t"
    "v_mfma_f32_16x16x32_bf16 v[180:183], v[120:123], v[40:43], v[180:183]\n\t"
    "v_cvt_pk_bf16_f32 v149, v202, v203\n\t"
    "buffer_load_dwordx4 v208, s[48:51], s62 offen offset:0 nt lds\n\t"
    "v_mfma_f32_16x16x32_bf16 v[176:179], v[108:111], v[44:47], v[176:179]\n\t"
    "v_exp_f32 v248, v248\n\t"
    "v_mfma_f32_16x16x32_bf16 v[180:183], v[124:127], v[44:47], v[180:183]\n\t"
    "s_add_u32 s32, s92, s33\n\t"
    "v_mfma_f32_16x16x32_bf16 a[128:131], a[216:219], v[144:147], 0\n\t"
    "v_exp_f32 v249, v249\n\t"
    "v_mfma_f32_16x16x32_bf16 a[132:135], a[220:223], v[144:147], 0\n\t"
    "v_cvt_pk_bf16_f32 v150, v248, v249\n\t"
    "v_mfma_f32_16x16x32_bf16 a[136:139], a[224:227], v[144:147], 0\n\t"
    "v_exp_f32 v250, v250\n\t"
    "v_mfma_f32_16x16x32_bf16 a[140:143], a[228:231], v[144:147], 0\n\t"
    "v_exp_f32 v251, v251\n\t"
    "v_mfma_f32_16x16x32_bf16 a[144:147], a[232:235], v[144:147], 0\n\t"
    "v_max3_f32 v252, v252, v176, v177\n\t"
    "v_max3_f32 v252, v252, v178, v179\n\t"
    "v_mfma_f32_16x16x32_bf16 a[148:151], a[236:239], v[144:147], 0\n\t"
    "v_max3_f32 v252, v252, v180, v181\n\t"
    "v_max3_f32 v252, v252, v182, v183\n\t"
    "v_mfma_f32_16x16x32_bf16 a[152:155], a[240:243], v[144:147], 0\n\t"
    "v_cmp_lt_f32 vcc, s64, v252\n\t"
    "buffer_load_dwordx4 v209, s[48:51], s62 offen offset:1024 nt lds\n\t"
    "v_mfma_f32_16x16x32_bf16 a[156:159], a[244:247], v[144:147], 0\n\t"
    "v_cvt_pk_bf16_f32 v151, v250, v251\n\t"
    "v_mfma_f32_16x16x32_bf16 a[208:211], a[248:251], v[144:147], 0\n\t"
    "s_cbranch_vccnz .LSTUB_F_0_%=\n\t"
    ".LRET_F_0_%=:\n\t"
    ".p2align 6\n\t"
    ".LATL_%=:\n\t"
    "v_mfma_f32_16x16x32_bf16 v[184:187], v[96:99], v[48:51], v[164:167]\n\t"
    "v_exp_f32 v128, v128\n\t"
    "v_mfma_f32_16x16x32_bf16 v[188:191], v[112:115], v[48:51], v[164:167]\n\t"
    "v_exp_f32 v129, v129\n\t"
    "v_mfma_f32_16x16x32_bf16 v[184:187], v[100:103], v[52:55], v[184:187]\n\t"
    "v_exp_f32 v130, v130\n\t"
    "v_mfma_f32_16x16x32_bf16 v[188:191], v[116:119], v[52:55], v[188:191]\n\t"
    "buffer_load_dwordx4 v221, s[48:51], s62 offen offset:2048 nt lds\n\t"
    "v_cvt_pk_bf16_f32 v144, v128, v129\n\t"
    "v_mfma_f32_16x16x32_bf16 v[184:187], v[104:107], v[56:59], v[184:187]\n\t"
    "v_exp_f32 v131, v131\n\t"
    "v_mfma_f32_16x16x32_bf16 v[188:191], v[120:123], v[56:59], v[188:191]\n\t"
    "v_exp_f32 v132, v132\n\t"
    "v_mfma_f32_16x16x32_bf16 v[184:187], v[108:111], v[60:63], v[184:187]\n\t"
    "v_exp_f32 v133, v133\n\t"
    "v_mfma_f32_16x16x32_bf16 v[188:191], v[124:127], v[60:63], v[188:191]\n\t"
    "v_exp_f32 v134, v134\n\t"
    "v_mfma_f32_16x16x32_bf16 a[160:163], a[216:219], v[148:151], a[160:163]\n\t"
    "ds_read_b64_tr_b16 a[216:217], v228 offset:0\n\t"
    "ds_read_b64_tr_b16 a[218:219], v228 offset:4096\n\t"
    "v_mfma_f32_16x16x32_bf16 a[164:167], a[220:223], v[148:151], a[164:167]\n\t"
    "v_exp_f32 v135, v135\n\t"
    "v_mfma_f32_16x16x32_bf16 a[168:171], a[224:227], v[148:151], a[168:171]\n\t"
    "ds_read_b64_tr_b16 a[220:221], v229 offset:0\n\t"
    "ds_read_b64_tr_b16 a[222:223], v229 offset:4096\n\t"
    "v_mfma_f32_16x16x32_bf16 a[172:175], a[228:231], v[148:151], a[172:175]\n\t"
    "ds_read_b64_tr_b16 a[224:225], v228 offset:256\n\t"
    "v_cvt_pk_bf16_f32 v145, v130, v131\n\t"
    "v_mfma_f32_16x16x32_bf16 a[176:179], a[232:235], v[148:151], a[176:179]\n\t"
    "ds_read_b64_tr_b16 a[226:227], v228 offset:4352\n\t"
    "v_max3_f32 v252, v184, v185, v186\n\t"
    "v_mfma_f32_16x16x32_bf16 a[180:183], a[236:239], v[148:151], a[180:183]\n\t"
    "ds_read_b64_tr_b16 a[228:229], v229 offset:256\n\t"
    "v_max3_f32 v252, v252, v187, v188\n\t"
    "v_mfma_f32_16x16x32_bf16 a[184:187], a[240:243], v[148:151], a[184:187]\n\t"
    "ds_read_b64_tr_b16 a[230:231], v229 offset:4352\n\t"
    "v_max3_f32 v252, v252, v189, v190\n\t"
    "v_mfma_f32_16x16x32_bf16 a[188:191], a[244:247], v[148:151], a[188:191]\n\t"
    "v_cvt_pk_bf16_f32 v146, v132, v133\n\t"
    "v_max_f32 v252, v252, v191\n\t"
    "v_mfma_f32_16x16x32_bf16 a[212:215], a[248:251], v[148:151], a[212:215]\n\t"
    "ds_read_b64_tr_b16 a[232:233], v228 offset:512\n\t"
    "v_cvt_pk_bf16_f32 v147, v134, v135\n\t"
    "v_mfma_f32_16x16x32_bf16 v[192:195], v[96:99], v[64:67], v[168:171]\n\t"
    "ds_read_b64_tr_b16 a[234:235], v228 offset:4608\n\t"
    "ds_read_b64_tr_b16 a[236:237], v229 offset:512\n\t"
    "v_mfma_f32_16x16x32_bf16 v[196:199], v[112:115], v[64:67], v[168:171]\n\t"
    "ds_read_b64_tr_b16 a[238:239], v229 offset:4608\n\t"
    "ds_read_b64_tr_b16 a[240:241], v228 offset:768\n\t"
    "v_mfma_f32_16x16x32_bf16 v[192:195], v[100:103], v[68:71], v[192:195]\n\t"
    "ds_read_b64_tr_b16 a[242:243], v228 offset:4864\n\t"
    "ds_read_b64_tr_b16 a[244:245], v229 offset:768\n\t"
    "v_mfma_f32_16x16x32_bf16 v[196:199], v[116:119], v[68:71], v[196:199]\n\t"
    "ds_read_b64_tr_b16 a[246:247], v229 offset:4864\n\t"
    "buffer_load_dwordx4 v246, s[48:51], s62 offen offset:3072 nt lds\n\t"
    "v_mfma_f32_16x16x32_bf16 v[192:195], v[104:107], v[72:75], v[192:195]\n\t"
    "v_exp_f32 v136, v136\n\t"
    "v_mfma_f32_16x16x32_bf16 v[196:199], v[120:123], v[72:75], v[196:199]\n\t"
    "v_exp_f32 v137, v137\n\t"
    "v_mfma_f32_16x16x32_bf16 v[192:195], v[108:111], v[76:79], v[192:195]\n\t"
    "v_exp_f32 v138, v138\n\t"
    "v_mfma_f32_16x16x32_bf16 v[196:199], v[124:127], v[76:79], v[196:199]\n\t"
    "v_exp_f32 v139, v139\n\t"
    "s_waitcnt lgkmcnt(7)\n\t"
    "v_mfma_f32_16x16x32_bf16 a[0:3], a[216:219], v[144:147], a[0:3]\n\t"
    "v_exp_f32 v140, v140\n\t"
    "v_mfma_f32_16x16x32_bf16 a[4:7], a[220:223], v[144:147], a[4:7]\n\t"
    "v_exp_f32 v141, v141\n\t"
    "v_mfma_f32_16x16x32_bf16 a[8:11], a[224:227], v[144:147], a[8:11]\n\t"
    "v_exp_f32 v142, v142\n\t"
    "v_mfma_f32_16x16x32_bf16 a[12:15], a[228:231], v[144:147], a[12:15]\n\t"
    "v_exp_f32 v143, v143\n\t"
    "s_waitcnt lgkmcnt(0)\n\t"
    "v_mfma_f32_16x16x32_bf16 a[16:19], a[232:235], v[144:147], a[16:19]\n\t"
    "v_cvt_pk_bf16_f32 v148, v136, v137\n\t"
    "v_cvt_pk_bf16_f32 v149, v138, v139\n\t"
    "v_mfma_f32_16x16x32_bf16 a[20:23], a[236:239], v[144:147], a[20:23]\n\t"
    "v_cvt_pk_bf16_f32 v150, v140, v141\n\t"
    "v_cvt_pk_bf16_f32 v151, v142, v143\n\t"
    "v_mfma_f32_16x16x32_bf16 a[24:27], a[240:243], v[144:147], a[24:27]\n\t"
    "v_max3_f32 v252, v252, v192, v193\n\t"
    "v_max3_f32 v252, v252, v194, v195\n\t"
    "v_mfma_f32_16x16x32_bf16 a[28:31], a[244:247], v[144:147], a[28:31]\n\t"
    "v_max3_f32 v252, v252, v196, v197\n\t"
    "v_max3_f32 v252, v252, v198, v199\n\t"
    "v_mfma_f32_16x16x32_bf16 a[192:195], a[248:251], v[144:147], a[192:195]\n\t"
    "s_waitcnt vmcnt(24)\n\t"
    "s_barrier\n\t"
    "v_mfma_f32_16x16x32_bf16 v[200:203], v[96:99], v[80:83], v[172:175]\n\t"
    "v_exp_f32 v176, v176\n\t"
    "v_mfma_f32_16x16x32_bf16 v[248:251], v[112:115], v[80:83], v[172:175]\n\t"
    "ds_read_b128 v[96:99], v224 offset:0\n\t"
    "ds_read_b128 v[112:115], v224 offset:4096\n\t"
    "v_mfma_f32_16x16x32_bf16 v[200:203], v[100:103], v[84:87], v[200:203]\n\t"
    "v_exp_f32 v177, v177\n\t"
    "v_mfma_f32_16x16x32_bf16 v[248:251], v[116:119], v[84:87], v[248:251]\n\t"
    "ds_read_b128 v[100:103], v225 offset:0\n\t"
    "ds_read_b128 v[116:119], v225 offset:4096\n\t"
    "v_mfma_f32_16x16x32_bf16 v[200:203], v[104:107], v[88:91], v[200:203]\n\t"
    "v_exp_f32 v178, v178\n\t"
    "v_mfma_f32_16x16x32_bf16 v[248:251], v[120:123], v[88:91], v[248:251]\n\t"
    "ds_read_b128 v[104:107], v224 offset:512\n\t"
    "ds_read_b128 v[120:123], v224 offset:4608\n\t"
    "v_mfma_f32_16x16x32_bf16 v[200:203], v[108:111], v[92:95], v[200:203]\n\t"
    "v_exp_f32 v179, v179\n\t"
    "v_mfma_f32_16x16x32_bf16 v[248:251], v[124:127], v[92:95], v[248:251]\n\t"
    "ds_read_b128 v[108:111], v225 offset:512\n\t"
    "ds_read_b128 v[124:127], v225 offset:4608\n\t"
    "v_mfma_f32_16x16x32_bf16 a[32:35], a[216:219], v[148:151], a[32:35]\n\t"
    "s_add_u32 s62, s62, 8192\n\t"
    "s_memrealtime s[92:93]\n\t"
    "v_mfma_f32_16x16x32_bf16 a[36:39], a[220:223], v[148:151], a[36:39]\n\t"
    "v_exp_f32 v180, v180\n\t"
    "v_mfma_f32_16x16x32_bf16 a[40:43], a[224:227], v[148:151], a[40:43]\n\t"
    "v_exp_f32 v181, v181\n\t"
    "v_mfma_f32_16x16x32_bf16 a[44:47], a[228:231], v[148:151], a[44:47]\n\t"
    "v_exp_f32 v182, v182\n\t"
    "v_mfma_f32_16x16x32_bf16 a[48:51], a[232:235], v[148:151], a[48:51]\n\t"
    "v_max3_f32 v252, v252, v200, v201\n\t"
    "v_max3_f32 v252, v252, v202, v203\n\t"
    "v_mfma_f32_16x16x32_bf16 a[52:55], a[236:239], v[148:151], a[52:55]\n\t"
    "v_max3_f32 v252, v252, v248, v249\n\t"
    "v_max3_f32 v252, v252, v250, v251\n\t"
    "v_mfma_f32_16x16x32_bf16 a[56:59], a[240:243], v[148:151], a[56:59]\n\t"
    "v_cmp_lt_f32 vcc, s64, v252\n\t"
    "s_mov_b32 m0, s72\n\t"
    "v_mfma_f32_16x16x32_bf16 a[60:63], a[244:247], v[148:151], a[60:63]\n\t"
    "v_exp_f32 v183, v183\n\t"
    "v_mfma_f32_16x16x32_bf16 a[196:199], a[248:251], v[148:151], a[196:199]\n\t"
    "buffer_load_dwordx4 v204, s[44:47], s62 offen offset:0 nt lds\n\t"
    "s_cbranch_vccnz .LSTUB_L_1_%=\n\t"
    ".LRET_L_1_%=:\n\t"
    "s_waitcnt lgkmcnt(0)\n\t"
    "v_mfma_f32_16x16x32_bf16 v[128:131], v[96:99], v[0:3], v[152:155]\n\t"
    "v_cvt_pk_bf16_f32 v144, v176, v177\n\t"
    "v_cvt_pk_bf16_f32 v145, v178, v179\n\t"
    "v_mfma_f32_16x16x32_bf16 v[132:135], v[112:115], v[0:3], v[152:155]\n\t"
    "v_cvt_pk_bf16_f32 v146, v180, v181\n\t"
    "v_cvt_pk_bf16_f32 v147, v182, v183\n\t"
    "v_mfma_f32_16x16x32_bf16 v[128:131], v[100:103], v[4:7], v[128:131]\n\t"
    "v_exp_f32 v184, v184\n\t"
    "v_mfma_f32_16x16x32_bf16 v[132:135], v[116:119], v[4:7], v[132:135]\n\t"
    "v_exp_f32 v185, v185\n\t"
    "v_mfma_f32_16x16x32_bf16 v[128:131], v[104:107], v[8:11], v[128:131]\n\t"
    "v_cvt_pk_bf16_f32 v148, v184, v185\n\t"
    "v_add_u32 v228, s71, v214\n\t"
    "v_mfma_f32_16x16x32_bf16 v[132:135], v[120:123], v[8:11], v[132:135]\n\t"
    "v_exp_f32 v186, v186\n\t"
    "v_mfma_f32_16x16x32_bf16 v[128:131], v[108:111], v[12:15], v[128:131]\n\t"
    "v_exp_f32 v187, v187\n\t"
    "v_mfma_f32_16x16x32_bf16 v[132:135], v[124:127], v[12:15], v[132:135]\n\t"
    "v_cvt_pk_bf16_f32 v149, v186, v187\n\t"
    "v_add_u32 v229, s71, v215\n\t"
    "v_mfma_f32_16x16x32_bf16 a[64:67], a[216:219], v[144:147], a[64:67]\n\t"
    "v_exp_f32 v188, v188\n\t"
    "v_mfma_f32_16x16x32_bf16 a[68:71], a[220:223], v[144:147], a[68:71]\n\t"
    "v_exp_f32 v189, v189\n\t"
    "v_mfma_f32_16x16x32_bf16 a[72:75], a[224:227], v[144:147], a[72:75]\n\t"
    "v_cvt_pk_bf16_f32 v150, v188, v189\n\t"
    "buffer_load_dwordx4 v205, s[44:47], s62 offen offset:1024 nt lds\n\t"
    "v_mfma_f32_16x16x32_bf16 a[76:79], a[228:231], v[144:147], a[76:79]\n\t"
    "s_lshr_b32 s34, s62, 13\n\t"
    "s_sub_u32 s74, s92, s32\n\t"
    "v_mfma_f32_16x16x32_bf16 a[80:83], a[232:235], v[144:147], a[80:83]\n\t"
    "v_exp_f32 v190, v190\n\t"
    "v_mfma_f32_16x16x32_bf16 a[84:87], a[236:239], v[144:147], a[84:87]\n\t"
    "v_max3_f32 v252, v128, v129, v130\n\t"
    "v_max3_f32 v252, v252, v131, v132\n\t"
    "v_mfma_f32_16x16x32_bf16 a[88:91], a[240:243], v[144:147], a[88:91]\n\t"
    "v_exp_f32 v191, v191\n\t"
    "v_mfma_f32_16x16x32_bf16 a[92:95], a[244:247], v[144:147], a[92:95]\n\t"
    "v_max3_f32 v252, v252, v133, v134\n\t"
    "v_max_f32 v252, v252, v135\n\t"
    "v_mfma_f32_16x16x32_bf16 a[200:203], a[248:251], v[144:147], a[200:203]\n\t"
    "v_cvt_pk_bf16_f32 v151, v190, v191\n\t"
    "v_readlane_b32 s33, v247, s34\n\t"
    "v_mfma_f32_16x16x32_bf16 v[136:139], v[96:99], v[16:19], v[156:159]\n\t"
    "v_exp_f32 v192, v192\n\t"
    "v_mfma_f32_16x16x32_bf16 v[140:143], v[112:115], v[16:19], v[156:159]\n\t"
    "s_add_u32 s72, s71, s63\n\t"
    "s_add_u32 s71, s71, 32768\n\t"
    "v_mfma_f32_16x16x32_bf16 v[136:139], v[100:103], v[20:23], v[136:139]\n\t"
    "v_exp_f32 v193, v193\n\t"
    "v_mfma_f32_16x16x32_bf16 v[140:143], v[116:119], v[20:23], v[140:143]\n\t"
    "v_cvt_pk_bf16_f32 v144, v192, v193\n\t"
    "buffer_load_dwordx4 v206, s[44:47], s62 offen offset:2048 nt lds\n\t"
    "v_mfma_f32_16x16x32_bf16 v[136:139], v[104:107], v[24:27], v[136:139]\n\t"
    "v_exp_f32 v194, v194\n\t"
    "v_mfma_f32_16x16x32_bf16 v[140:143], v[120:123], v[24:27], v[140:143]\n\t"
    "s_cmp_eq_u32 s71, 163840\n\t"
    "s_cselect_b32 s71, 0, s71\n\t"
    "v_mfma_f32_16x16x32_bf16 v[136:139], v[108:111], v[28:31], v[136:139]\n\t"
    "v_exp_f32 v195, v195\n\t"
    "v_mfma_f32_16x16x32_bf16 v[140:143], v[124:127], v[28:31], v[140:143]\n\t"
    "v_cvt_pk_bf16_f32 v145, v194, v195\n\t"
    "v_add_u32 v224, s71, v210\n\t"
    "v_mfma_f32_16x16x32_bf16 a[96:99], a[216:219], v[148:151], a[96:99]\n\t"
    "v_exp_f32 v196, v196\n\t"
    "v_mfma_f32_16x16x32_bf16 a[100:103], a[220:223], v[148:151], a[100:103]\n\t"
    "v_add_u32 v225, s71, v211\n\t"
    "v_mfma_f32_16x16x32_bf16 a[104:107], a[224:227], v[148:151], a[104:107]\n\t"
    "v_exp_f32 v197, v197\n\t"
    "v_mfma_f32_16x16x32_bf16 a[108:111], a[228:231], v[148:151], a[108:111]\n\t"
    "v_cvt_pk_bf16_f32 v146, v196, v197\n\t"
    "buffer_load_dwordx4 v207, s[44:47], s62 offen offset:3072 nt lds\n\t"
    "v_mfma_f32_16x16x32_bf16 a[112:115], a[232:235], v[148:151], a[112:115]\n\t"
    "v_exp_f32 v198, v198\n\t"
    "v_mfma_f32_16x16x32_bf16 a[116:119], a[236:239], v[148:151], a[116:119]\n\t"
    "v_max3_f32 v252, v252, v136, v137\n\t"
    "v_max3_f32 v252, v252, v138, v139\n\t"
    "v_mfma_f32_16x16x32_bf16 a[120:123], a[240:243], v[148:151], a[120:123]\n\t"
    "v_exp_f32 v199, v199\n\t"
    "v_mfma_f32_16x16x32_bf16 a[124:127], a[244:247], v[148:151], a[124:127]\n\t"
    "v_max3_f32 v252, v252, v140, v141\n\t"
    "v_max3_f32 v252, v252, v142, v143\n\t"
    "v_mfma_f32_16x16x32_bf16 a[204:207], a[248:251], v[148:151], a[204:207]\n\t"
    "v_cvt_pk_bf16_f32 v147, v198, v199\n\t"
    "s_addk_i32 m0, 8192\n\t"
    "v_mfma_f32_16x16x32_bf16 v[176:179], v[96:99], v[32:35], v[160:163]\n\t"
    "v_exp_f32 v200, v200\n\t"
    "v_mfma_f32_16x16x32_bf16 v[180:183], v[112:115], v[32:35], v[160:163]\n\t"
    "v_exp_f32 v201, v201\n\t"
    "v_mfma_f32_16x16x32_bf16 v[176:179], v[100:103], v[36:39], v[176:179]\n\t"
    "v_cvt_pk_bf16_f32 v148, v200, v201\n\t"
    "s_cmp_ge_i32 s74, 0\n\t"
    "v_mfma_f32_16x16x32_bf16 v[180:183], v[116:119], v[36:39], v[180:183]\n\t"
    "v_exp_f32 v202, v202\n\t"
    "v_mfma_f32_16x16x32_bf16 v[176:179], v[104:107], v[40:43], v[176:179]\n\t"
    "v_exp_f32 v203, v203\n\t"
    "v_mfma_f32_16x16x32_bf16 v[180:183], v[120:123], v[40:43], v[180:183]\n\t"
    "v_cvt_pk_bf16_f32 v149, v202, v203\n\t"
    "buffer_load_dwordx4 v208, s[48:51], s62 offen offset:0 nt lds\n\t"
    "v_mfma_f32_16x16x32_bf16 v[176:179], v[108:111], v[44:47], v[176:179]\n\t"
    "v_exp_f32 v248, v248\n\t"
    "v_mfma_f32_16x16x32_bf16 v[180:183], v[124:127], v[44:47], v[180:183]\n\t"
    "s_cselect_b32 s73, s62, s65\n\t"
    "s_cselect_b32 s32, s92, s32\n\t"
    "v_mfma_f32_16x16x32_bf16 a[128:131], a[216:219], v[144:147], a[128:131]\n\t"
    "v_exp_f32 v249, v249\n\t"
    "v_mfma_f32_16x16x32_bf16 a[132:135], a[220:223], v[144:147], a[132:135]\n\t"
    "v_cvt_pk_bf16_f32 v150, v248, v249\n\t"
    "s_add_u32 s32, s32, s33\n\t"
    "v_mfma_f32_16x16x32_bf16 a[136:139], a[224:227], v[144:147], a[136:139]\n\t"
    "v_exp_f32 v250, v250\n\t"
    "v_mfma_f32_16x16x32_bf16 a[140:143], a[228:231], v[144:147], a[140:143]\n\t"
    "v_exp_f32 v251, v251\n\t"
    "v_mfma_f32_16x16x32_bf16 a[144:147], a[232:235], v[144:147], a[144:147]\n\t"
    "v_max3_f32 v252, v252, v176, v177\n\t"
    "v_max3_f32 v252, v252, v178, v179\n\t"
    "v_mfma_f32_16x16x32_bf16 a[148:151], a[236:239], v[144:147], a[148:151]\n\t"
    "v_max3_f32 v252, v252, v180, v181\n\t"
    "v_max3_f32 v252, v252, v182, v183\n\t"
    "v_mfma_f32_16x16x32_bf16 a[152:155], a[240:243], v[144:147], a[152:155]\n\t"
    "v_cmp_lt_f32 vcc, s64, v252\n\t"
    "buffer_load_dwordx4 v209, s[48:51], s62 offen offset:1024 nt lds\n\t"
    "v_mfma_f32_16x16x32_bf16 a[156:159], a[244:247], v[144:147], a[156:159]\n\t"
    "v_cvt_pk_bf16_f32 v151, v250, v251\n\t"
    "v_mfma_f32_16x16x32_bf16 a[208:211], a[248:251], v[144:147], a[208:211]\n\t"
    "s_cbranch_vccnz .LSTUB_L_0_%=\n\t"
    ".LRET_L_0_%=:\n\t"
    "s_cmp_lg_u32 s73, s65\n\t"
    "s_cbranch_scc1 .LATL_%=\n\t"
    "s_cmp_eq_u32 s62, s65\n\t"
    "s_cbranch_scc1 .LATX_%=\n\t"
    "s_sub_u32 s74, s32, s33\n\t"
    "s_add_u32 s74, s74, 58\n\t"
    "s_mov_b32 s73, 4096\n\t"
    ".LATW_%=:\n\t"
    "s_memrealtime s[92:93]\n\t"
    "s_sub_u32 s73, s73, 1\n\t"
    "s_cmp_eq_u32 s73, 0\n\t"
    "s_waitcnt lgkmcnt(0)\n\t"
    "s_cbranch_scc1 .LATL_%=\n\t"
    "s_sub_u32 s92, s92, s74\n\t"
    "s_cmp_ge_i32 s92, 0\n\t"
    "s_cbranch_scc0 .LATW_%=\n\t"
    "s_branch .LATL_%=\n\t"
    ".LATX_%=:\n\t"
    "s_cmp_eq_u32 s65, 286720\n\t"
    "s_cbranch_scc1 .LATY_%=\n\t"
    "s_mov_b32 s65, 286720\n\t"
    "global_load_dword a255, v245, s[100:101]\n\t"
    "s_getpc_b64 s[68:69]\n\t"
    ".LCT4_%=:\n\t"
    "v_min_u32 v254, .LEND_%=-.LCT4_%=-4, v223\n\t"
    "global_load_dword a255, v254, s[68:69]\n\t"
    "buffer_load_dword a254, v220, s[24:27], 0 offen offset:0 sc1\n\t"
    "s_branch .LATL_%=\n\t"
    ".LATY_%=:\n\t"
    "s_waitcnt vmcnt(0)\n\t"
    "v_accvgpr_read_b32 v208, a252\n\t"
    "v_accvgpr_read_b32 v209, a253\n\t"
    "v_accvgpr_read_b32 v221, a254\n\t"
    "v_add_u32 v253, 1540483477, v208\n\t"
    "v_add_u32 v208, 1, v208\n\t"
    "v_add_u32 v209, 1, v209\n\t"
    "v_cmp_ne_u32 vcc, v221, v253\n\t"
    "s_nop 1\n\t"
    "s_and_b32 s73, vcc_lo, 0x1ff\n\t"
    "s_cbranch_scc0 .LACKOK_%=\n\t"
    "s_mov_b32 s74, 65536\n\t"
    ".LACK_%=:\n\t"
    "buffer_load_dword v221, v220, s[24:27], 0 offen offset:0 sc1\n\t"
    "s_waitcnt vmcnt(0)\n\t"
    "v_cmp_ne_u32 vcc, v221, v253\n\t"
    "s_nop 1\n\t"
    "s_and_b32 s73, vcc_lo, 0x1ff\n\t"
    "s_cbranch_scc0 .LACKOK_%=\n\t"
    "s_sleep 1\n\t"
    "s_sub_u32 s74, s74, 1\n\t"
    "s_cmp_lg_u32 s74, 0\n\t"
    "s_cbranch_scc1 .LACK_%=\n\t"
    ".LACKOK_%=:\n\t"
    "v_add_u32 v204, s31, v216\n\t"
    "v_xor_b32 v205, 65536, v204\n\t"
    "v_lshlrev_b32 v206, 2, v204\n\t"
    "v_and_b32 v206, 16384, v206\n\t"
    "v_and_b32 v207, 4294963199, v204\n\t"
    "v_or_b32 v206, v206, v207\n\t"
    "v_xor_b32 v207, 65536, v206\n\t"
    "v_mfma_f32_16x16x32_bf16 v[184:187], v[96:99], v[48:51], v[164:167]\n\t"
    "v_exp_f32 v128, v128\n\t"
    "v_mfma_f32_16x16x32_bf16 v[188:191], v[112:115], v[48:51], v[164:167]\n\t"
    "v_exp_f32 v129, v129\n\t"
    "v_mfma_f32_16x16x32_bf16 v[184:187], v[100:103], v[52:55], v[184:187]\n\t"
    "v_exp_f32 v130, v130\n\t"
    "v_mfma_f32_16x16x32_bf16 v[188:191], v[116:119], v[52:55], v[188:191]\n\t"
    "v_exp_f32 v131, v131\n\t"
    "v_mfma_f32_16x16x32_bf16 v[184:187], v[104:107], v[56:59], v[184:187]\n\t"
    "v_exp_f32 v132, v132\n\t"
    "v_mfma_f32_16x16x32_bf16 v[188:191], v[120:123], v[56:59], v[188:191]\n\t"
    "v_exp_f32 v133, v133\n\t"
    "v_mfma_f32_16x16x32_bf16 v[184:187], v[108:111], v[60:63], v[184:187]\n\t"
    "v_exp_f32 v134, v134\n\t"
    "v_mfma_f32_16x16x32_bf16 v[188:191], v[124:127], v[60:63], v[188:191]\n\t"
    "v_exp_f32 v135, v135\n\t"
    "v_mfma_f32_16x16x32_bf16 a[160:163], a[216:219], v[148:151], a[160:163]\n\t"
    "ds_read_b64_tr_b16 a[216:217], v228 offset:0\n\t"
    "v_cvt_pk_bf16_f32 v144, v128, v129\n\t"
    "v_mfma_f32_16x16x32_bf16 a[164:167], a[220:223], v[148:151], a[164:167]\n\t"
    "ds_read_b64_tr_b16 a[218:219], v228 offset:4096\n\t"
    "v_cvt_pk_bf16_f32 v145, v130, v131\n\t"
    "v_mfma_f32_16x16x32_bf16 a[168:171], a[224:227], v[148:151], a[168:171]\n\t"
    "ds_read_b64_tr_b16 a[220:221], v229 offset:0\n\t"
    "v_cvt_pk_bf16_f32 v146, v132, v133\n\t"
    "v_mfma_f32_16x16x32_bf16 a[172:175], a[228:231], v[148:151], a[172:175]\n\t"
    "ds_read_b64_tr_b16 a[222:223], v229 offset:4096\n\t"
    "v_cvt_pk_bf16_f32 v147, v134, v135\n\t"
    "v_mfma_f32_16x16x32_bf16 a[176:179], a[232:235], v[148:151], a[176:179]\n\t"
    "ds_read_b64_tr_b16 a[224:225], v228 offset:256\n\t"
    "v_max3_f32 v252, v184, v185, v186\n\t"
    "v_mfma_f32_16x16x32_bf16 a[180:183], a[236:239], v[148:151], a[180:183]\n\t"
    "ds_read_b64_tr_b16 a[226:227], v228 offset:4352\n\t"
    "v_max3_f32 v252, v252, v187, v188\n\t"
    "v_mfma_f32_16x16x32_bf16 a[184:187], a[240:243], v[148:151], a[184:187]\n\t"
    "ds_read_b64_tr_b16 a[228:229], v229 offset:256\n\t"
    "v_max3_f32 v252, v252, v189, v190\n\t"
    "v_mfma_f32_16x16x32_bf16 a[188:191], a[244:247], v[148:151], a[188:191]\n\t"
    "ds_read_b64_tr_b16 a[230:231], v229 offset:4352\n\t"
    "v_max_f32 v252, v252, v191\n\t"
    "v_mfma_f32_16x16x32_bf16 a[212:215], a[248:251], v[148:151], a[212:215]\n\t"
    "ds_read_b64_tr_b16 a[232:233], v228 offset:512\n\t"
    "ds_read_b64_tr_b16 a[234:235], v228 offset:4608\n\t"
    "v_mfma_f32_16x16x32_bf16 v[192:195], v[96:99], v[64:67], v[168:171]\n\t"
    "ds_read_b64_tr_b16 a[236:237], v229 offset:512\n\t"
    "ds_read_b64_tr_b16 a[238:239], v229 offset:4608\n\t"
    "v_mfma_f32_16x16x32_bf16 v[196:199], v[112:115], v[64:67], v[168:171]\n\t"
    "ds_read_b64_tr_b16 a[240:241], v228 offset:768\n\t"
    "ds_read_b64_tr_b16 a[242:243], v228 offset:4864\n\t"
    "v_mfma_f32_16x16x32_bf16 v[192:195], v[100:103], v[68:71], v[192:195]\n\t"
    "ds_read_b64_tr_b16 a[244:245], v229 offset:768\n\t"
    "ds_read_b64_tr_b16 a[246:247], v229 offset:4864\n\t"
    "v_mfma_f32_16x16x32_bf16 v[196:199], v[116:119], v[68:71], v[196:199]\n\t"
    "v_exp_f32 v136, v136\n\t"
    "v_mfma_f32_16x16x32_bf16 v[192:195], v[104:107], v[72:75], v[192:195]\n\t"
    "v_exp_f32 v137, v137\n\t"
    "v_mfma_f32_16x16x32_bf16 v[196:199], v[120:123], v[72:75], v[196:199]\n\t"
    "v_exp_f32 v138, v138\n\t"
    "v_mfma_f32_16x16x32_bf16 v[192:195], v[108:111], v[76:79], v[192:195]\n\t"
    "v_exp_f32 v139, v139\n\t"
    "v_mfma_f32_16x16x32_bf16 v[196:199], v[124:127], v[76:79], v[196:199]\n\t"
    "s_waitcnt lgkmcnt(6)\n\t"
    "v_cvt_pk_bf16_f32 v148, v136, v137\n\t"
    "v_mfma_f32_16x16x32_bf16 a[0:3], a[216:219], v[144:147], a[0:3]\n\t"
    "v_exp_f32 v140, v140\n\t"
    "v_mfma_f32_16x16x32_bf16 a[4:7], a[220:223], v[144:147], a[4:7]\n\t"
    "v_exp_f32 v141, v141\n\t"
    "v_mfma_f32_16x16x32_bf16 a[8:11], a[224:227], v[144:147], a[8:11]\n\t"
    "v_exp_f32 v142, v142\n\t"
    "v_mfma_f32_16x16x32_bf16 a[12:15], a[228:231], v[144:147], a[12:15]\n\t"
    "v_exp_f32 v143, v143\n\t"
    "v_mfma_f32_16x16x32_bf16 a[16:19], a[232:235], v[144:147], a[16:19]\n\t"
    "s_waitcnt lgkmcnt(0)\n\t"
    "v_cvt_pk_bf16_f32 v149, v138, v139\n\t"
    "v_mfma_f32_16x16x32_bf16 a[20:23], a[236:239], v[144:147], a[20:23]\n\t"
    "v_cvt_pk_bf16_f32 v150, v140, v141\n\t"
    "v_cvt_pk_bf16_f32 v151, v142, v143\n\t"
    "v_mfma_f32_16x16x32_bf16 a[24:27], a[240:243], v[144:147], a[24:27]\n\t"
    "v_max3_f32 v252, v252, v192, v193\n\t"
    "v_max3_f32 v252, v252, v194, v195\n\t"
    "v_mfma_f32_16x16x32_bf16 a[28:31], a[244:247], v[144:147], a[28:31]\n\t"
    "v_max3_f32 v252, v252, v196, v197\n\t"
    "v_max3_f32 v252, v252, v198, v199\n\t"
    "v_mfma_f32_16x16x32_bf16 a[192:195], a[248:251], v[144:147], a[192:195]\n\t"
    "s_barrier\n\t"
    "v_mfma_f32_16x16x32_bf16 v[200:203], v[96:99], v[80:83], v[172:175]\n\t"
    "v_exp_f32 v176, v176\n\t"
    "v_mfma_f32_16x16x32_bf16 v[248:251], v[112:115], v[80:83], v[172:175]\n\t"
    "v_mfma_f32_16x16x32_bf16 v[200:203], v[100:103], v[84:87], v[200:203]\n\t"
    "v_exp_f32 v177, v177\n\t"
    "v_mfma_f32_16x16x32_bf16 v[248:251], v[116:119], v[84:87], v[248:251]\n\t"
    "s_nop 0\n\t"
    "v_accvgpr_read_b32 v160, a192\n\t"
    "v_rcp_f32 v160, v160\n\t"
    "v_accvgpr_read_b32 v128, a0\n\t"
    "v_accvgpr_read_b32 v129, a1\n\t"
    "v_accvgpr_read_b32 v130, a2\n\t"
    "v_accvgpr_read_b32 v131, a3\n\t"
    "v_mfma_f32_16x16x32_bf16 v[200:203], v[104:107], v[88:91], v[200:203]\n\t"
    "s_mov_b32 s94, 0\n\t"
    "v_accvgpr_read_b32 v132, a4\n\t"
    "v_accvgpr_read_b32 v133, a5\n\t"
    "v_accvgpr_read_b32 v134, a6\n\t"
    "v_accvgpr_read_b32 v135, a7\n\t"
    "v_pk_mul_f32 v[128:129], v[128:129], v[160:161] op_sel_hi:[1,0]\n\t"
    "v_exp_f32 v178, v178\n\t"
    "v_mfma_f32_16x16x32_bf16 v[248:251], v[120:123], v[88:91], v[248:251]\n\t"
    "v_pk_mul_f32 v[130:131], v[130:131], v[160:161] op_sel_hi:[1,0]\n\t"
    "s_mov_b32 s95, 0\n\t"
    "v_pk_mul_f32 v[132:133], v[132:133], v[160:161] op_sel_hi:[1,0]\n\t"
    "v_pk_mul_f32 v[134:135], v[134:135], v[160:161] op_sel_hi:[1,0]\n\t"
    "v_cvt_pk_bf16_f32 v0, v128, v129\n\t"
    "v_cvt_pk_bf16_f32 v1, v130, v131\n\t"
    "v_mfma_f32_16x16x32_bf16 v[200:203], v[108:111], v[92:95], v[200:203]\n\t"
    "v_cvt_pk_bf16_f32 v2, v132, v133\n\t"
    "v_cvt_pk_bf16_f32 v3, v134, v135\n\t"
    "s_add_u32 m0, s31, 0\n\t"
    "v_accvgpr_read_b32 v136, a8\n\t"
    "s_mov_b32 s72, 0\n\t"
    "buffer_store_dwordx4 v[0:3], v216, s[52:55], s72 offen offset:0 sc1\n\t"
    "v_accvgpr_read_b32 v137, a9\n\t"
    "v_exp_f32 v179, v179\n\t"
    "v_mfma_f32_16x16x32_bf16 v[248:251], v[124:127], v[92:95], v[248:251]\n\t"
    "v_accvgpr_read_b32 v138, a10\n\t"
    "v_accvgpr_read_b32 v139, a11\n\t"
    "v_accvgpr_read_b32 v140, a12\n\t"
    "buffer_load_dwordx4 v232, s[12:15], s28 offen offset:0 lds\n\t"
    "v_accvgpr_read_b32 v141, a13\n\t"
    "v_accvgpr_read_b32 v142, a14\n\t"
    "v_mfma_f32_16x16x32_bf16 a[32:35], a[216:219], v[148:151], a[32:35]\n\t"
    "v_accvgpr_read_b32 v143, a15\n\t"
    "v_pk_mul_f32 v[136:137], v[136:137], v[160:161] op_sel_hi:[1,0]\n\t"
    "v_pk_mul_f32 v[138:139], v[138:139], v[160:161] op_sel_hi:[1,0]\n\t"
    "v_pk_mul_f32 v[140:141], v[140:141], v[160:161] op_sel_hi:[1,0]\n\t"
    "buffer_load_dwordx4 v233, s[12:15], s28 offen offset:1024 lds\n\t"
    "v_pk_mul_f32 v[142:143], v[142:143], v[160:161] op_sel_hi:[1,0]\n\t"
    "v_exp_f32 v180, v180\n\t"
    "v_mfma_f32_16x16x32_bf16 a[36:39], a[220:223], v[148:151], a[36:39]\n\t"
    "v_cvt_pk_bf16_f32 v24, v136, v137\n\t"
    "v_cvt_pk_bf16_f32 v25, v138, v139\n\t"
    "v_cvt_pk_bf16_f32 v26, v140, v141\n\t"
    "v_cvt_pk_bf16_f32 v27, v142, v143\n\t"
    "buffer_load_dwordx4 v234, s[12:15], s28 offen offset:2048 lds\n\t"
    "v_exp_f32 v181, v181\n\t"
    "v_mfma_f32_16x16x32_bf16 a[40:43], a[224:227], v[148:151], a[40:43]\n\t"
    "v_accvgpr_read_b32 v152, a16\n\t"
    "v_accvgpr_read_b32 v153, a17\n\t"
    "v_accvgpr_read_b32 v154, a18\n\t"
    "v_accvgpr_read_b32 v155, a19\n\t"
    "v_accvgpr_read_b32 v156, a20\n\t"
    "v_accvgpr_read_b32 v157, a21\n\t"
    "s_mov_b32 s72, 4096\n\t"
    "buffer_store_dwordx4 v[24:27], v216, s[52:55], s72 offen offset:2048 sc1\n\t"
    "v_exp_f32 v182, v182\n\t"
    "v_mfma_f32_16x16x32_bf16 a[44:47], a[228:231], v[148:151], a[44:47]\n\t"
    "buffer_load_dwordx4 v235, s[12:15], s28 offen offset:3072 lds\n\t"
    "v_accvgpr_read_b32 v158, a22\n\t"
    "v_accvgpr_read_b32 v159, a23\n\t"
    "v_pk_mul_f32 v[152:153], v[152:153], v[160:161] op_sel_hi:[1,0]\n\t"
    "v_pk_mul_f32 v[154:155], v[154:155], v[160:161] op_sel_hi:[1,0]\n\t"
    "v_pk_mul_f32 v[156:157], v[156:157], v[160:161] op_sel_hi:[1,0]\n\t"
    "v_exp_f32 v183, v183\n\t"
    "v_mfma_f32_16x16x32_bf16 a[48:51], a[232:235], v[148:151], a[48:51]\n\t"
    "v_pk_mul_f32 v[158:159], v[158:159], v[160:161] op_sel_hi:[1,0]\n\t"
    "v_cvt_pk_bf16_f32 v48, v152, v153\n\t"
    "v_cvt_pk_bf16_f32 v49, v154, v155\n\t"
    "v_cvt_pk_bf16_f32 v50, v156, v157\n\t"
    "v_cvt_pk_bf16_f32 v51, v158, v159\n\t"
    "v_max3_f32 v252, v252, v200, v201\n\t"
    "v_max3_f32 v252, v252, v202, v203\n\t"
    "v_mfma_f32_16x16x32_bf16 a[52:55], a[236:239], v[148:151], a[52:55]\n\t"
    "v_accvgpr_read_b32 v128, a24\n\t"
    "v_accvgpr_read_b32 v129, a25\n\t"
    "v_accvgpr_read_b32 v130, a26\n\t"
    "v_accvgpr_read_b32 v131, a27\n\t"
    "v_accvgpr_read_b32 v132, a28\n\t"
    "v_accvgpr_read_b32 v133, a29\n\t"
    "v_max3_f32 v252, v252, v248, v249\n\t"
    "v_max3_f32 v252, v252, v250, v251\n\t"
    "v_mfma_f32_16x16x32_bf16 a[56:59], a[240:243], v[148:151], a[56:59]\n\t"
    "v_accvgpr_read_b32 v134, a30\n\t"
    "v_accvgpr_read_b32 v135, a31\n\t"
    "v_pk_mul_f32 v[128:129], v[128:129], v[160:161] op_sel_hi:[1,0]\n\t"
    "v_pk_mul_f32 v[130:131], v[130:131], v[160:161] op_sel_hi:[1,0]\n\t"
    "v_pk_mul_f32 v[132:133], v[132:133], v[160:161] op_sel_hi:[1,0]\n\t"
    "s_mov_b32 s72, 12288\n\t"
    "buffer_store_dwordx4 v[48:51], v216, s[52:55], s72 offen offset:0 sc1\n\t"
    "v_pk_mul_f32 v[134:135], v[134:135], v[160:161] op_sel_hi:[1,0]\n\t"
    "v_cmp_lt_f32 vcc, s64, v252\n\t"
    "v_cvt_pk_bf16_f32 v144, v176, v177\n\t"
    "v_mfma_f32_16x16x32_bf16 a[60:63], a[244:247], v[148:151], a[60:63]\n\t"
    "v_cvt_pk_bf16_f32 v72, v128, v129\n\t"
    "v_cvt_pk_bf16_f32 v73, v130, v131\n\t"
    "v_cvt_pk_bf16_f32 v74, v132, v133\n\t"
    "v_cvt_pk_bf16_f32 v75, v134, v135\n\t"
    "v_cvt_pk_bf16_f32 v145, v178, v179\n\t"
    "v_cvt_pk_bf16_f32 v146, v180, v181\n\t"
    "v_mfma_f32_16x16x32_bf16 a[196:199], a[248:251], v[148:151], a[196:199]\n\t"
    "v_cvt_pk_bf16_f32 v147, v182, v183\n\t"
    "s_cbranch_vccnz .LSTUB_Z_1_%=\n\t"
    ".LRET_Z_1_%=:\n\t"
    "s_nop 0\n\t"
    "v_mfma_f32_16x16x32_bf16 a[64:67], a[216:219], v[144:147], a[64:67]\n\t"
    "v_exp_f32 v184, v184\n\t"
    "v_mfma_f32_16x16x32_bf16 a[68:71], a[220:223], v[144:147], a[68:71]\n\t"
    "v_exp_f32 v185, v185\n\t"
    "s_nop 0\n\t"
    "v_accvgpr_read_b32 v162, a196\n\t"
    "v_rcp_f32 v162, v162\n\t"
    "v_accvgpr_read_b32 v136, a32\n\t"
    "v_accvgpr_read_b32 v137, a33\n\t"
    "v_accvgpr_read_b32 v138, a34\n\t"
    "v_accvgpr_read_b32 v139, a35\n\t"
    "v_cvt_pk_bf16_f32 v148, v184, v185\n\t"
    "v_mfma_f32_16x16x32_bf16 a[72:75], a[224:227], v[144:147], a[72:75]\n\t"
    "s_add_u32 s28, s28, 65536\n\t"
    "v_accvgpr_read_b32 v140, a36\n\t"
    "v_accvgpr_read_b32 v141, a37\n\t"
    "v_accvgpr_read_b32 v142, a38\n\t"
    "v_accvgpr_read_b32 v143, a39\n\t"
    "s_mov_b32 s72, 16384\n\t"
    "buffer_store_dwordx4 v[72:75], v216, s[52:55], s72 offen offset:2048 sc1\n\t"
    "v_pk_mul_f32 v[136:137], v[136:137], v[162:163] op_sel_hi:[1,0]\n\t"
    "v_exp_f32 v186, v186\n\t"
    "v_pk_mul_f32 v[138:139], v[138:139], v[162:163] op_sel_hi:[1,0]\n\t"
    "s_add_u32 m0, s31, 8192\n\t"
    "v_pk_mul_f32 v[140:141], v[140:141], v[162:163] op_sel_hi:[1,0]\n\t"
    "v_pk_mul_f32 v[142:143], v[142:143], v[162:163] op_sel_hi:[1,0]\n\t"
    "v_cvt_pk_bf16_f32 v4, v136, v137\n\t"
    "v_cvt_pk_bf16_f32 v5, v138, v139\n\t"
    "v_mfma_f32_16x16x32_bf16 a[76:79], a[228:231], v[144:147], a[76:79]\n\t"
    "v_cvt_pk_bf16_f32 v6, v140, v141\n\t"
    "v_cvt_pk_bf16_f32 v7, v142, v143\n\t"
    "buffer_load_dwordx4 v232, s[12:15], s28 offen offset:0 lds\n\t"
    "v_accvgpr_read_b32 v152, a40\n\t"
    "v_accvgpr_read_b32 v153, a41\n\t"
    "v_exp_f32 v187, v187\n\t"
    "v_accvgpr_read_b32 v154, a42\n\t"
    "v_accvgpr_read_b32 v155, a43\n\t"
    "v_accvgpr_read_b32 v156, a44\n\t"
    "buffer_load_dwordx4 v233, s[12:15], s28 offen offset:1024 lds\n\t"
    "v_accvgpr_read_b32 v157, a45\n\t"
    "v_accvgpr_read_b32 v158, a46\n\t"
    "v_cvt_pk_bf16_f32 v149, v186, v187\n\t"
    "v_mfma_f32_16x16x32_bf16 a[80:83], a[232:235], v[144:147], a[80:83]\n\t"
    "v_accvgpr_read_b32 v159, a47\n\t"
    "v_pk_mul_f32 v[152:153], v[152:153], v[162:163] op_sel_hi:[1,0]\n\t"
    "v_pk_mul_f32 v[154:155], v[154:155], v[162:163] op_sel_hi:[1,0]\n\t"
    "v_pk_mul_f32 v[156:157], v[156:157], v[162:163] op_sel_hi:[1,0]\n\t"
    "buffer_load_dwordx4 v234, s[12:15], s28 offen offset:2048 lds\n\t"
    "v_pk_mul_f32 v[158:159], v[158:159], v[162:163] op_sel_hi:[1,0]\n\t"
    "v_exp_f32 v188, v188\n\t"
    "v_cvt_pk_bf16_f32 v28, v152, v153\n\t"
    "v_cvt_pk_bf16_f32 v29, v154, v155\n\t"
    "v_cvt_pk_bf16_f32 v30, v156, v157\n\t"
    "s_mov_b32 s72, 0\n\t"
    "buffer_store_dwordx4 v[4:7], v216, s[52:55], s72 offen offset:1024 sc1\n\t"
    "v_cvt_pk_bf16_f32 v31, v158, v159\n\t"
    "buffer_load_dwordx4 v235, s[12:15], s28 offen offset:3072 lds\n\t"
    "v_mfma_f32_16x16x32_bf16 a[84:87], a[236:239], v[144:147], a[84:87]\n\t"
    "v_accvgpr_read_b32 v128, a48\n\t"
    "v_accvgpr_read_b32 v129, a49\n\t"
    "v_accvgpr_read_b32 v130, a50\n\t"
    "v_accvgpr_read_b32 v131, a51\n\t"
    "v_accvgpr_read_b32 v132, a52\n\t"
    "v_accvgpr_read_b32 v133, a53\n\t"
    "v_exp_f32 v189, v189\n\t"
    "s_add_u32 s28, s28, 65536\n\t"
    "v_accvgpr_read_b32 v134, a54\n\t"
    "v_accvgpr_read_b32 v135, a55\n\t"
    "v_pk_mul_f32 v[128:129], v[128:129], v[162:163] op_sel_hi:[1,0]\n\t"
    "v_pk_mul_f32 v[130:131], v[130:131], v[162:163] op_sel_hi:[1,0]\n\t"
    "v_pk_mul_f32 v[132:133], v[132:133], v[162:163] op_sel_hi:[1,0]\n\t"
    "v_cvt_pk_bf16_f32 v150, v188, v189\n\t"
    "v_mfma_f32_16x16x32_bf16 a[88:91], a[240:243], v[144:147], a[88:91]\n\t"
    "v_pk_mul_f32 v[134:135], v[134:135], v[162:163] op_sel_hi:[1,0]\n\t"
    "v_cvt_pk_bf16_f32 v52, v128, v129\n\t"
    "v_cvt_pk_bf16_f32 v53, v130, v131\n\t"
    "v_cvt_pk_bf16_f32 v54, v132, v133\n\t"
    "v_cvt_pk_bf16_f32 v55, v134, v135\n\t"
    "v_exp_f32 v190, v190\n\t"
    "v_accvgpr_read_b32 v136, a56\n\t"
    "v_accvgpr_read_b32 v137, a57\n\t"
    "v_accvgpr_read_b32 v138, a58\n\t"
    "v_accvgpr_read_b32 v139, a59\n\t"
    "v_accvgpr_read_b32 v140, a60\n\t"
    "v_accvgpr_read_b32 v141, a61\n\t"
    "v_mfma_f32_16x16x32_bf16 a[92:95], a[244:247], v[144:147], a[92:95]\n\t"
    "v_accvgpr_read_b32 v142, a62\n\t"
    "v_accvgpr_read_b32 v143, a63\n\t"
    "s_mov_b32 s72, 4096\n\t"
    "buffer_store_dwordx4 v[28:31], v216, s[52:55], s72 offen offset:3072 sc1\n\t"
    "v_pk_mul_f32 v[136:137], v[136:137], v[162:163] op_sel_hi:[1,0]\n\t"
    "v_pk_mul_f32 v[138:139], v[138:139], v[162:163] op_sel_hi:[1,0]\n\t"
    "v_pk_mul_f32 v[140:141], v[140:141], v[162:163] op_sel_hi:[1,0]\n\t"
    "v_pk_mul_f32 v[142:143], v[142:143], v[162:163] op_sel_hi:[1,0]\n\t"
    "v_exp_f32 v191, v191\n\t"
    "v_cvt_pk_bf16_f32 v76, v136, v137\n\t"
    "v_cvt_pk_bf16_f32 v77, v138, v139\n\t"
    "v_cvt_pk_bf16_f32 v78, v140, v141\n\t"
    "v_cvt_pk_bf16_f32 v79, v142, v143\n\t"
    "v_mfma_f32_16x16x32_bf16 a[200:203], a[248:251], v[144:147], a[200:203]\n\t"
    "v_cvt_pk_bf16_f32 v151, v190, v191\n\t"
    "s_nop 1\n\t"
    "v_mfma_f32_16x16x32_bf16 a[96:99], a[216:219], v[148:151], a[96:99]\n\t"
    "v_exp_f32 v192, v192\n\t"
    "v_mfma_f32_16x16x32_bf16 a[100:103], a[220:223], v[148:151], a[100:103]\n\t"
    "v_exp_f32 v193, v193\n\t"
    "s_nop 0\n\t"
    "v_accvgpr_read_b32 v160, a200\n\t"
    "v_rcp_f32 v160, v160\n\t"
    "v_accvgpr_read_b32 v152, a64\n\t"
    "v_accvgpr_read_b32 v153, a65\n\t"
    "v_accvgpr_read_b32 v154, a66\n\t"
    "v_accvgpr_read_b32 v155, a67\n\t"
    "v_cvt_pk_bf16_f32 v144, v192, v193\n\t"
    "v_mfma_f32_16x16x32_bf16 a[104:107], a[224:227], v[148:151], a[104:107]\n\t"
    "s_add_u32 m0, s31, 16384\n\t"
    "v_accvgpr_read_b32 v156, a68\n\t"
    "v_accvgpr_read_b32 v157, a69\n\t"
    "v_accvgpr_read_b32 v158, a70\n\t"
    "v_accvgpr_read_b32 v159, a71\n\t"
    "v_pk_mul_f32 v[152:153], v[152:153], v[160:161] op_sel_hi:[1,0]\n\t"
    "s_mov_b32 s72, 12288\n\t"
    "buffer_store_dwordx4 v[52:55], v216, s[52:55], s72 offen offset:1024 sc1\n\t"
    "v_exp_f32 v194, v194\n\t"
    "v_pk_mul_f32 v[154:155], v[154:155], v[160:161] op_sel_hi:[1,0]\n\t"
    "buffer_load_dwordx4 v232, s[12:15], s28 offen offset:0 lds\n\t"
    "v_pk_mul_f32 v[156:157], v[156:157], v[160:161] op_sel_hi:[1,0]\n\t"
    "v_pk_mul_f32 v[158:159], v[158:159], v[160:161] op_sel_hi:[1,0]\n\t"
    "v_cvt_pk_bf16_f32 v8, v152, v153\n\t"
    "v_cvt_pk_bf16_f32 v9, v154, v155\n\t"
    "v_mfma_f32_16x16x32_bf16 a[108:111], a[228:231], v[148:151], a[108:111]\n\t"
    "v_cvt_pk_bf16_f32 v10, v156, v157\n\t"
    "v_cvt_pk_bf16_f32 v11, v158, v159\n\t"
    "buffer_load_dwordx4 v233, s[12:15], s28 offen offset:1024 lds\n\t"
    "v_accvgpr_read_b32 v128, a72\n\t"
    "v_accvgpr_read_b32 v129, a73\n\t"
    "v_exp_f32 v195, v195\n\t"
    "v_accvgpr_read_b32 v130, a74\n\t"
    "v_accvgpr_read_b32 v131, a75\n\t"
    "v_accvgpr_read_b32 v132, a76\n\t"
    "buffer_load_dwordx4 v234, s[12:15], s28 offen offset:2048 lds\n\t"
    "v_accvgpr_read_b32 v133, a77\n\t"
    "v_accvgpr_read_b32 v134, a78\n\t"
    "v_cvt_pk_bf16_f32 v145, v194, v195\n\t"
    "v_mfma_f32_16x16x32_bf16 a[112:115], a[232:235], v[148:151], a[112:115]\n\t"
    "v_accvgpr_read_b32 v135, a79\n\t"
    "v_pk_mul_f32 v[128:129], v[128:129], v[160:161] op_sel_hi:[1,0]\n\t"
    "v_pk_mul_f32 v[130:131], v[130:131], v[160:161] op_sel_hi:[1,0]\n\t"
    "v_pk_mul_f32 v[132:133], v[132:133], v[160:161] op_sel_hi:[1,0]\n\t"
    "buffer_load_dwordx4 v235, s[12:15], s28 offen offset:3072 lds\n\t"
    "v_pk_mul_f32 v[134:135], v[134:135], v[160:161] op_sel_hi:[1,0]\n\t"
    "v_exp_f32 v196, v196\n\t"
    "v_cvt_pk_bf16_f32 v32, v128, v129\n\t"
    "v_cvt_pk_bf16_f32 v33, v130, v131\n\t"
    "v_cvt_pk_bf16_f32 v34, v132, v133\n\t"
    "v_cvt_pk_bf16_f32 v35, v134, v135\n\t"
    "s_mov_b32 s72, 16384\n\t"
    "buffer_store_dwordx4 v[76:79], v216, s[52:55], s72 offen offset:3072 sc1\n\t"
    "s_add_u32 s28, s28, 65536\n\t"
    "v_mfma_f32_16x16x32_bf16 a[116:119], a[236:239], v[148:151], a[116:119]\n\t"
    "v_accvgpr_read_b32 v136, a80\n\t"
    "v_accvgpr_read_b32 v137, a81\n\t"
    "v_accvgpr_read_b32 v138, a82\n\t"
    "v_accvgpr_read_b32 v139, a83\n\t"
    "v_accvgpr_read_b32 v140, a84\n\t"
    "v_accvgpr_read_b32 v141, a85\n\t"
    "v_exp_f32 v197, v197\n\t"
    "s_add_u32 m0, s31, 24576\n\t"
    "v_accvgpr_read_b32 v142, a86\n\t"
    "v_accvgpr_read_b32 v143, a87\n\t"
    "v_pk_mul_f32 v[136:137], v[136:137], v[160:161] op_sel_hi:[1,0]\n\t"
    "v_pk_mul_f32 v[138:139], v[138:139], v[160:161] op_sel_hi:[1,0]\n\t"
    "v_pk_mul_f32 v[140:141], v[140:141], v[160:161] op_sel_hi:[1,0]\n\t"
    "v_cvt_pk_bf16_f32 v146, v196, v197\n\t"
    "v_mfma_f32_16x16x32_bf16 a[120:123], a[240:243], v[148:151], a[120:123]\n\t"
    "v_pk_mul_f32 v[142:143], v[142:143], v[160:161] op_sel_hi:[1,0]\n\t"
    "v_cvt_pk_bf16_f32 v56, v136, v137\n\t"
    "v_cvt_pk_bf16_f32 v57, v138, v139\n\t"
    "v_cvt_pk_bf16_f32 v58, v140, v141\n\t"
    "v_cvt_pk_bf16_f32 v59, v142, v143\n\t"
    "v_exp_f32 v198, v198\n\t"
    "v_accvgpr_read_b32 v152, a88\n\t"
    "v_accvgpr_read_b32 v153, a89\n\t"
    "v_accvgpr_read_b32 v154, a90\n\t"
    "v_accvgpr_read_b32 v155, a91\n\t"
    "v_accvgpr_read_b32 v156, a92\n\t"
    "v_accvgpr_read_b32 v157, a93\n\t"
    "v_mfma_f32_16x16x32_bf16 a[124:127], a[244:247], v[148:151], a[124:127]\n\t"
    "v_accvgpr_read_b32 v158, a94\n\t"
    "v_accvgpr_read_b32 v159, a95\n\t"
    "v_pk_mul_f32 v[152:153], v[152:153], v[160:161] op_sel_hi:[1,0]\n\t"
    "s_mov_b32 s72, 0\n\t"
    "buffer_store_dwordx4 v[8:11], v216, s[52:55], s72 offen offset:2048 sc1\n\t"
    "v_pk_mul_f32 v[154:155], v[154:155], v[160:161] op_sel_hi:[1,0]\n\t"
    "v_pk_mul_f32 v[156:157], v[156:157], v[160:161] op_sel_hi:[1,0]\n\t"
    "v_pk_mul_f32 v[158:159], v[158:159], v[160:161] op_sel_hi:[1,0]\n\t"
    "v_exp_f32 v199, v199\n\t"
    "v_cvt_pk_bf16_f32 v80, v152, v153\n\t"
    "v_cvt_pk_bf16_f32 v81, v154, v155\n\t"
    "v_cvt_pk_bf16_f32 v82, v156, v157\n\t"
    "v_cvt_pk_bf16_f32 v83, v158, v159\n\t"
    "v_mfma_f32_16x16x32_bf16 a[204:207], a[248:251], v[148:151], a[204:207]\n\t"
    "v_cvt_pk_bf16_f32 v147, v198, v199\n\t"
    "s_nop 1\n\t"
    "v_mfma_f32_16x16x32_bf16 a[128:131], a[216:219], v[144:147], a[128:131]\n\t"
    "v_exp_f32 v200, v200\n\t"
    "v_mfma_f32_16x16x32_bf16 a[132:135], a[220:223], v[144:147], a[132:135]\n\t"
    "v_exp_f32 v201, v201\n\t"
    "s_nop 0\n\t"
    "v_accvgpr_read_b32 v162, a204\n\t"
    "v_rcp_f32 v162, v162\n\t"
    "v_accvgpr_read_b32 v128, a96\n\t"
    "v_accvgpr_read_b32 v129, a97\n\t"
    "v_accvgpr_read_b32 v130, a98\n\t"
    "v_accvgpr_read_b32 v131, a99\n\t"
    "v_cvt_pk_bf16_f32 v148, v200, v201\n\t"
    "v_mfma_f32_16x16x32_bf16 a[136:139], a[224:227], v[144:147], a[136:139]\n\t"
    "buffer_load_dwordx4 v232, s[12:15], s28 offen offset:0 lds\n\t"
    "v_accvgpr_read_b32 v132, a100\n\t"
    "v_accvgpr_read_b32 v133, a101\n\t"
    "v_accvgpr_read_b32 v134, a102\n\t"
    "v_accvgpr_read_b32 v135, a103\n\t"
    "v_pk_mul_f32 v[128:129], v[128:129], v[162:163] op_sel_hi:[1,0]\n\t"
    "v_exp_f32 v202, v202\n\t"
    "s_mov_b32 s72, 8192\n\t"
    "buffer_store_dwordx4 v[32:35], v216, s[52:55], s72 offen offset:0 sc1\n\t"
    "v_pk_mul_f32 v[130:131], v[130:131], v[162:163] op_sel_hi:[1,0]\n\t"
    "buffer_load_dwordx4 v233, s[12:15], s28 offen offset:1024 lds\n\t"
    "v_pk_mul_f32 v[132:133], v[132:133], v[162:163] op_sel_hi:[1,0]\n\t"
    "v_pk_mul_f32 v[134:135], v[134:135], v[162:163] op_sel_hi:[1,0]\n\t"
    "v_cvt_pk_bf16_f32 v12, v128, v129\n\t"
    "v_cvt_pk_bf16_f32 v13, v130, v131\n\t"
    "v_mfma_f32_16x16x32_bf16 a[140:143], a[228:231], v[144:147], a[140:143]\n\t"
    "v_cvt_pk_bf16_f32 v14, v132, v133\n\t"
    "v_cvt_pk_bf16_f32 v15, v134, v135\n\t"
    "buffer_load_dwordx4 v234, s[12:15], s28 offen offset:2048 lds\n\t"
    "v_accvgpr_read_b32 v136, a104\n\t"
    "v_accvgpr_read_b32 v137, a105\n\t"
    "v_exp_f32 v203, v203\n\t"
    "v_accvgpr_read_b32 v138, a106\n\t"
    "v_accvgpr_read_b32 v139, a107\n\t"
    "v_accvgpr_read_b32 v140, a108\n\t"
    "buffer_load_dwordx4 v235, s[12:15], s28 offen offset:3072 lds\n\t"
    "v_accvgpr_read_b32 v141, a109\n\t"
    "v_accvgpr_read_b32 v142, a110\n\t"
    "v_cvt_pk_bf16_f32 v149, v202, v203\n\t"
    "v_mfma_f32_16x16x32_bf16 a[144:147], a[232:235], v[144:147], a[144:147]\n\t"
    "v_accvgpr_read_b32 v143, a111\n\t"
    "v_pk_mul_f32 v[136:137], v[136:137], v[162:163] op_sel_hi:[1,0]\n\t"
    "v_pk_mul_f32 v[138:139], v[138:139], v[162:163] op_sel_hi:[1,0]\n\t"
    "v_pk_mul_f32 v[140:141], v[140:141], v[162:163] op_sel_hi:[1,0]\n\t"
    "s_cmp_eq_u32 s94, s29\n\t"
    "s_addc_u32 s94, s94, 0\n\t"
    "v_pk_mul_f32 v[142:143], v[142:143], v[162:163] op_sel_hi:[1,0]\n\t"
    "v_exp_f32 v248, v248\n\t"
    "v_cvt_pk_bf16_f32 v36, v136, v137\n\t"
    "v_cvt_pk_bf16_f32 v37, v138, v139\n\t"
    "v_cvt_pk_bf16_f32 v38, v140, v141\n\t"
    "v_cvt_pk_bf16_f32 v39, v142, v143\n\t"
    "s_mul_i32 s28, s94, 524288\n\t"
    "s_mov_b32 s72, 12288\n\t"
    "buffer_store_dwordx4 v[56:59], v216, s[52:55], s72 offen offset:2048 sc1\n\t"
    "v_mfma_f32_16x16x32_bf16 a[148:151], a[236:239], v[144:147], a[148:151]\n\t"
    "v_accvgpr_read_b32 v152, a112\n\t"
    "v_accvgpr_read_b32 v153, a113\n\t"
    "v_accvgpr_read_b32 v154, a114\n\t"
    "v_accvgpr_read_b32 v155, a115\n\t"
    "v_accvgpr_read_b32 v156, a116\n\t"
    "v_accvgpr_read_b32 v157, a117\n\t"
    "v_exp_f32 v249, v249\n\t"
    "s_add_u32 s94, s94, 1\n\t"
    "v_accvgpr_read_b32 v158, a118\n\t"
    "v_accvgpr_read_b32 v159, a119\n\t"
    "v_pk_mul_f32 v[152:153], v[152:153], v[162:163] op_sel_hi:[1,0]\n\t"
    "v_pk_mul_f32 v[154:155], v[154:155], v[162:163] op_sel_hi:[1,0]\n\t"
    "v_pk_mul_f32 v[156:157], v[156:157], v[162:163] op_sel_hi:[1,0]\n\t"
    "v_cvt_pk_bf16_f32 v150, v248, v249\n\t"
    "v_mfma_f32_16x16x32_bf16 a[152:155], a[240:243], v[144:147], a[152:155]\n\t"
    "v_pk_mul_f32 v[158:159], v[158:159], v[162:163] op_sel_hi:[1,0]\n\t"
    "v_cvt_pk_bf16_f32 v60, v152, v153\n\t"
    "v_cvt_pk_bf16_f32 v61, v154, v155\n\t"
    "v_cvt_pk_bf16_f32 v62, v156, v157\n\t"
    "v_cvt_pk_bf16_f32 v63, v158, v159\n\t"
    "v_exp_f32 v250, v250\n\t"
    "v_accvgpr_read_b32 v128, a120\n\t"
    "v_accvgpr_read_b32 v129, a121\n\t"
    "v_accvgpr_read_b32 v130, a122\n\t"
    "v_accvgpr_read_b32 v131, a123\n\t"
    "v_accvgpr_read_b32 v132, a124\n\t"
    "v_accvgpr_read_b32 v133, a125\n\t"
    "v_mfma_f32_16x16x32_bf16 a[156:159], a[244:247], v[144:147], a[156:159]\n\t"
    "v_accvgpr_read_b32 v134, a126\n\t"
    "v_accvgpr_read_b32 v135, a127\n\t"
    "v_pk_mul_f32 v[128:129], v[128:129], v[162:163] op_sel_hi:[1,0]\n\t"
    "v_pk_mul_f32 v[130:131], v[130:131], v[162:163] op_sel_hi:[1,0]\n\t"
    "s_mov_b32 s72, 20480\n\t"
    "buffer_store_dwordx4 v[80:83], v216, s[52:55], s72 offen offset:0 sc1\n\t"
    "v_pk_mul_f32 v[132:133], v[132:133], v[162:163] op_sel_hi:[1,0]\n\t"
    "v_pk_mul_f32 v[134:135], v[134:135], v[162:163] op_sel_hi:[1,0]\n\t"
    "v_exp_f32 v251, v251\n\t"
    "v_cvt_pk_bf16_f32 v84, v128, v129\n\t"
    "v_cvt_pk_bf16_f32 v85, v130, v131\n\t"
    "v_cvt_pk_bf16_f32 v86, v132, v133\n\t"
    "v_cvt_pk_bf16_f32 v87, v134, v135\n\t"
    "v_mfma_f32_16x16x32_bf16 a[208:211], a[248:251], v[144:147], a[208:211]\n\t"
    "v_cvt_pk_bf16_f32 v151, v250, v251\n\t"
    "s_nop 1\n\t"
    "v_mfma_f32_16x16x32_bf16 a[160:163], a[216:219], v[148:151], a[160:163]\n\t"
    "v_mfma_f32_16x16x32_bf16 a[164:167], a[220:223], v[148:151], a[164:167]\n\t"
    "v_mfma_f32_16x16x32_bf16 a[168:171], a[224:227], v[148:151], a[168:171]\n\t"
    "s_nop 1\n\t"
    "v_accvgpr_read_b32 v160, a208\n\t"
    "v_rcp_f32 v160, v160\n\t"
    "v_accvgpr_read_b32 v136, a128\n\t"
    "v_accvgpr_read_b32 v137, a129\n\t"
    "v_accvgpr_read_b32 v138, a130\n\t"
    "v_accvgpr_read_b32 v139, a131\n\t"
    "v_accvgpr_read_b32 v140, a132\n\t"
    "v_accvgpr_read_b32 v141, a133\n\t"
    "v_accvgpr_read_b32 v142, a134\n\t"
    "v_accvgpr_read_b32 v143, a135\n\t"
    "v_mfma_f32_16x16x32_bf16 a[172:175], a[228:231], v[148:151], a[172:175]\n\t"
    "v_pk_mul_f32 v[136:137], v[136:137], v[160:161] op_sel_hi:[1,0]\n\t"
    "v_pk_mul_f32 v[138:139], v[138:139], v[160:161] op_sel_hi:[1,0]\n\t"
    "v_pk_mul_f32 v[140:141], v[140:141], v[160:161] op_sel_hi:[1,0]\n\t"
    "s_mov_b32 s72, 0\n\t"
    "buffer_store_dwordx4 v[12:15], v216, s[52:55], s72 offen offset:3072 sc1\n\t"
    "v_pk_mul_f32 v[142:143], v[142:143], v[160:161] op_sel_hi:[1,0]\n\t"
    "v_cvt_pk_bf16_f32 v16, v136, v137\n\t"
    "v_cvt_pk_bf16_f32 v17, v138, v139\n\t"
    "v_cvt_pk_bf16_f32 v18, v140, v141\n\t"
    "v_cvt_pk_bf16_f32 v19, v142, v143\n\t"
    "v_accvgpr_read_b32 v152, a136\n\t"
    "v_mfma_f32_16x16x32_bf16 a[176:179], a[232:235], v[148:151], a[176:179]\n\t"
    "v_accvgpr_read_b32 v153, a137\n\t"
    "v_accvgpr_read_b32 v154, a138\n\t"
    "v_accvgpr_read_b32 v155, a139\n\t"
    "v_accvgpr_read_b32 v156, a140\n\t"
    "v_accvgpr_read_b32 v157, a141\n\t"
    "v_accvgpr_read_b32 v158, a142\n\t"
    "v_accvgpr_read_b32 v159, a143\n\t"
    "v_pk_mul_f32 v[152:153], v[152:153], v[160:161] op_sel_hi:[1,0]\n\t"
    "v_pk_mul_f32 v[154:155], v[154:155], v[160:161] op_sel_hi:[1,0]\n\t"
    "v_pk_mul_f32 v[156:157], v[156:157], v[160:161] op_sel_hi:[1,0]\n\t"
    "v_mfma_f32_16x16x32_bf16 a[180:183], a[236:239], v[148:151], a[180:183]\n\t"
    "v_pk_mul_f32 v[158:159], v[158:159], v[160:161] op_sel_hi:[1,0]\n\t"
    "v_cvt_pk_bf16_f32 v40, v152, v153\n\t"
    "v_cvt_pk_bf16_f32 v41, v154, v155\n\t"
    "v_cvt_pk_bf16_f32 v42, v156, v157\n\t"
    "v_cvt_pk_bf16_f32 v43, v158, v159\n\t"
    "v_accvgpr_read_b32 v128, a144\n\t"
    "v_accvgpr_read_b32 v129, a145\n\t"
    "v_accvgpr_read_b32 v130, a146\n\t"
    "v_accvgpr_read_b32 v131, a147\n\t"
    "v_mfma_f32_16x16x32_bf16 a[184:187], a[240:243], v[148:151], a[184:187]\n\t"
    "v_accvgpr_read_b32 v132, a148\n\t"
    "v_accvgpr_read_b32 v133, a149\n\t"
    "v_accvgpr_read_b32 v134, a150\n\t"
    "v_accvgpr_read_b32 v135, a151\n\t"
    "v_pk_mul_f32 v[128:129], v[128:129], v[160:161] op_sel_hi:[1,0]\n\t"
    "v_pk_mul_f32 v[130:131], v[130:131], v[160:161] op_sel_hi:[1,0]\n\t"
    "v_pk_mul_f32 v[132:133], v[132:133], v[160:161] op_sel_hi:[1,0]\n\t"
    "s_mov_b32 s72, 8192\n\t"
    "buffer_store_dwordx4 v[36:39], v216, s[52:55], s72 offen offset:1024 sc1\n\t"
    "v_pk_mul_f32 v[134:135], v[134:135], v[160:161] op_sel_hi:[1,0]\n\t"
    "v_cvt_pk_bf16_f32 v64, v128, v129\n\t"
    "v_cvt_pk_bf16_f32 v65, v130, v131\n\t"
    "v_mfma_f32_16x16x32_bf16 a[188:191], a[244:247], v[148:151], a[188:191]\n\t"
    "v_cvt_pk_bf16_f32 v66, v132, v133\n\t"
    "v_cvt_pk_bf16_f32 v67, v134, v135\n\t"
    "v_accvgpr_read_b32 v136, a152\n\t"
    "v_accvgpr_read_b32 v137, a153\n\t"
    "v_accvgpr_read_b32 v138, a154\n\t"
    "v_accvgpr_read_b32 v139, a155\n\t"
    "v_accvgpr_read_b32 v140, a156\n\t"
    "v_accvgpr_read_b32 v141, a157\n\t"
    "v_accvgpr_read_b32 v142, a158\n\t"
    "v_mfma_f32_16x16x32_bf16 a[212:215], a[248:251], v[148:151], a[212:215]\n\t"
    "v_accvgpr_read_b32 v143, a159\n\t"
    "v_pk_mul_f32 v[136:137], v[136:137], v[160:161] op_sel_hi:[1,0]\n\t"
    "v_pk_mul_f32 v[138:139], v[138:139], v[160:161] op_sel_hi:[1,0]\n\t"
    "v_pk_mul_f32 v[140:141], v[140:141], v[160:161] op_sel_hi:[1,0]\n\t"
    "v_pk_mul_f32 v[142:143], v[142:143], v[160:161] op_sel_hi:[1,0]\n\t"
    "v_cvt_pk_bf16_f32 v88, v136, v137\n\t"
    "v_cvt_pk_bf16_f32 v89, v138, v139\n\t"
    "v_cvt_pk_bf16_f32 v90, v140, v141\n\t"
    "v_cvt_pk_bf16_f32 v91, v142, v143\n\t"
    "s_waitcnt vmcnt(17)\n\t"
    "s_barrier\n\t"
    "v_accvgpr_read_b32 v162, a212\n\t"
    "v_rcp_f32 v162, v162\n\t"
    "v_accvgpr_read_b32 v152, a160\n\t"
    "v_accvgpr_read_b32 v153, a161\n\t"
    "v_accvgpr_read_b32 v154, a162\n\t"
    "v_accvgpr_read_b32 v155, a163\n\t"
    "v_accvgpr_read_b32 v156, a164\n\t"
    "v_accvgpr_read_b32 v157, a165\n\t"
    "v_accvgpr_read_b32 v158, a166\n\t"
    "v_accvgpr_read_b32 v159, a167\n\t"
    "s_mov_b32 s72, 12288\n\t"
    "buffer_store_dwordx4 v[60:63], v216, s[52:55], s72 offen offset:3072 sc1\n\t"
    "ds_read_b64_tr_b16 v[96:97], v238 offset:0\n\t"
    "ds_read_b64_tr_b16 v[98:99], v238 offset:4096\n\t"
    "ds_read_b64_tr_b16 v[100:101], v239 offset:0\n\t"
    "ds_read_b64_tr_b16 v[102:103], v239 offset:4096\n\t"
    "v_pk_mul_f32 v[152:153], v[152:153], v[162:163] op_sel_hi:[1,0]\n\t"
    "v_pk_mul_f32 v[154:155], v[154:155], v[162:163] op_sel_hi:[1,0]\n\t"
    "v_pk_mul_f32 v[156:157], v[156:157], v[162:163] op_sel_hi:[1,0]\n\t"
    "v_pk_mul_f32 v[158:159], v[158:159], v[162:163] op_sel_hi:[1,0]\n\t"
    "v_cvt_pk_bf16_f32 v20, v152, v153\n\t"
    "v_cvt_pk_bf16_f32 v21, v154, v155\n\t"
    "v_cvt_pk_bf16_f32 v22, v156, v157\n\t"
    "v_cvt_pk_bf16_f32 v23, v158, v159\n\t"
    "v_accvgpr_read_b32 v128, a168\n\t"
    "v_accvgpr_read_b32 v129, a169\n\t"
    "v_accvgpr_read_b32 v130, a170\n\t"
    "v_accvgpr_read_b32 v131, a171\n\t"
    "v_accvgpr_read_b32 v132, a172\n\t"
    "v_accvgpr_read_b32 v133, a173\n\t"
    "v_accvgpr_read_b32 v134, a174\n\t"
    "v_accvgpr_read_b32 v135, a175\n\t"
    "ds_read_b64_tr_b16 v[104:105], v238 offset:256\n\t"
    "ds_read_b64_tr_b16 v[106:107], v238 offset:4352\n\t"
    "ds_read_b64_tr_b16 v[108:109], v239 offset:256\n\t"
    "ds_read_b64_tr_b16 v[110:111], v239 offset:4352\n\t"
    "v_pk_mul_f32 v[128:129], v[128:129], v[162:163] op_sel_hi:[1,0]\n\t"
    "v_pk_mul_f32 v[130:131], v[130:131], v[162:163] op_sel_hi:[1,0]\n\t"
    "v_pk_mul_f32 v[132:133], v[132:133], v[162:163] op_sel_hi:[1,0]\n\t"
    "v_pk_mul_f32 v[134:135], v[134:135], v[162:163] op_sel_hi:[1,0]\n\t"
    "v_cvt_pk_bf16_f32 v44, v128, v129\n\t"
    "v_cvt_pk_bf16_f32 v45, v130, v131\n\t"
    "v_cvt_pk_bf16_f32 v46, v132, v133\n\t"
    "v_cvt_pk_bf16_f32 v47, v134, v135\n\t"
    "v_accvgpr_read_b32 v136, a176\n\t"
    "v_accvgpr_read_b32 v137, a177\n\t"
    "v_accvgpr_read_b32 v138, a178\n\t"
    "v_accvgpr_read_b32 v139, a179\n\t"
    "v_accvgpr_read_b32 v140, a180\n\t"
    "v_accvgpr_read_b32 v141, a181\n\t"
    "s_mov_b32 s72, 20480\n\t"
    "buffer_store_dwordx4 v[84:87], v216, s[52:55], s72 offen offset:1024 sc1\n\t"
    "v_accvgpr_read_b32 v142, a182\n\t"
    "v_accvgpr_read_b32 v143, a183\n\t"
    "ds_read_b64_tr_b16 v[112:113], v238 offset:512\n\t"
    "ds_read_b64_tr_b16 v[114:115], v238 offset:4608\n\t"
    "ds_read_b64_tr_b16 v[116:117], v239 offset:512\n\t"
    "ds_read_b64_tr_b16 v[118:119], v239 offset:4608\n\t"
    "v_pk_mul_f32 v[136:137], v[136:137], v[162:163] op_sel_hi:[1,0]\n\t"
    "v_pk_mul_f32 v[138:139], v[138:139], v[162:163] op_sel_hi:[1,0]\n\t"
    "v_pk_mul_f32 v[140:141], v[140:141], v[162:163] op_sel_hi:[1,0]\n\t"
    "v_pk_mul_f32 v[142:143], v[142:143], v[162:163] op_sel_hi:[1,0]\n\t"
    "v_cvt_pk_bf16_f32 v68, v136, v137\n\t"
    "v_cvt_pk_bf16_f32 v69, v138, v139\n\t"
    "v_cvt_pk_bf16_f32 v70, v140, v141\n\t"
    "v_cvt_pk_bf16_f32 v71, v142, v143\n\t"
    "v_accvgpr_read_b32 v152, a184\n\t"
    "v_accvgpr_read_b32 v153, a185\n\t"
    "v_accvgpr_read_b32 v154, a186\n\t"
    "v_accvgpr_read_b32 v155, a187\n\t"
    "v_accvgpr_read_b32 v156, a188\n\t"
    "v_accvgpr_read_b32 v157, a189\n\t"
    "v_accvgpr_read_b32 v158, a190\n\t"
    "v_accvgpr_read_b32 v159, a191\n\t"
    "ds_read_b64_tr_b16 v[120:121], v238 offset:768\n\t"
    "ds_read_b64_tr_b16 v[122:123], v238 offset:4864\n\t"
    "ds_read_b64_tr_b16 v[124:125], v239 offset:768\n\t"
    "ds_read_b64_tr_b16 v[126:127], v239 offset:4864\n\t"
    "v_pk_mul_f32 v[152:153], v[152:153], v[162:163] op_sel_hi:[1,0]\n\t"
    "v_pk_mul_f32 v[154:155], v[154:155], v[162:163] op_sel_hi:[1,0]\n\t"
    "v_pk_mul_f32 v[156:157], v[156:157], v[162:163] op_sel_hi:[1,0]\n\t"
    "v_pk_mul_f32 v[158:159], v[158:159], v[162:163] op_sel_hi:[1,0]\n\t"
    "v_cvt_pk_bf16_f32 v92, v152, v153\n\t"
    "v_cvt_pk_bf16_f32 v93, v154, v155\n\t"
    "v_cvt_pk_bf16_f32 v94, v156, v157\n\t"
    "v_cvt_pk_bf16_f32 v95, v158, v159\n\t"
    "s_waitcnt lgkmcnt(0)\n\t"
    "v_mfma_f32_16x16x32_bf16 a[0:3], v[96:99], v[0:3], 0\n\t"
    "ds_read_b64_tr_b16 v[128:129], v238 offset:8192\n\t"
    "s_mov_b32 s72, 4096\n\t"
    "buffer_store_dwordx4 v[16:19], v216, s[52:55], s72 offen offset:0 sc1\n\t"
    "s_add_u32 m0, s31, 32768\n\t"
    "v_mfma_f32_16x16x32_bf16 a[32:35], v[96:99], v[4:7], 0\n\t"
    "v_mfma_f32_16x16x32_bf16 a[64:67], v[96:99], v[8:11], 0\n\t"
    "ds_read_b64_tr_b16 v[130:131], v238 offset:12288\n\t"
    "v_mfma_f32_16x16x32_bf16 a[96:99], v[96:99], v[12:15], 0\n\t"
    "v_mfma_f32_16x16x32_bf16 a[128:131], v[96:99], v[16:19], 0\n\t"
    "ds_read_b64_tr_b16 v[132:133], v239 offset:8192\n\t"
    "v_mfma_f32_16x16x32_bf16 a[160:163], v[96:99], v[20:23], 0\n\t"
    "v_mfma_f32_16x16x32_bf16 a[4:7], v[100:103], v[0:3], 0\n\t"
    "ds_read_b64_tr_b16 v[134:135], v239 offset:12288\n\t"
    "v_mfma_f32_16x16x32_bf16 a[36:39], v[100:103], v[4:7], 0\n\t"
    "v_mfma_f32_16x16x32_bf16 a[68:71], v[100:103], v[8:11], 0\n\t"
    "buffer_load_dwordx4 v232, s[12:15], s28 offen offset:0 lds\n\t"
    "v_mfma_f32_16x16x32_bf16 a[100:103], v[100:103], v[12:15], 0\n\t"
    "ds_read_b64_tr_b16 v[136:137], v238 offset:8448\n\t"
    "v_mfma_f32_16x16x32_bf16 a[132:135], v[100:103], v[16:19], 0\n\t"
    "s_mov_b32 s72, 8192\n\t"
    "buffer_store_dwordx4 v[40:43], v216, s[52:55], s72 offen offset:2048 sc1\n\t"
    "v_mfma_f32_16x16x32_bf16 a[164:167], v[100:103], v[20:23], 0\n\t"
    "ds_read_b64_tr_b16 v[138:139], v238 offset:12544\n\t"
    "v_mfma_f32_16x16x32_bf16 a[8:11], v[104:107], v[0:3], 0\n\t"
    "v_mfma_f32_16x16x32_bf16 a[40:43], v[104:107], v[4:7], 0\n\t"
    "ds_read_b64_tr_b16 v[140:141], v239 offset:8448\n\t"
    "v_mfma_f32_16x16x32_bf16 a[72:75], v[104:107], v[8:11], 0\n\t"
    "v_mfma_f32_16x16x32_bf16 a[104:107], v[104:107], v[12:15], 0\n\t"
    "v_mfma_f32_16x16x32_bf16 a[136:139], v[104:107], v[16:19], 0\n\t"
    "ds_read_b64_tr_b16 v[142:143], v239 offset:12544\n\t"
    "buffer_load_dwordx4 v233, s[12:15], s28 offen offset:1024 lds\n\t"
    "v_mfma_f32_16x16x32_bf16 a[168:171], v[104:107], v[20:23], 0\n\t"
    "v_mfma_f32_16x16x32_bf16 a[12:15], v[108:111], v[0:3], 0\n\t"
    "ds_read_b64_tr_b16 v[144:145], v238 offset:8704\n\t"
    "v_mfma_f32_16x16x32_bf16 a[44:47], v[108:111], v[4:7], 0\n\t"
    "v_mfma_f32_16x16x32_bf16 a[76:79], v[108:111], v[8:11], 0\n\t"
    "s_mov_b32 s72, 16384\n\t"
    "buffer_store_dwordx4 v[64:67], v216, s[52:55], s72 offen offset:0 sc1\n\t"
    "ds_read_b64_tr_b16 v[146:147], v238 offset:12800\n\t"
    "v_mfma_f32_16x16x32_bf16 a[108:111], v[108:111], v[12:15], 0\n\t"
    "v_mfma_f32_16x16x32_bf16 a[140:143], v[108:111], v[16:19], 0\n\t"
    "v_mfma_f32_16x16x32_bf16 a[172:175], v[108:111], v[20:23], 0\n\t"
    "ds_read_b64_tr_b16 v[148:149], v239 offset:8704\n\t"
    "v_mfma_f32_16x16x32_bf16 a[16:19], v[112:115], v[0:3], 0\n\t"
    "buffer_load_dwordx4 v234, s[12:15], s28 offen offset:2048 lds\n\t"
    "v_mfma_f32_16x16x32_bf16 a[48:51], v[112:115], v[4:7], 0\n\t"
    "ds_read_b64_tr_b16 v[150:151], v239 offset:12800\n\t"
    "v_mfma_f32_16x16x32_bf16 a[80:83], v[112:115], v[8:11], 0\n\t"
    "v_mfma_f32_16x16x32_bf16 a[112:115], v[112:115], v[12:15], 0\n\t"
    "ds_read_b64_tr_b16 v[152:153], v238 offset:8960\n\t"
    "v_mfma_f32_16x16x32_bf16 a[144:147], v[112:115], v[16:19], 0\n\t"
    "v_mfma_f32_16x16x32_bf16 a[176:179], v[112:115], v[20:23], 0\n\t"
    "v_mfma_f32_16x16x32_bf16 a[20:23], v[116:119], v[0:3], 0\n\t"
    "s_mov_b32 s72, 20480\n\t"
    "buffer_store_dwordx4 v[88:91], v216, s[52:55], s72 offen offset:2048 sc1\n\t"
    "ds_read_b64_tr_b16 v[154:155], v238 offset:13056\n\t"
    "v_mfma_f32_16x16x32_bf16 a[52:55], v[116:119], v[4:7], 0\n\t"
    "v_mfma_f32_16x16x32_bf16 a[84:87], v[116:119], v[8:11], 0\n\t"
    "ds_read_b64_tr_b16 v[156:157], v239 offset:8960\n\t"
    "buffer_load_dwordx4 v235, s[12:15], s28 offen offset:3072 lds\n\t"
    "v_mfma_f32_16x16x32_bf16 a[116:119], v[116:119], v[12:15], 0\n\t"
    "v_mfma_f32_16x16x32_bf16 a[148:151], v[116:119], v[16:19], 0\n\t"
    "ds_read_b64_tr_b16 v[158:159], v239 offset:13056\n\t"
    "v_mfma_f32_16x16x32_bf16 a[180:183], v[116:119], v[20:23], 0\n\t"
    "v_mfma_f32_16x16x32_bf16 a[24:27], v[120:123], v[0:3], 0\n\t"
    "v_mfma_f32_16x16x32_bf16 a[56:59], v[120:123], v[4:7], 0\n\t"
    "v_mfma_f32_16x16x32_bf16 a[88:91], v[120:123], v[8:11], 0\n\t"
    "v_mfma_f32_16x16x32_bf16 a[120:123], v[120:123], v[12:15], 0\n\t"
    "v_mfma_f32_16x16x32_bf16 a[152:155], v[120:123], v[16:19], 0\n\t"
    "s_mov_b32 s72, 4096\n\t"
    "buffer_store_dwordx4 v[20:23], v216, s[52:55], s72 offen offset:1024 sc1\n\t"
    "s_add_u32 s28, s28, 65536\n\t"
    "v_mfma_f32_16x16x32_bf16 a[184:187], v[120:123], v[20:23], 0\n\t"
    "v_mfma_f32_16x16x32_bf16 a[28:31], v[124:127], v[0:3], 0\n\t"
    "v_mfma_f32_16x16x32_bf16 a[60:63], v[124:127], v[4:7], 0\n\t"
    "v_mfma_f32_16x16x32_bf16 a[92:95], v[124:127], v[8:11], 0\n\t"
    "v_mfma_f32_16x16x32_bf16 a[124:127], v[124:127], v[12:15], 0\n\t"
    "v_mfma_f32_16x16x32_bf16 a[156:159], v[124:127], v[16:19], 0\n\t"
    "v_mfma_f32_16x16x32_bf16 a[188:191], v[124:127], v[20:23], 0\n\t"
    "s_waitcnt vmcnt(15) lgkmcnt(0)\n\t"
    "s_barrier\n\t"
    "v_mfma_f32_16x16x32_bf16 a[0:3], v[128:131], v[24:27], a[0:3]\n\t"
    "ds_read_b64_tr_b16 v[96:97], v238 offset:16384\n\t"
    "s_add_u32 m0, s31, 40960\n\t"
    "v_mfma_f32_16x16x32_bf16 a[32:35], v[128:131], v[28:31], a[32:35]\n\t"
    "v_mfma_f32_16x16x32_bf16 a[64:67], v[128:131], v[32:35], a[64:67]\n\t"
    "s_mov_b32 s72, 8192\n\t"
    "buffer_store_dwordx4 v[44:47], v216, s[52:55], s72 offen offset:3072 sc1\n\t"
    "ds_read_b64_tr_b16 v[98:99], v238 offset:20480\n\t"
    "v_mfma_f32_16x16x32_bf16 a[96:99], v[128:131], v[36:39], a[96:99]\n\t"
    "buffer_load_dwordx4 v232, s[12:15], s28 offen offset:0 lds\n\t"
    "v_mfma_f32_16x16x32_bf16 a[128:131], v[128:131], v[40:43], a[128:131]\n\t"
    "ds_read_b64_tr_b16 v[100:101], v239 offset:16384\n\t"
    "v_mfma_f32_16x16x32_bf16 a[160:163], v[128:131], v[44:47], a[160:163]\n\t"
    "v_mfma_f32_16x16x32_bf16 a[4:7], v[132:135], v[24:27], a[4:7]\n\t"
    "ds_read_b64_tr_b16 v[102:103], v239 offset:20480\n\t"
    "v_mfma_f32_16x16x32_bf16 a[36:39], v[132:135], v[28:31], a[36:39]\n\t"
    "buffer_load_dwordx4 v233, s[12:15], s28 offen offset:1024 lds\n\t"
    "v_mfma_f32_16x16x32_bf16 a[68:71], v[132:135], v[32:35], a[68:71]\n\t"
    "v_mfma_f32_16x16x32_bf16 a[100:103], v[132:135], v[36:39], a[100:103]\n\t"
    "ds_read_b64_tr_b16 v[104:105], v238 offset:16640\n\t"
    "v_mfma_f32_16x16x32_bf16 a[132:135], v[132:135], v[40:43], a[132:135]\n\t"
    "v_mfma_f32_16x16x32_bf16 a[164:167], v[132:135], v[44:47], a[164:167]\n\t"
    "ds_read_b64_tr_b16 v[106:107], v238 offset:20736\n\t"
    "buffer_load_dwordx4 v234, s[12:15], s28 offen offset:2048 lds\n\t"
    "v_mfma_f32_16x16x32_bf16 a[8:11], v[136:139], v[24:27], a[8:11]\n\t"
    "s_mov_b32 s72, 16384\n\t"
    "buffer_store_dwordx4 v[68:71], v216, s[52:55], s72 offen offset:1024 sc1\n\t"
    "v_mfma_f32_16x16x32_bf16 a[40:43], v[136:139], v[28:31], a[40:43]\n\t"
    "ds_read_b64_tr_b16 v[108:109], v239 offset:16640\n\t"
    "v_mfma_f32_16x16x32_bf16 a[72:75], v[136:139], v[32:35], a[72:75]\n\t"
    "buffer_load_dwordx4 v235, s[12:15], s28 offen offset:3072 lds\n\t"
    "v_mfma_f32_16x16x32_bf16 a[104:107], v[136:139], v[36:39], a[104:107]\n\t"
    "v_mfma_f32_16x16x32_bf16 a[136:139], v[136:139], v[40:43], a[136:139]\n\t"
    "ds_read_b64_tr_b16 v[110:111], v239 offset:20736\n\t"
    "v_mfma_f32_16x16x32_bf16 a[168:171], v[136:139], v[44:47], a[168:171]\n\t"
    "v_mfma_f32_16x16x32_bf16 a[12:15], v[140:143], v[24:27], a[12:15]\n\t"
    "ds_read_b64_tr_b16 v[112:113], v238 offset:16896\n\t"
    "s_add_u32 s28, s28, 65536\n\t"
    "v_mfma_f32_16x16x32_bf16 a[44:47], v[140:143], v[28:31], a[44:47]\n\t"
    "v_mfma_f32_16x16x32_bf16 a[76:79], v[140:143], v[32:35], a[76:79]\n\t"
    "ds_read_b64_tr_b16 v[114:115], v238 offset:20992\n\t"
    "v_mfma_f32_16x16x32_bf16 a[108:111], v[140:143], v[36:39], a[108:111]\n\t"
    "v_mfma_f32_16x16x32_bf16 a[140:143], v[140:143], v[40:43], a[140:143]\n\t"
    "s_mov_b32 s72, 20480\n\t"
    "buffer_store_dwordx4 v[92:95], v216, s[52:55], s72 offen offset:3072 sc1\n\t"
    "s_add_u32 m0, s31, 49152\n\t"
    "v_mfma_f32_16x16x32_bf16 a[172:175], v[140:143], v[44:47], a[172:175]\n\t"
    "ds_read_b64_tr_b16 v[116:117], v239 offset:16896\n\t"
    "v_mfma_f32_16x16x32_bf16 a[16:19], v[144:147], v[24:27], a[16:19]\n\t"
    "v_mfma_f32_16x16x32_bf16 a[48:51], v[144:147], v[28:31], a[48:51]\n\t"
    "ds_read_b64_tr_b16 v[118:119], v239 offset:20992\n\t"
    "buffer_load_dwordx4 v232, s[12:15], s28 offen offset:0 lds\n\t"
    "v_mfma_f32_16x16x32_bf16 a[80:83], v[144:147], v[32:35], a[80:83]\n\t"
    "v_mfma_f32_16x16x32_bf16 a[112:115], v[144:147], v[36:39], a[112:115]\n\t"
    "ds_read_b64_tr_b16 v[120:121], v238 offset:17152\n\t"
    "v_mfma_f32_16x16x32_bf16 a[144:147], v[144:147], v[40:43], a[144:147]\n\t"
    "v_mfma_f32_16x16x32_bf16 a[176:179], v[144:147], v[44:47], a[176:179]\n\t"
    "buffer_load_dwordx4 v233, s[12:15], s28 offen offset:1024 lds\n\t"
    "v_mfma_f32_16x16x32_bf16 a[20:23], v[148:151], v[24:27], a[20:23]\n\t"
    "ds_read_b64_tr_b16 v[122:123], v238 offset:21248\n\t"
    "v_mfma_f32_16x16x32_bf16 a[52:55], v[148:151], v[28:31], a[52:55]\n\t"
    "v_mfma_f32_16x16x32_bf16 a[84:87], v[148:151], v[32:35], a[84:87]\n\t"
    "ds_read_b64_tr_b16 v[124:125], v239 offset:17152\n\t"
    "v_mfma_f32_16x16x32_bf16 a[116:119], v[148:151], v[36:39], a[116:119]\n\t"
    "buffer_load_dwordx4 v234, s[12:15], s28 offen offset:2048 lds\n\t"
    "v_mfma_f32_16x16x32_bf16 a[148:151], v[148:151], v[40:43], a[148:151]\n\t"
    "ds_read_b64_tr_b16 v[126:127], v239 offset:21248\n\t"
    "v_mfma_f32_16x16x32_bf16 a[180:183], v[148:151], v[44:47], a[180:183]\n\t"
    "v_mfma_f32_16x16x32_bf16 a[24:27], v[152:155], v[24:27], a[24:27]\n\t"
    "buffer_load_dwordx4 v235, s[12:15], s28 offen offset:3072 lds\n\t"
    "v_mfma_f32_16x16x32_bf16 a[56:59], v[152:155], v[28:31], a[56:59]\n\t"
    "v_mfma_f32_16x16x32_bf16 a[88:91], v[152:155], v[32:35], a[88:91]\n\t"
    "v_mfma_f32_16x16x32_bf16 a[120:123], v[152:155], v[36:39], a[120:123]\n\t"
    "v_mfma_f32_16x16x32_bf16 a[152:155], v[152:155], v[40:43], a[152:155]\n\t"
    "s_add_u32 s28, s28, 65536\n\t"
    "v_mfma_f32_16x16x32_bf16 a[184:187], v[152:155], v[44:47], a[184:187]\n\t"
    "v_mfma_f32_16x16x32_bf16 a[28:31], v[156:159], v[24:27], a[28:31]\n\t"
    "v_mfma_f32_16x16x32_bf16 a[60:63], v[156:159], v[28:31], a[60:63]\n\t"
    "v_mfma_f32_16x16x32_bf16 a[92:95], v[156:159], v[32:35], a[92:95]\n\t"
    "s_getpc_b64 s[68:69]\n\t"
    ".LCT5_%=:\n\t"
    "v_min_u32 v251, .LEND_%=-.LCT5_%=-4, v223\n\t"
    "global_load_dword a255, v251, s[68:69]\n\t"
    "v_mfma_f32_16x16x32_bf16 a[124:127], v[156:159], v[36:39], a[124:127]\n\t"
    "v_mfma_f32_16x16x32_bf16 a[156:159], v[156:159], v[40:43], a[156:159]\n\t"
    "v_mfma_f32_16x16x32_bf16 a[188:191], v[156:159], v[44:47], a[188:191]\n\t"
    "s_waitcnt lgkmcnt(0)\n\t"
    "v_mfma_f32_16x16x32_bf16 a[0:3], v[96:99], v[48:51], a[0:3]\n\t"
    "ds_read_b64_tr_b16 v[128:129], v238 offset:24576\n\t"
    "s_add_u32 m0, s31, 57344\n\t"
    "v_mfma_f32_16x16x32_bf16 a[32:35], v[96:99], v[52:55], a[32:35]\n\t"
    "v_mfma_f32_16x16x32_bf16 a[64:67], v[96:99], v[56:59], a[64:67]\n\t"
    "ds_read_b64_tr_b16 v[130:131], v238 offset:28672\n\t"
    "v_mfma_f32_16x16x32_bf16 a[96:99], v[96:99], v[60:63], a[96:99]\n\t"
    "buffer_load_dwordx4 v232, s[12:15], s28 offen offset:0 lds\n\t"
    "v_mfma_f32_16x16x32_bf16 a[128:131], v[96:99], v[64:67], a[128:131]\n\t"
    "ds_read_b64_tr_b16 v[132:133], v239 offset:24576\n\t"
    "v_mfma_f32_16x16x32_bf16 a[160:163], v[96:99], v[68:71], a[160:163]\n\t"
    "v_mfma_f32_16x16x32_bf16 a[4:7], v[100:103], v[48:51], a[4:7]\n\t"
    "ds_read_b64_tr_b16 v[134:135], v239 offset:28672\n\t"
    "buffer_load_dwordx4 v233, s[12:15], s28 offen offset:1024 lds\n\t"
    "v_mfma_f32_16x16x32_bf16 a[36:39], v[100:103], v[52:55], a[36:39]\n\t"
    "v_mfma_f32_16x16x32_bf16 a[68:71], v[100:103], v[56:59], a[68:71]\n\t"
    "v_mfma_f32_16x16x32_bf16 a[100:103], v[100:103], v[60:63], a[100:103]\n\t"
    "ds_read_b64_tr_b16 v[136:137], v238 offset:24832\n\t"
    "v_mfma_f32_16x16x32_bf16 a[132:135], v[100:103], v[64:67], a[132:135]\n\t"
    "buffer_load_dwordx4 v234, s[12:15], s28 offen offset:2048 lds\n\t"
    "v_mfma_f32_16x16x32_bf16 a[164:167], v[100:103], v[68:71], a[164:167]\n\t"
    "ds_read_b64_tr_b16 v[138:139], v238 offset:28928\n\t"
    "v_mfma_f32_16x16x32_bf16 a[8:11], v[104:107], v[48:51], a[8:11]\n\t"
    "v_mfma_f32_16x16x32_bf16 a[40:43], v[104:107], v[52:55], a[40:43]\n\t"
    "ds_read_b64_tr_b16 v[140:141], v239 offset:24832\n\t"
    "buffer_load_dwordx4 v235, s[12:15], s28 offen offset:3072 lds\n\t"
    "v_mfma_f32_16x16x32_bf16 a[72:75], v[104:107], v[56:59], a[72:75]\n\t"
    "v_mfma_f32_16x16x32_bf16 a[104:107], v[104:107], v[60:63], a[104:107]\n\t"
    "v_mfma_f32_16x16x32_bf16 a[136:139], v[104:107], v[64:67], a[136:139]\n\t"
    "ds_read_b64_tr_b16 v[142:143], v239 offset:28928\n\t"
    "v_mfma_f32_16x16x32_bf16 a[168:171], v[104:107], v[68:71], a[168:171]\n\t"
    "s_cmp_eq_u32 s94, s29\n\t"
    "s_addc_u32 s94, s94, 0\n\t"
    "v_mfma_f32_16x16x32_bf16 a[12:15], v[108:111], v[48:51], a[12:15]\n\t"
    "ds_read_b64_tr_b16 v[144:145], v238 offset:25088\n\t"
    "v_mfma_f32_16x16x32_bf16 a[44:47], v[108:111], v[52:55], a[44:47]\n\t"
    "v_mfma_f32_16x16x32_bf16 a[76:79], v[108:111], v[56:59], a[76:79]\n\t"
    "ds_read_b64_tr_b16 v[146:147], v238 offset:29184\n\t"
    "s_mul_i32 s28, s94, 524288\n\t"
    "v_mfma_f32_16x16x32_bf16 a[108:111], v[108:111], v[60:63], a[108:111]\n\t"
    "v_mfma_f32_16x16x32_bf16 a[140:143], v[108:111], v[64:67], a[140:143]\n\t"
    "v_mfma_f32_16x16x32_bf16 a[172:175], v[108:111], v[68:71], a[172:175]\n\t"
    "ds_read_b64_tr_b16 v[148:149], v239 offset:25088\n\t"
    "v_mfma_f32_16x16x32_bf16 a[16:19], v[112:115], v[48:51], a[16:19]\n\t"
    "s_waitcnt vmcnt(9)\n\t"
    "s_barrier\n\t"
    "buffer_store_dword v208, v242, s[24:27], 0 offen offset:0 sc1\n\t"
    "s_add_u32 s94, s94, 1\n\t"
    "v_mfma_f32_16x16x32_bf16 a[48:51], v[112:115], v[52:55], a[48:51]\n\t"
    "ds_read_b64_tr_b16 v[150:151], v239 offset:29184\n\t"
    "v_mfma_f32_16x16x32_bf16 a[80:83], v[112:115], v[56:59], a[80:83]\n\t"
    "v_mfma_f32_16x16x32_bf16 a[112:115], v[112:115], v[60:63], a[112:115]\n\t"
    "ds_read_b64_tr_b16 v[152:153], v238 offset:25344\n\t"
    "s_add_u32 m0, s31, 0\n\t"
    "v_mfma_f32_16x16x32_bf16 a[144:147], v[112:115], v[64:67], a[144:147]\n\t"
    "v_mfma_f32_16x16x32_bf16 a[176:179], v[112:115], v[68:71], a[176:179]\n\t"
    "v_mfma_f32_16x16x32_bf16 a[20:23], v[116:119], v[48:51], a[20:23]\n\t"
    "ds_read_b64_tr_b16 v[154:155], v238 offset:29440\n\t"
    "buffer_load_dwordx4 v232, s[12:15], s28 offen offset:0 lds\n\t"
    "v_mfma_f32_16x16x32_bf16 a[52:55], v[116:119], v[52:55], a[52:55]\n\t"
    "v_mfma_f32_16x16x32_bf16 a[84:87], v[116:119], v[56:59], a[84:87]\n\t"
    "ds_read_b64_tr_b16 v[156:157], v239 offset:25344\n\t"
    "v_mfma_f32_16x16x32_bf16 a[116:119], v[116:119], v[60:63], a[116:119]\n\t"
    "v_mfma_f32_16x16x32_bf16 a[148:151], v[116:119], v[64:67], a[148:151]\n\t"
    "ds_read_b64_tr_b16 v[158:159], v239 offset:29440\n\t"
    "buffer_load_dwordx4 v233, s[12:15], s28 offen offset:1024 lds\n\t"
    "v_mfma_f32_16x16x32_bf16 a[180:183], v[116:119], v[68:71], a[180:183]\n\t"
    "v_mfma_f32_16x16x32_bf16 a[24:27], v[120:123], v[48:51], a[24:27]\n\t"
    "v_mfma_f32_16x16x32_bf16 a[56:59], v[120:123], v[52:55], a[56:59]\n\t"
    "buffer_load_dwordx4 v234, s[12:15], s28 offen offset:2048 lds\n\t"
    "v_mfma_f32_16x16x32_bf16 a[88:91], v[120:123], v[56:59], a[88:91]\n\t"
    "v_mfma_f32_16x16x32_bf16 a[120:123], v[120:123], v[60:63], a[120:123]\n\t"
    "v_mfma_f32_16x16x32_bf16 a[152:155], v[120:123], v[64:67], a[152:155]\n\t"
    "v_mfma_f32_16x16x32_bf16 a[184:187], v[120:123], v[68:71], a[184:187]\n\t"
    "buffer_load_dwordx4 v235, s[12:15], s28 offen offset:3072 lds\n\t"
    "v_mfma_f32_16x16x32_bf16 a[28:31], v[124:127], v[48:51], a[28:31]\n\t"
    "v_mfma_f32_16x16x32_bf16 a[60:63], v[124:127], v[52:55], a[60:63]\n\t"
    "v_mfma_f32_16x16x32_bf16 a[92:95], v[124:127], v[56:59], a[92:95]\n\t"
    "s_add_u32 s28, s28, 65536\n\t"
    "v_mfma_f32_16x16x32_bf16 a[124:127], v[124:127], v[60:63], a[124:127]\n\t"
    "v_mfma_f32_16x16x32_bf16 a[156:159], v[124:127], v[64:67], a[156:159]\n\t"
    "v_mfma_f32_16x16x32_bf16 a[188:191], v[124:127], v[68:71], a[188:191]\n\t"
    "s_waitcnt vmcnt(15) lgkmcnt(0)\n\t"
    "s_barrier\n\t"
    "v_mfma_f32_16x16x32_bf16 a[0:3], v[128:131], v[72:75], a[0:3]\n\t"
    "ds_read_b64_tr_b16 v[96:97], v238 offset:32768\n\t"
    "s_add_u32 m0, s31, 8192\n\t"
    "v_mfma_f32_16x16x32_bf16 a[32:35], v[128:131], v[76:79], a[32:35]\n\t"
    "v_mfma_f32_16x16x32_bf16 a[64:67], v[128:131], v[80:83], a[64:67]\n\t"
    "ds_read_b64_tr_b16 v[98:99], v238 offset:36864\n\t"
    "v_mfma_f32_16x16x32_bf16 a[96:99], v[128:131], v[84:87], a[96:99]\n\t"
    "buffer_load_dwordx4 v232, s[12:15], s28 offen offset:0 lds\n\t"
    "v_mfma_f32_16x16x32_bf16 a[128:131], v[128:131], v[88:91], a[128:131]\n\t"
    "ds_read_b64_tr_b16 v[100:101], v239 offset:32768\n\t"
    "v_mfma_f32_16x16x32_bf16 a[160:163], v[128:131], v[92:95], a[160:163]\n\t"
    "v_mfma_f32_16x16x32_bf16 a[4:7], v[132:135], v[72:75], a[4:7]\n\t"
    "ds_read_b64_tr_b16 v[102:103], v239 offset:36864\n\t"
    "buffer_load_dwordx4 v233, s[12:15], s28 offen offset:1024 lds\n\t"
    "v_mfma_f32_16x16x32_bf16 a[36:39], v[132:135], v[76:79], a[36:39]\n\t"
    "v_mfma_f32_16x16x32_bf16 a[68:71], v[132:135], v[80:83], a[68:71]\n\t"
    "v_mfma_f32_16x16x32_bf16 a[100:103], v[132:135], v[84:87], a[100:103]\n\t"
    "ds_read_b64_tr_b16 v[104:105], v238 offset:33024\n\t"
    "v_mfma_f32_16x16x32_bf16 a[132:135], v[132:135], v[88:91], a[132:135]\n\t"
    "buffer_load_dwordx4 v234, s[12:15], s28 offen offset:2048 lds\n\t"
    "v_mfma_f32_16x16x32_bf16 a[164:167], v[132:135], v[92:95], a[164:167]\n\t"
    "ds_read_b64_tr_b16 v[106:107], v238 offset:37120\n\t"
    "v_mfma_f32_16x16x32_bf16 a[8:11], v[136:139], v[72:75], a[8:11]\n\t"
    "v_mfma_f32_16x16x32_bf16 a[40:43], v[136:139], v[76:79], a[40:43]\n\t"
    "ds_read_b64_tr_b16 v[108:109], v239 offset:33024\n\t"
    "buffer_load_dwordx4 v235, s[12:15], s28 offen offset:3072 lds\n\t"
    "v_mfma_f32_16x16x32_bf16 a[72:75], v[136:139], v[80:83], a[72:75]\n\t"
    "v_mfma_f32_16x16x32_bf16 a[104:107], v[136:139], v[84:87], a[104:107]\n\t"
    "v_mfma_f32_16x16x32_bf16 a[136:139], v[136:139], v[88:91], a[136:139]\n\t"
    "ds_read_b64_tr_b16 v[110:111], v239 offset:37120\n\t"
    "v_mfma_f32_16x16x32_bf16 a[168:171], v[136:139], v[92:95], a[168:171]\n\t"
    "s_add_u32 s28, s28, 65536\n\t"
    "v_mfma_f32_16x16x32_bf16 a[12:15], v[140:143], v[72:75], a[12:15]\n\t"
    "ds_read_b64_tr_b16 v[112:113], v238 offset:33280\n\t"
    "v_mfma_f32_16x16x32_bf16 a[44:47], v[140:143], v[76:79], a[44:47]\n\t"
    "v_mfma_f32_16x16x32_bf16 a[76:79], v[140:143], v[80:83], a[76:79]\n\t"
    "ds_read_b64_tr_b16 v[114:115], v238 offset:37376\n\t"
    "s_add_u32 m0, s31, 16384\n\t"
    "v_mfma_f32_16x16x32_bf16 a[108:111], v[140:143], v[84:87], a[108:111]\n\t"
    "v_mfma_f32_16x16x32_bf16 a[140:143], v[140:143], v[88:91], a[140:143]\n\t"
    "v_mfma_f32_16x16x32_bf16 a[172:175], v[140:143], v[92:95], a[172:175]\n\t"
    "ds_read_b64_tr_b16 v[116:117], v239 offset:33280\n\t"
    "v_mfma_f32_16x16x32_bf16 a[16:19], v[144:147], v[72:75], a[16:19]\n\t"
    "buffer_load_dwordx4 v232, s[12:15], s28 offen offset:0 lds\n\t"
    "v_mfma_f32_16x16x32_bf16 a[48:51], v[144:147], v[76:79], a[48:51]\n\t"
    "ds_read_b64_tr_b16 v[118:119], v239 offset:37376\n\t"
    "v_mfma_f32_16x16x32_bf16 a[80:83], v[144:147], v[80:83], a[80:83]\n\t"
    "v_mfma_f32_16x16x32_bf16 a[112:115], v[144:147], v[84:87], a[112:115]\n\t"
    "ds_read_b64_tr_b16 v[120:121], v238 offset:33536\n\t"
    "buffer_load_dwordx4 v233, s[12:15], s28 offen offset:1024 lds\n\t"
    "v_mfma_f32_16x16x32_bf16 a[144:147], v[144:147], v[88:91], a[144:147]\n\t"
    "v_mfma_f32_16x16x32_bf16 a[176:179], v[144:147], v[92:95], a[176:179]\n\t"
    "v_mfma_f32_16x16x32_bf16 a[20:23], v[148:151], v[72:75], a[20:23]\n\t"
    "ds_read_b64_tr_b16 v[122:123], v238 offset:37632\n\t"
    "buffer_load_dwordx4 v234, s[12:15], s28 offen offset:2048 lds\n\t"
    "v_mfma_f32_16x16x32_bf16 a[52:55], v[148:151], v[76:79], a[52:55]\n\t"
    "v_mfma_f32_16x16x32_bf16 a[84:87], v[148:151], v[80:83], a[84:87]\n\t"
    "ds_read_b64_tr_b16 v[124:125], v239 offset:33536\n\t"
    "v_mfma_f32_16x16x32_bf16 a[116:119], v[148:151], v[84:87], a[116:119]\n\t"
    "v_mfma_f32_16x16x32_bf16 a[148:151], v[148:151], v[88:91], a[148:151]\n\t"
    "ds_read_b64_tr_b16 v[126:127], v239 offset:37632\n\t"
    "buffer_load_dwordx4 v235, s[12:15], s28 offen offset:3072 lds\n\t"
    "v_mfma_f32_16x16x32_bf16 a[180:183], v[148:151], v[92:95], a[180:183]\n\t"
    "v_mfma_f32_16x16x32_bf16 a[24:27], v[152:155], v[72:75], a[24:27]\n\t"
    "v_mfma_f32_16x16x32_bf16 a[56:59], v[152:155], v[76:79], a[56:59]\n\t"
    "s_add_u32 s28, s28, 65536\n\t"
    "v_mfma_f32_16x16x32_bf16 a[88:91], v[152:155], v[80:83], a[88:91]\n\t"
    "v_mfma_f32_16x16x32_bf16 a[120:123], v[152:155], v[84:87], a[120:123]\n\t"
    "v_mfma_f32_16x16x32_bf16 a[152:155], v[152:155], v[88:91], a[152:155]\n\t"
    "v_mfma_f32_16x16x32_bf16 a[184:187], v[152:155], v[92:95], a[184:187]\n\t"
    "v_mfma_f32_16x16x32_bf16 a[28:31], v[156:159], v[72:75], a[28:31]\n\t"
    "v_mfma_f32_16x16x32_bf16 a[60:63], v[156:159], v[76:79], a[60:63]\n\t"
    "v_mfma_f32_16x16x32_bf16 a[92:95], v[156:159], v[80:83], a[92:95]\n\t"
    "buffer_load_dword v221, v218, s[24:27], 0 offen offset:0 sc1\n\t"
    "v_mfma_f32_16x16x32_bf16 a[124:127], v[156:159], v[84:87], a[124:127]\n\t"
    "v_mfma_f32_16x16x32_bf16 a[156:159], v[156:159], v[88:91], a[156:159]\n\t"
    "v_mfma_f32_16x16x32_bf16 a[188:191], v[156:159], v[92:95], a[188:191]\n\t"
    "s_waitcnt vmcnt(0)\n\t"
    "v_cmp_ne_u32 vcc, v221, v209\n\t"
    "s_nop 1\n\t"
    "s_and_b32 s73, vcc_lo, 0x7\n\t"
    "s_cbranch_scc0 .LFLGOK_%=\n\t"
    "s_mov_b32 s74, 65536\n\t"
    ".LFLG_%=:\n\t"
    "buffer_load_dword v221, v218, s[24:27], 0 offen offset:0 sc1\n\t"
    "s_waitcnt vmcnt(0)\n\t"
    "v_cmp_ne_u32 vcc, v221, v209\n\t"
    "s_nop 1\n\t"
    "s_and_b32 s73, vcc_lo, 0x7\n\t"
    "s_cbranch_scc0 .LFLGOK_%=\n\t"
    "s_sleep 1\n\t"
    "s_sub_u32 s74, s74, 1\n\t"
    "s_cmp_lg_u32 s74, 0\n\t"
    "s_cbranch_scc1 .LFLG_%=\n\t"
    ".LFLGOK_%=:\n\t"
    "s_cmp_eq_u32 s95, s29\n\t"
    "s_addc_u32 s95, s95, 0\n\t"
    "s_mul_i32 s30, s95, 49152\n\t"
    "s_add_u32 s95, s95, 1\n\t"
    "buffer_load_dwordx4 v[0:3], v236, s[16:19], s30 offen offset:0 sc1\n\t"
    "buffer_load_dwordx4 v[4:7], v236, s[16:19], s30 offen offset:1024 sc1\n\t"
    "buffer_load_dwordx4 v[8:11], v236, s[16:19], s30 offen offset:2048 sc1\n\t"
    "buffer_load_dwordx4 v[12:15], v237, s[16:19], s30 offen offset:0 sc1\n\t"
    "buffer_load_dwordx4 v[16:19], v237, s[16:19], s30 offen offset:1024 sc1\n\t"
    "buffer_load_dwordx4 v[20:23], v237, s[16:19], s30 offen offset:2048 sc1\n\t"
    "s_add_u32 s30, s30, 6144\n\t"
    "buffer_load_dwordx4 v[24:27], v236, s[16:19], s30 offen offset:0 sc1\n\t"
    "buffer_load_dwordx4 v[28:31], v236, s[16:19], s30 offen offset:1024 sc1\n\t"
    "buffer_load_dwordx4 v[32:35], v236, s[16:19], s30 offen offset:2048 sc1\n\t"
    "buffer_load_dwordx4 v[36:39], v237, s[16:19], s30 offen offset:0 sc1\n\t"
    "buffer_load_dwordx4 v[40:43], v237, s[16:19], s30 offen offset:1024 sc1\n\t"
    "buffer_load_dwordx4 v[44:47], v237, s[16:19], s30 offen offset:2048 sc1\n\t"
    "s_add_u32 s30, s30, 6144\n\t"
    "buffer_load_dwordx4 v[48:51], v236, s[16:19], s30 offen offset:0 sc1\n\t"
    "buffer_load_dwordx4 v[52:55], v236, s[16:19], s30 offen offset:1024 sc1\n\t"
    "buffer_load_dwordx4 v[56:59], v236, s[16:19], s30 offen offset:2048 sc1\n\t"
    "buffer_load_dwordx4 v[60:63], v237, s[16:19], s30 offen offset:0 sc1\n\t"
    "buffer_load_dwordx4 v[64:67], v237, s[16:19], s30 offen offset:1024 sc1\n\t"
    "buffer_load_dwordx4 v[68:71], v237, s[16:19], s30 offen offset:2048 sc1\n\t"
    "s_waitcnt lgkmcnt(0)\n\t"
    "s_waitcnt vmcnt(17)\n\t"
    "v_mfma_f32_16x16x32_bf16 a[0:3], v[96:99], v[0:3], a[0:3]\n\t"
    "ds_read_b64_tr_b16 v[128:129], v238 offset:40960\n\t"
    "s_add_u32 m0, s31, 24576\n\t"
    "s_waitcnt vmcnt(16)\n\t"
    "v_mfma_f32_16x16x32_bf16 a[32:35], v[96:99], v[4:7], a[32:35]\n\t"
    "s_waitcnt vmcnt(15)\n\t"
    "v_mfma_f32_16x16x32_bf16 a[64:67], v[96:99], v[8:11], a[64:67]\n\t"
    "ds_read_b64_tr_b16 v[130:131], v238 offset:45056\n\t"
    "s_waitcnt vmcnt(14)\n\t"
    "v_mfma_f32_16x16x32_bf16 a[96:99], v[96:99], v[12:15], a[96:99]\n\t"
    "buffer_load_dwordx4 v232, s[12:15], s28 offen offset:0 lds\n\t"
    "s_waitcnt vmcnt(14)\n\t"
    "v_mfma_f32_16x16x32_bf16 a[128:131], v[96:99], v[16:19], a[128:131]\n\t"
    "ds_read_b64_tr_b16 v[132:133], v239 offset:40960\n\t"
    "s_waitcnt vmcnt(13)\n\t"
    "v_mfma_f32_16x16x32_bf16 a[160:163], v[96:99], v[20:23], a[160:163]\n\t"
    "v_mfma_f32_16x16x32_bf16 a[4:7], v[100:103], v[0:3], a[4:7]\n\t"
    "ds_read_b64_tr_b16 v[134:135], v239 offset:45056\n\t"
    "buffer_load_dwordx4 v233, s[12:15], s28 offen offset:1024 lds\n\t"
    "v_mfma_f32_16x16x32_bf16 a[36:39], v[100:103], v[4:7], a[36:39]\n\t"
    "v_mfma_f32_16x16x32_bf16 a[68:71], v[100:103], v[8:11], a[68:71]\n\t"
    "v_mfma_f32_16x16x32_bf16 a[100:103], v[100:103], v[12:15], a[100:103]\n\t"
    "ds_read_b64_tr_b16 v[136:137], v238 offset:41216\n\t"
    "buffer_load_dwordx4 v234, s[12:15], s28 offen offset:2048 lds\n\t"
    "v_mfma_f32_16x16x32_bf16 a[132:135], v[100:103], v[16:19], a[132:135]\n\t"
    "v_mfma_f32_16x16x32_bf16 a[164:167], v[100:103], v[20:23], a[164:167]\n\t"
    "ds_read_b64_tr_b16 v[138:139], v238 offset:45312\n\t"
    "v_mfma_f32_16x16x32_bf16 a[8:11], v[104:107], v[0:3], a[8:11]\n\t"
    "buffer_load_dwordx4 v235, s[12:15], s28 offen offset:3072 lds\n\t"
    "v_mfma_f32_16x16x32_bf16 a[40:43], v[104:107], v[4:7], a[40:43]\n\t"
    "ds_read_b64_tr_b16 v[140:141], v239 offset:41216\n\t"
    "v_mfma_f32_16x16x32_bf16 a[72:75], v[104:107], v[8:11], a[72:75]\n\t"
    "v_mfma_f32_16x16x32_bf16 a[104:107], v[104:107], v[12:15], a[104:107]\n\t"
    "v_mfma_f32_16x16x32_bf16 a[136:139], v[104:107], v[16:19], a[136:139]\n\t"
    "ds_read_b64_tr_b16 v[142:143], v239 offset:45312\n\t"
    "s_cmp_eq_u32 s94, s29\n\t"
    "s_addc_u32 s94, s94, 0\n\t"
    "v_mfma_f32_16x16x32_bf16 a[168:171], v[104:107], v[20:23], a[168:171]\n\t"
    "v_mfma_f32_16x16x32_bf16 a[12:15], v[108:111], v[0:3], a[12:15]\n\t"
    "ds_read_b64_tr_b16 v[144:145], v238 offset:41472\n\t"
    "v_mfma_f32_16x16x32_bf16 a[44:47], v[108:111], v[4:7], a[44:47]\n\t"
    "s_mul_i32 s28, s94, 524288\n\t"
    "v_mfma_f32_16x16x32_bf16 a[76:79], v[108:111], v[8:11], a[76:79]\n\t"
    "ds_read_b64_tr_b16 v[146:147], v238 offset:45568\n\t"
    "v_mfma_f32_16x16x32_bf16 a[108:111], v[108:111], v[12:15], a[108:111]\n\t"
    "v_mfma_f32_16x16x32_bf16 a[140:143], v[108:111], v[16:19], a[140:143]\n\t"
    "s_add_u32 s94, s94, 1\n\t"
    "v_mfma_f32_16x16x32_bf16 a[172:175], v[108:111], v[20:23], a[172:175]\n\t"
    "ds_read_b64_tr_b16 v[148:149], v239 offset:41472\n\t"
    "v_mfma_f32_16x16x32_bf16 a[16:19], v[112:115], v[0:3], a[16:19]\n\t"
    "v_mfma_f32_16x16x32_bf16 a[48:51], v[112:115], v[4:7], a[48:51]\n\t"
    "ds_read_b64_tr_b16 v[150:151], v239 offset:45568\n\t"
    "s_add_u32 s30, s30, 6144\n\t"
    "v_mfma_f32_16x16x32_bf16 a[80:83], v[112:115], v[8:11], a[80:83]\n\t"
    "v_mfma_f32_16x16x32_bf16 a[112:115], v[112:115], v[12:15], a[112:115]\n\t"
    "ds_read_b64_tr_b16 v[152:153], v238 offset:41728\n\t"
    "v_mfma_f32_16x16x32_bf16 a[144:147], v[112:115], v[16:19], a[144:147]\n\t"
    "buffer_load_dwordx4 v[72:75], v236, s[16:19], s30 offen offset:0 sc1\n\t"
    "v_mfma_f32_16x16x32_bf16 a[176:179], v[112:115], v[20:23], a[176:179]\n\t"
    "v_mfma_f32_16x16x32_bf16 a[20:23], v[116:119], v[0:3], a[20:23]\n\t"
    "ds_read_b64_tr_b16 v[154:155], v238 offset:45824\n\t"
    "v_mfma_f32_16x16x32_bf16 a[52:55], v[116:119], v[4:7], a[52:55]\n\t"
    "v_mfma_f32_16x16x32_bf16 a[84:87], v[116:119], v[8:11], a[84:87]\n\t"
    "ds_read_b64_tr_b16 v[156:157], v239 offset:41728\n\t"
    "buffer_load_dwordx4 v[76:79], v236, s[16:19], s30 offen offset:1024 sc1\n\t"
    "v_mfma_f32_16x16x32_bf16 a[116:119], v[116:119], v[12:15], a[116:119]\n\t"
    "v_mfma_f32_16x16x32_bf16 a[148:151], v[116:119], v[16:19], a[148:151]\n\t"
    "ds_read_b64_tr_b16 v[158:159], v239 offset:45824\n\t"
    "v_mfma_f32_16x16x32_bf16 a[180:183], v[116:119], v[20:23], a[180:183]\n\t"
    "buffer_load_dwordx4 v[80:83], v236, s[16:19], s30 offen offset:2048 sc1\n\t"
    "v_mfma_f32_16x16x32_bf16 a[24:27], v[120:123], v[0:3], a[24:27]\n\t"
    "v_mfma_f32_16x16x32_bf16 a[56:59], v[120:123], v[4:7], a[56:59]\n\t"
    "v_mfma_f32_16x16x32_bf16 a[88:91], v[120:123], v[8:11], a[88:91]\n\t"
    "buffer_load_dwordx4 v[84:87], v237, s[16:19], s30 offen offset:0 sc1\n\t"
    "v_mfma_f32_16x16x32_bf16 a[120:123], v[120:123], v[12:15], a[120:123]\n\t"
    "v_mfma_f32_16x16x32_bf16 a[152:155], v[120:123], v[16:19], a[152:155]\n\t"
    "v_mfma_f32_16x16x32_bf16 a[184:187], v[120:123], v[20:23], a[184:187]\n\t"
    "buffer_load_dwordx4 v[88:91], v237, s[16:19], s30 offen offset:1024 sc1\n\t"
    "v_mfma_f32_16x16x32_bf16 a[28:31], v[124:127], v[0:3], a[28:31]\n\t"
    "v_mfma_f32_16x16x32_bf16 a[60:63], v[124:127], v[4:7], a[60:63]\n\t"
    "v_mfma_f32_16x16x32_bf16 a[92:95], v[124:127], v[8:11], a[92:95]\n\t"
    "buffer_load_dwordx4 v[92:95], v237, s[16:19], s30 offen offset:2048 sc1\n\t"
    "v_mfma_f32_16x16x32_bf16 a[124:127], v[124:127], v[12:15], a[124:127]\n\t"
    "v_mfma_f32_16x16x32_bf16 a[156:159], v[124:127], v[16:19], a[156:159]\n\t"
    "v_mfma_f32_16x16x32_bf16 a[188:191], v[124:127], v[20:23], a[188:191]\n\t"
    "s_waitcnt vmcnt(42) lgkmcnt(0)\n\t"
    "s_barrier\n\t"
    "s_waitcnt vmcnt(21)\n\t"
    "v_mfma_f32_16x16x32_bf16 a[0:3], v[128:131], v[24:27], a[0:3]\n\t"
    "ds_read_b64_tr_b16 v[96:97], v238 offset:49152\n\t"
    "s_add_u32 m0, s31, 32768\n\t"
    "s_waitcnt vmcnt(20)\n\t"
    "v_mfma_f32_16x16x32_bf16 a[32:35], v[128:131], v[28:31], a[32:35]\n\t"
    "s_waitcnt vmcnt(19)\n\t"
    "v_mfma_f32_16x16x32_bf16 a[64:67], v[128:131], v[32:35], a[64:67]\n\t"
    "ds_read_b64_tr_b16 v[98:99], v238 offset:53248\n\t"
    "s_waitcnt vmcnt(18)\n\t"
    "v_mfma_f32_16x16x32_bf16 a[96:99], v[128:131], v[36:39], a[96:99]\n\t"
    "buffer_load_dwordx4 v232, s[12:15], s28 offen offset:0 lds\n\t"
    "s_waitcnt vmcnt(18)\n\t"
    "v_mfma_f32_16x16x32_bf16 a[128:131], v[128:131], v[40:43], a[128:131]\n\t"
    "ds_read_b64_tr_b16 v[100:101], v239 offset:49152\n\t"
    "s_waitcnt vmcnt(17)\n\t"
    "v_mfma_f32_16x16x32_bf16 a[160:163], v[128:131], v[44:47], a[160:163]\n\t"
    "v_mfma_f32_16x16x32_bf16 a[4:7], v[132:135], v[24:27], a[4:7]\n\t"
    "ds_read_b64_tr_b16 v[102:103], v239 offset:53248\n\t"
    "buffer_load_dwordx4 v233, s[12:15], s28 offen offset:1024 lds\n\t"
    "v_mfma_f32_16x16x32_bf16 a[36:39], v[132:135], v[28:31], a[36:39]\n\t"
    "v_mfma_f32_16x16x32_bf16 a[68:71], v[132:135], v[32:35], a[68:71]\n\t"
    "v_mfma_f32_16x16x32_bf16 a[100:103], v[132:135], v[36:39], a[100:103]\n\t"
    "ds_read_b64_tr_b16 v[104:105], v238 offset:49408\n\t"
    "buffer_load_dwordx4 v234, s[12:15], s28 offen offset:2048 lds\n\t"
    "v_mfma_f32_16x16x32_bf16 a[132:135], v[132:135], v[40:43], a[132:135]\n\t"
    "v_mfma_f32_16x16x32_bf16 a[164:167], v[132:135], v[44:47], a[164:167]\n\t"
    "ds_read_b64_tr_b16 v[106:107], v238 offset:53504\n\t"
    "v_mfma_f32_16x16x32_bf16 a[8:11], v[136:139], v[24:27], a[8:11]\n\t"
    "buffer_load_dwordx4 v235, s[12:15], s28 offen offset:3072 lds\n\t"
    "v_mfma_f32_16x16x32_bf16 a[40:43], v[136:139], v[28:31], a[40:43]\n\t"
    "ds_read_b64_tr_b16 v[108:109], v239 offset:49408\n\t"
    "v_mfma_f32_16x16x32_bf16 a[72:75], v[136:139], v[32:35], a[72:75]\n\t"
    "v_mfma_f32_16x16x32_bf16 a[104:107], v[136:139], v[36:39], a[104:107]\n\t"
    "v_mfma_f32_16x16x32_bf16 a[136:139], v[136:139], v[40:43], a[136:139]\n\t"
    "ds_read_b64_tr_b16 v[110:111], v239 offset:53504\n\t"
    "s_add_u32 s28, s28, 65536\n\t"
    "v_mfma_f32_16x16x32_bf16 a[168:171], v[136:139], v[44:47], a[168:171]\n\t"
    "v_mfma_f32_16x16x32_bf16 a[12:15], v[140:143], v[24:27], a[12:15]\n\t"
    "ds_read_b64_tr_b16 v[112:113], v238 offset:49664\n\t"
    "v_mfma_f32_16x16x32_bf16 a[44:47], v[140:143], v[28:31], a[44:47]\n\t"
    "s_cmp_eq_u32 s95, s29\n\t"
    "s_addc_u32 s95, s95, 0\n\t"
    "v_mfma_f32_16x16x32_bf16 a[76:79], v[140:143], v[32:35], a[76:79]\n\t"
    "ds_read_b64_tr_b16 v[114:115], v238 offset:53760\n\t"
    "v_mfma_f32_16x16x32_bf16 a[108:111], v[140:143], v[36:39], a[108:111]\n\t"
    "v_mfma_f32_16x16x32_bf16 a[140:143], v[140:143], v[40:43], a[140:143]\n\t"
    "s_mul_i32 s30, s95, 49152\n\t"
    "v_mfma_f32_16x16x32_bf16 a[172:175], v[140:143], v[44:47], a[172:175]\n\t"
    "ds_read_b64_tr_b16 v[116:117], v239 offset:49664\n\t"
    "v_mfma_f32_16x16x32_bf16 a[16:19], v[144:147], v[24:27], a[16:19]\n\t"
    "v_mfma_f32_16x16x32_bf16 a[48:51], v[144:147], v[28:31], a[48:51]\n\t"
    "ds_read_b64_tr_b16 v[118:119], v239 offset:53760\n\t"
    "s_add_u32 s95, s95, 1\n\t"
    "v_mfma_f32_16x16x32_bf16 a[80:83], v[144:147], v[32:35], a[80:83]\n\t"
    "v_mfma_f32_16x16x32_bf16 a[112:115], v[144:147], v[36:39], a[112:115]\n\t"
    "ds_read_b64_tr_b16 v[120:121], v238 offset:49920\n\t"
    "v_mfma_f32_16x16x32_bf16 a[144:147], v[144:147], v[40:43], a[144:147]\n\t"
    "buffer_load_dwordx4 v[0:3], v236, s[16:19], s30 offen offset:0 sc1\n\t"
    "v_mfma_f32_16x16x32_bf16 a[176:179], v[144:147], v[44:47], a[176:179]\n\t"
    "v_mfma_f32_16x16x32_bf16 a[20:23], v[148:151], v[24:27], a[20:23]\n\t"
    "ds_read_b64_tr_b16 v[122:123], v238 offset:54016\n\t"
    "v_mfma_f32_16x16x32_bf16 a[52:55], v[148:151], v[28:31], a[52:55]\n\t"
    "v_mfma_f32_16x16x32_bf16 a[84:87], v[148:151], v[32:35], a[84:87]\n\t"
    "ds_read_b64_tr_b16 v[124:125], v239 offset:49920\n\t"
    "buffer_load_dwordx4 v[4:7], v236, s[16:19], s30 offen offset:1024 sc1\n\t"
    "v_mfma_f32_16x16x32_bf16 a[116:119], v[148:151], v[36:39], a[116:119]\n\t"
    "v_mfma_f32_16x16x32_bf16 a[148:151], v[148:151], v[40:43], a[148:151]\n\t"
    "ds_read_b64_tr_b16 v[126:127], v239 offset:54016\n\t"
    "v_mfma_f32_16x16x32_bf16 a[180:183], v[148:151], v[44:47], a[180:183]\n\t"
    "buffer_load_dwordx4 v[8:11], v236, s[16:19], s30 offen offset:2048 sc1\n\t"
    "v_mfma_f32_16x16x32_bf16 a[24:27], v[152:155], v[24:27], a[24:27]\n\t"
    "v_mfma_f32_16x16x32_bf16 a[56:59], v[152:155], v[28:31], a[56:59]\n\t"
    "v_mfma_f32_16x16x32_bf16 a[88:91], v[152:155], v[32:35], a[88:91]\n\t"
    "buffer_load_dwordx4 v[12:15], v237, s[16:19], s30 offen offset:0 sc1\n\t"
    "v_mfma_f32_16x16x32_bf16 a[120:123], v[152:155], v[36:39], a[120:123]\n\t"
    "v_mfma_f32_16x16x32_bf16 a[152:155], v[152:155], v[40:43], a[152:155]\n\t"
    "v_mfma_f32_16x16x32_bf16 a[184:187], v[152:155], v[44:47], a[184:187]\n\t"
    "buffer_load_dwordx4 v[16:19], v237, s[16:19], s30 offen offset:1024 sc1\n\t"
    "v_mfma_f32_16x16x32_bf16 a[28:31], v[156:159], v[24:27], a[28:31]\n\t"
    "v_mfma_f32_16x16x32_bf16 a[60:63], v[156:159], v[28:31], a[60:63]\n\t"
    "v_mfma_f32_16x16x32_bf16 a[92:95], v[156:159], v[32:35], a[92:95]\n\t"
    "buffer_load_dwordx4 v[20:23], v237, s[16:19], s30 offen offset:2048 sc1\n\t"
    "v_mfma_f32_16x16x32_bf16 a[124:127], v[156:159], v[36:39], a[124:127]\n\t"
    "v_mfma_f32_16x16x32_bf16 a[156:159], v[156:159], v[40:43], a[156:159]\n\t"
    "v_mfma_f32_16x16x32_bf16 a[188:191], v[156:159], v[44:47], a[188:191]\n\t"
    "s_waitcnt vmcnt(20) lgkmcnt(0)\n\t"
    "v_mfma_f32_16x16x32_bf16 a[0:3], v[96:99], v[48:51], a[0:3]\n\t"
    "ds_read_b64_tr_b16 v[128:129], v238 offset:57344\n\t"
    "s_add_u32 m0, s31, 40960\n\t"
    "v_mfma_f32_16x16x32_bf16 a[32:35], v[96:99], v[52:55], a[32:35]\n\t"
    "v_mfma_f32_16x16x32_bf16 a[64:67], v[96:99], v[56:59], a[64:67]\n\t"
    "ds_read_b64_tr_b16 v[130:131], v238 offset:61440\n\t"
    "v_mfma_f32_16x16x32_bf16 a[96:99], v[96:99], v[60:63], a[96:99]\n\t"
    "buffer_load_dwordx4 v232, s[12:15], s28 offen offset:0 lds\n\t"
    "v_mfma_f32_16x16x32_bf16 a[128:131], v[96:99], v[64:67], a[128:131]\n\t"
    "ds_read_b64_tr_b16 v[132:133], v239 offset:57344\n\t"
    "v_mfma_f32_16x16x32_bf16 a[160:163], v[96:99], v[68:71], a[160:163]\n\t"
    "v_mfma_f32_16x16x32_bf16 a[4:7], v[100:103], v[48:51], a[4:7]\n\t"
    "ds_read_b64_tr_b16 v[134:135], v239 offset:61440\n\t"
    "v_mfma_f32_16x16x32_bf16 a[36:39], v[100:103], v[52:55], a[36:39]\n\t"
    "buffer_load_dwordx4 v233, s[12:15], s28 offen offset:1024 lds\n\t"
    "v_mfma_f32_16x16x32_bf16 a[68:71], v[100:103], v[56:59], a[68:71]\n\t"
    "v_mfma_f32_16x16x32_bf16 a[100:103], v[100:103], v[60:63], a[100:103]\n\t"
    "ds_read_b64_tr_b16 v[136:137], v238 offset:57600\n\t"
    "v_mfma_f32_16x16x32_bf16 a[132:135], v[100:103], v[64:67], a[132:135]\n\t"
    "v_mfma_f32_16x16x32_bf16 a[164:167], v[100:103], v[68:71], a[164:167]\n\t"
    "ds_read_b64_tr_b16 v[138:139], v238 offset:61696\n\t"
    "buffer_load_dwordx4 v234, s[12:15], s28 offen offset:2048 lds\n\t"
    "v_mfma_f32_16x16x32_bf16 a[8:11], v[104:107], v[48:51], a[8:11]\n\t"
    "v_mfma_f32_16x16x32_bf16 a[40:43], v[104:107], v[52:55], a[40:43]\n\t"
    "ds_read_b64_tr_b16 v[140:141], v239 offset:57600\n\t"
    "v_mfma_f32_16x16x32_bf16 a[72:75], v[104:107], v[56:59], a[72:75]\n\t"
    "buffer_load_dwordx4 v235, s[12:15], s28 offen offset:3072 lds\n\t"
    "v_mfma_f32_16x16x32_bf16 a[104:107], v[104:107], v[60:63], a[104:107]\n\t"
    "v_mfma_f32_16x16x32_bf16 a[136:139], v[104:107], v[64:67], a[136:139]\n\t"
    "ds_read_b64_tr_b16 v[142:143], v239 offset:61696\n\t"
    "v_mfma_f32_16x16x32_bf16 a[168:171], v[104:107], v[68:71], a[168:171]\n\t"
    "v_mfma_f32_16x16x32_bf16 a[12:15], v[108:111], v[48:51], a[12:15]\n\t"
    "ds_read_b64_tr_b16 v[144:145], v238 offset:57856\n\t"
    "s_add_u32 s28, s28, 65536\n\t"
    "v_mfma_f32_16x16x32_bf16 a[44:47], v[108:111], v[52:55], a[44:47]\n\t"
    "v_mfma_f32_16x16x32_bf16 a[76:79], v[108:111], v[56:59], a[76:79]\n\t"
    "ds_read_b64_tr_b16 v[146:147], v238 offset:61952\n\t"
    "v_mfma_f32_16x16x32_bf16 a[108:111], v[108:111], v[60:63], a[108:111]\n\t"
    "v_mfma_f32_16x16x32_bf16 a[140:143], v[108:111], v[64:67], a[140:143]\n\t"
    "s_add_u32 s30, s30, 6144\n\t"
    "v_mfma_f32_16x16x32_bf16 a[172:175], v[108:111], v[68:71], a[172:175]\n\t"
    "ds_read_b64_tr_b16 v[148:149], v239 offset:57856\n\t"
    "v_mfma_f32_16x16x32_bf16 a[16:19], v[112:115], v[48:51], a[16:19]\n\t"
    "v_mfma_f32_16x16x32_bf16 a[48:51], v[112:115], v[52:55], a[48:51]\n\t"
    "ds_read_b64_tr_b16 v[150:151], v239 offset:61952\n\t"
    "buffer_load_dwordx4 v[24:27], v236, s[16:19], s30 offen offset:0 sc1\n\t"
    "v_mfma_f32_16x16x32_bf16 a[80:83], v[112:115], v[56:59], a[80:83]\n\t"
    "v_mfma_f32_16x16x32_bf16 a[112:115], v[112:115], v[60:63], a[112:115]\n\t"
    "ds_read_b64_tr_b16 v[152:153], v238 offset:58112\n\t"
    "v_mfma_f32_16x16x32_bf16 a[144:147], v[112:115], v[64:67], a[144:147]\n\t"
    "v_mfma_f32_16x16x32_bf16 a[176:179], v[112:115], v[68:71], a[176:179]\n\t"
    "buffer_load_dwordx4 v[28:31], v236, s[16:19], s30 offen offset:1024 sc1\n\t"
    "v_mfma_f32_16x16x32_bf16 a[20:23], v[116:119], v[48:51], a[20:23]\n\t"
    "ds_read_b64_tr_b16 v[154:155], v238 offset:62208\n\t"
    "v_mfma_f32_16x16x32_bf16 a[52:55], v[116:119], v[52:55], a[52:55]\n\t"
    "v_mfma_f32_16x16x32_bf16 a[84:87], v[116:119], v[56:59], a[84:87]\n\t"
    "ds_read_b64_tr_b16 v[156:157], v239 offset:58112\n\t"
    "v_mfma_f32_16x16x32_bf16 a[116:119], v[116:119], v[60:63], a[116:119]\n\t"
    "buffer_load_dwordx4 v[32:35], v236, s[16:19], s30 offen offset:2048 sc1\n\t"
    "v_mfma_f32_16x16x32_bf16 a[148:151], v[116:119], v[64:67], a[148:151]\n\t"
    "ds_read_b64_tr_b16 v[158:159], v239 offset:62208\n\t"
    "v_mfma_f32_16x16x32_bf16 a[180:183], v[116:119], v[68:71], a[180:183]\n\t"
    "v_mfma_f32_16x16x32_bf16 a[24:27], v[120:123], v[48:51], a[24:27]\n\t"
    "buffer_load_dwordx4 v[36:39], v237, s[16:19], s30 offen offset:0 sc1\n\t"
    "v_mfma_f32_16x16x32_bf16 a[56:59], v[120:123], v[52:55], a[56:59]\n\t"
    "v_mfma_f32_16x16x32_bf16 a[88:91], v[120:123], v[56:59], a[88:91]\n\t"
    "v_mfma_f32_16x16x32_bf16 a[120:123], v[120:123], v[60:63], a[120:123]\n\t"
    "v_mfma_f32_16x16x32_bf16 a[152:155], v[120:123], v[64:67], a[152:155]\n\t"
    "buffer_load_dwordx4 v[40:43], v237, s[16:19], s30 offen offset:1024 sc1\n\t"
    "v_mfma_f32_16x16x32_bf16 a[184:187], v[120:123], v[68:71], a[184:187]\n\t"
    "v_mfma_f32_16x16x32_bf16 a[28:31], v[124:127], v[48:51], a[28:31]\n\t"
    "v_mfma_f32_16x16x32_bf16 a[60:63], v[124:127], v[52:55], a[60:63]\n\t"
    "v_mfma_f32_16x16x32_bf16 a[92:95], v[124:127], v[56:59], a[92:95]\n\t"
    "buffer_load_dwordx4 v[44:47], v237, s[16:19], s30 offen offset:2048 sc1\n\t"
    "v_mfma_f32_16x16x32_bf16 a[124:127], v[124:127], v[60:63], a[124:127]\n\t"
    "v_mfma_f32_16x16x32_bf16 a[156:159], v[124:127], v[64:67], a[156:159]\n\t"
    "v_mfma_f32_16x16x32_bf16 a[188:191], v[124:127], v[68:71], a[188:191]\n\t"
    "s_waitcnt vmcnt(20) lgkmcnt(0)\n\t"
    "s_barrier\n\t"
    "v_mfma_f32_16x16x32_bf16 a[0:3], v[128:131], v[72:75], a[0:3]\n\t"
    "ds_read_b64_tr_b16 v[96:97], v238 offset:0\n\t"
    "s_add_u32 m0, s31, 49152\n\t"
    "v_mfma_f32_16x16x32_bf16 a[32:35], v[128:131], v[76:79], a[32:35]\n\t"
    "v_mfma_f32_16x16x32_bf16 a[64:67], v[128:131], v[80:83], a[64:67]\n\t"
    "ds_read_b64_tr_b16 v[98:99], v238 offset:4096\n\t"
    "v_mfma_f32_16x16x32_bf16 a[96:99], v[128:131], v[84:87], a[96:99]\n\t"
    "buffer_load_dwordx4 v232, s[12:15], s28 offen offset:0 lds\n\t"
    "v_mfma_f32_16x16x32_bf16 a[128:131], v[128:131], v[88:91], a[128:131]\n\t"
    "ds_read_b64_tr_b16 v[100:101], v239 offset:0\n\t"
    "v_mfma_f32_16x16x32_bf16 a[160:163], v[128:131], v[92:95], a[160:163]\n\t"
    "v_mfma_f32_16x16x32_bf16 a[4:7], v[132:135], v[72:75], a[4:7]\n\t"
    "ds_read_b64_tr_b16 v[102:103], v239 offset:4096\n\t"
    "buffer_load_dwordx4 v233, s[12:15], s28 offen offset:1024 lds\n\t"
    "v_mfma_f32_16x16x32_bf16 a[36:39], v[132:135], v[76:79], a[36:39]\n\t"
    "v_mfma_f32_16x16x32_bf16 a[68:71], v[132:135], v[80:83], a[68:71]\n\t"
    "v_mfma_f32_16x16x32_bf16 a[100:103], v[132:135], v[84:87], a[100:103]\n\t"
    "ds_read_b64_tr_b16 v[104:105], v238 offset:256\n\t"
    "v_mfma_f32_16x16x32_bf16 a[132:135], v[132:135], v[88:91], a[132:135]\n\t"
    "buffer_load_dwordx4 v234, s[12:15], s28 offen offset:2048 lds\n\t"
    "v_mfma_f32_16x16x32_bf16 a[164:167], v[132:135], v[92:95], a[164:167]\n\t"
    "ds_read_b64_tr_b16 v[106:107], v238 offset:4352\n\t"
    "v_mfma_f32_16x16x32_bf16 a[8:11], v[136:139], v[72:75], a[8:11]\n\t"
    "v_mfma_f32_16x16x32_bf16 a[40:43], v[136:139], v[76:79], a[40:43]\n\t"
    "ds_read_b64_tr_b16 v[108:109], v239 offset:256\n\t"
    "buffer_load_dwordx4 v235, s[12:15], s28 offen offset:3072 lds\n\t"
    "v_mfma_f32_16x16x32_bf16 a[72:75], v[136:139], v[80:83], a[72:75]\n\t"
    "v_mfma_f32_16x16x32_bf16 a[104:107], v[136:139], v[84:87], a[104:107]\n\t"
    "v_mfma_f32_16x16x32_bf16 a[136:139], v[136:139], v[88:91], a[136:139]\n\t"
    "ds_read_b64_tr_b16 v[110:111], v239 offset:4352\n\t"
    "v_mfma_f32_16x16x32_bf16 a[168:171], v[136:139], v[92:95], a[168:171]\n\t"
    "s_add_u32 s28, s28, 65536\n\t"
    "v_mfma_f32_16x16x32_bf16 a[12:15], v[140:143], v[72:75], a[12:15]\n\t"
    "ds_read_b64_tr_b16 v[112:113], v238 offset:512\n\t"
    "v_mfma_f32_16x16x32_bf16 a[44:47], v[140:143], v[76:79], a[44:47]\n\t"
    "v_mfma_f32_16x16x32_bf16 a[76:79], v[140:143], v[80:83], a[76:79]\n\t"
    "ds_read_b64_tr_b16 v[114:115], v238 offset:4608\n\t"
    "s_add_u32 s30, s30, 6144\n\t"
    "v_mfma_f32_16x16x32_bf16 a[108:111], v[140:143], v[84:87], a[108:111]\n\t"
    "v_mfma_f32_16x16x32_bf16 a[140:143], v[140:143], v[88:91], a[140:143]\n\t"
    "v_mfma_f32_16x16x32_bf16 a[172:175], v[140:143], v[92:95], a[172:175]\n\t"
    "ds_read_b64_tr_b16 v[116:117], v239 offset:512\n\t"
    "v_mfma_f32_16x16x32_bf16 a[16:19], v[144:147], v[72:75], a[16:19]\n\t"
    "buffer_load_dwordx4 v[48:51], v236, s[16:19], s30 offen offset:0 sc1\n\t"
    "v_mfma_f32_16x16x32_bf16 a[48:51], v[144:147], v[76:79], a[48:51]\n\t"
    "ds_read_b64_tr_b16 v[118:119], v239 offset:4608\n\t"
    "v_mfma_f32_16x16x32_bf16 a[80:83], v[144:147], v[80:83], a[80:83]\n\t"
    "v_mfma_f32_16x16x32_bf16 a[112:115], v[144:147], v[84:87], a[112:115]\n\t"
    "ds_read_b64_tr_b16 v[120:121], v238 offset:768\n\t"
    "buffer_load_dwordx4 v[52:55], v236, s[16:19], s30 offen offset:1024 sc1\n\t"
    "v_mfma_f32_16x16x32_bf16 a[144:147], v[144:147], v[88:91], a[144:147]\n\t"
    "v_mfma_f32_16x16x32_bf16 a[176:179], v[144:147], v[92:95], a[176:179]\n\t"
    "v_mfma_f32_16x16x32_bf16 a[20:23], v[148:151], v[72:75], a[20:23]\n\t"
    "ds_read_b64_tr_b16 v[122:123], v238 offset:4864\n\t"
    "buffer_load_dwordx4 v[56:59], v236, s[16:19], s30 offen offset:2048 sc1\n\t"
    "v_mfma_f32_16x16x32_bf16 a[52:55], v[148:151], v[76:79], a[52:55]\n\t"
    "v_mfma_f32_16x16x32_bf16 a[84:87], v[148:151], v[80:83], a[84:87]\n\t"
    "ds_read_b64_tr_b16 v[124:125], v239 offset:768\n\t"
    "v_mfma_f32_16x16x32_bf16 a[116:119], v[148:151], v[84:87], a[116:119]\n\t"
    "v_mfma_f32_16x16x32_bf16 a[148:151], v[148:151], v[88:91], a[148:151]\n\t"
    "ds_read_b64_tr_b16 v[126:127], v239 offset:4864\n\t"
    "buffer_load_dwordx4 v[60:63], v237, s[16:19], s30 offen offset:0 sc1\n\t"
    "v_mfma_f32_16x16x32_bf16 a[180:183], v[148:151], v[92:95], a[180:183]\n\t"
    "v_mfma_f32_16x16x32_bf16 a[24:27], v[152:155], v[72:75], a[24:27]\n\t"
    "v_mfma_f32_16x16x32_bf16 a[56:59], v[152:155], v[76:79], a[56:59]\n\t"
    "buffer_load_dwordx4 v[64:67], v237, s[16:19], s30 offen offset:1024 sc1\n\t"
    "v_mfma_f32_16x16x32_bf16 a[88:91], v[152:155], v[80:83], a[88:91]\n\t"
    "v_mfma_f32_16x16x32_bf16 a[120:123], v[152:155], v[84:87], a[120:123]\n\t"
    "v_mfma_f32_16x16x32_bf16 a[152:155], v[152:155], v[88:91], a[152:155]\n\t"
    "v_mfma_f32_16x16x32_bf16 a[184:187], v[152:155], v[92:95], a[184:187]\n\t"
    "buffer_load_dwordx4 v[68:71], v237, s[16:19], s30 offen offset:2048 sc1\n\t"
    "v_mfma_f32_16x16x32_bf16 a[28:31], v[156:159], v[72:75], a[28:31]\n\t"
    "v_mfma_f32_16x16x32_bf16 a[60:63], v[156:159], v[76:79], a[60:63]\n\t"
    "v_mfma_f32_16x16x32_bf16 a[92:95], v[156:159], v[80:83], a[92:95]\n\t"
    "buffer_load_dword v221, v218, s[24:27], 0 offen offset:0 sc1\n\t"
    "v_mfma_f32_16x16x32_bf16 a[124:127], v[156:159], v[84:87], a[124:127]\n\t"
    "v_mfma_f32_16x16x32_bf16 a[156:159], v[156:159], v[88:91], a[156:159]\n\t"
    "v_mfma_f32_16x16x32_bf16 a[188:191], v[156:159], v[92:95], a[188:191]\n\t"
    "s_waitcnt vmcnt(21) lgkmcnt(0)\n\t"
    "v_mfma_f32_16x16x32_bf16 a[0:3], v[96:99], v[0:3], a[0:3]\n\t"
    "ds_read_b64_tr_b16 v[128:129], v238 offset:8192\n\t"
    "s_add_u32 m0, s31, 57344\n\t"
    "v_mfma_f32_16x16x32_bf16 a[32:35], v[96:99], v[4:7], a[32:35]\n\t"
    "v_mfma_f32_16x16x32_bf16 a[64:67], v[96:99], v[8:11], a[64:67]\n\t"
    "ds_read_b64_tr_b16 v[130:131], v238 offset:12288\n\t"
    "v_mfma_f32_16x16x32_bf16 a[96:99], v[96:99], v[12:15], a[96:99]\n\t"
    "buffer_load_dwordx4 v232, s[12:15], s28 offen offset:0 lds\n\t"
    "v_mfma_f32_16x16x32_bf16 a[128:131], v[96:99], v[16:19], a[128:131]\n\t"
    "ds_read_b64_tr_b16 v[132:133], v239 offset:8192\n\t"
    "v_mfma_f32_16x16x32_bf16 a[160:163], v[96:99], v[20:23], a[160:163]\n\t"
    "v_mfma_f32_16x16x32_bf16 a[4:7], v[100:103], v[0:3], a[4:7]\n\t"
    "ds_read_b64_tr_b16 v[134:135], v239 offset:12288\n\t"
    "buffer_load_dwordx4 v233, s[12:15], s28 offen offset:1024 lds\n\t"
    "v_mfma_f32_16x16x32_bf16 a[36:39], v[100:103], v[4:7], a[36:39]\n\t"
    "v_mfma_f32_16x16x32_bf16 a[68:71], v[100:103], v[8:11], a[68:71]\n\t"
    "v_mfma_f32_16x16x32_bf16 a[100:103], v[100:103], v[12:15], a[100:103]\n\t"
    "ds_read_b64_tr_b16 v[136:137], v238 offset:8448\n\t"
    "buffer_load_dwordx4 v234, s[12:15], s28 offen offset:2048 lds\n\t"
    "v_mfma_f32_16x16x32_bf16 a[132:135], v[100:103], v[16:19], a[132:135]\n\t"
    "v_mfma_f32_16x16x32_bf16 a[164:167], v[100:103], v[20:23], a[164:167]\n\t"
    "ds_read_b64_tr_b16 v[138:139], v238 offset:12544\n\t"
    "v_mfma_f32_16x16x32_bf16 a[8:11], v[104:107], v[0:3], a[8:11]\n\t"
    "buffer_load_dwordx4 v235, s[12:15], s28 offen offset:3072 lds\n\t"
    "v_mfma_f32_16x16x32_bf16 a[40:43], v[104:107], v[4:7], a[40:43]\n\t"
    "ds_read_b64_tr_b16 v[140:141], v239 offset:8448\n\t"
    "v_mfma_f32_16x16x32_bf16 a[72:75], v[104:107], v[8:11], a[72:75]\n\t"
    "v_mfma_f32_16x16x32_bf16 a[104:107], v[104:107], v[12:15], a[104:107]\n\t"
    "v_mfma_f32_16x16x32_bf16 a[136:139], v[104:107], v[16:19], a[136:139]\n\t"
    "ds_read_b64_tr_b16 v[142:143], v239 offset:12544\n\t"
    "s_cmp_eq_u32 s94, s29\n\t"
    "s_addc_u32 s94, s94, 0\n\t"
    "v_mfma_f32_16x16x32_bf16 a[168:171], v[104:107], v[20:23], a[168:171]\n\t"
    "v_mfma_f32_16x16x32_bf16 a[12:15], v[108:111], v[0:3], a[12:15]\n\t"
    "ds_read_b64_tr_b16 v[144:145], v238 offset:8704\n\t"
    "v_mfma_f32_16x16x32_bf16 a[44:47], v[108:111], v[4:7], a[44:47]\n\t"
    "s_mul_i32 s28, s94, 524288\n\t"
    "v_mfma_f32_16x16x32_bf16 a[76:79], v[108:111], v[8:11], a[76:79]\n\t"
    "ds_read_b64_tr_b16 v[146:147], v238 offset:12800\n\t"
    "v_mfma_f32_16x16x32_bf16 a[108:111], v[108:111], v[12:15], a[108:111]\n\t"
    "v_mfma_f32_16x16x32_bf16 a[140:143], v[108:111], v[16:19], a[140:143]\n\t"
    "s_add_u32 s94, s94, 1\n\t"
    "v_mfma_f32_16x16x32_bf16 a[172:175], v[108:111], v[20:23], a[172:175]\n\t"
    "ds_read_b64_tr_b16 v[148:149], v239 offset:8704\n\t"
    "v_mfma_f32_16x16x32_bf16 a[16:19], v[112:115], v[0:3], a[16:19]\n\t"
    "v_mfma_f32_16x16x32_bf16 a[48:51], v[112:115], v[4:7], a[48:51]\n\t"
    "ds_read_b64_tr_b16 v[150:151], v239 offset:12800\n\t"
    "s_add_u32 s30, s30, 6144\n\t"
    "v_mfma_f32_16x16x32_bf16 a[80:83], v[112:115], v[8:11], a[80:83]\n\t"
    "v_mfma_f32_16x16x32_bf16 a[112:115], v[112:115], v[12:15], a[112:115]\n\t"
    "ds_read_b64_tr_b16 v[152:153], v238 offset:8960\n\t"
    "v_mfma_f32_16x16x32_bf16 a[144:147], v[112:115], v[16:19], a[144:147]\n\t"
    "buffer_load_dwordx4 v[72:75], v236, s[16:19], s30 offen offset:0 sc1\n\t"
    "v_mfma_f32_16x16x32_bf16 a[176:179], v[112:115], v[20:23], a[176:179]\n\t"
    "v_mfma_f32_16x16x32_bf16 a[20:23], v[116:119], v[0:3], a[20:23]\n\t"
    "ds_read_b64_tr_b16 v[154:155], v238 offset:13056\n\t"
    "v_mfma_f32_16x16x32_bf16 a[52:55], v[116:119], v[4:7], a[52:55]\n\t"
    "v_mfma_f32_16x16x32_bf16 a[84:87], v[116:119], v[8:11], a[84:87]\n\t"
    "ds_read_b64_tr_b16 v[156:157], v239 offset:8960\n\t"
    "buffer_load_dwordx4 v[76:79], v236, s[16:19], s30 offen offset:1024 sc1\n\t"
    "v_mfma_f32_16x16x32_bf16 a[116:119], v[116:119], v[12:15], a[116:119]\n\t"
    "v_mfma_f32_16x16x32_bf16 a[148:151], v[116:119], v[16:19], a[148:151]\n\t"
    "ds_read_b64_tr_b16 v[158:159], v239 offset:13056\n\t"
    "v_mfma_f32_16x16x32_bf16 a[180:183], v[116:119], v[20:23], a[180:183]\n\t"
    "buffer_load_dwordx4 v[80:83], v236, s[16:19], s30 offen offset:2048 sc1\n\t"
    "v_mfma_f32_16x16x32_bf16 a[24:27], v[120:123], v[0:3], a[24:27]\n\t"
    "v_mfma_f32_16x16x32_bf16 a[56:59], v[120:123], v[4:7], a[56:59]\n\t"
    "v_mfma_f32_16x16x32_bf16 a[88:91], v[120:123], v[8:11], a[88:91]\n\t"
    "buffer_load_dwordx4 v[84:87], v237, s[16:19], s30 offen offset:0 sc1\n\t"
    "v_mfma_f32_16x16x32_bf16 a[120:123], v[120:123], v[12:15], a[120:123]\n\t"
    "v_mfma_f32_16x16x32_bf16 a[152:155], v[120:123], v[16:19], a[152:155]\n\t"
    "v_mfma_f32_16x16x32_bf16 a[184:187], v[120:123], v[20:23], a[184:187]\n\t"
    "buffer_load_dwordx4 v[88:91], v237, s[16:19], s30 offen offset:1024 sc1\n\t"
    "v_mfma_f32_16x16x32_bf16 a[28:31], v[124:127], v[0:3], a[28:31]\n\t"
    "v_mfma_f32_16x16x32_bf16 a[60:63], v[124:127], v[4:7], a[60:63]\n\t"
    "v_mfma_f32_16x16x32_bf16 a[92:95], v[124:127], v[8:11], a[92:95]\n\t"
    "buffer_load_dwordx4 v[92:95], v237, s[16:19], s30 offen offset:2048 sc1\n\t"
    "v_mfma_f32_16x16x32_bf16 a[124:127], v[124:127], v[12:15], a[124:127]\n\t"
    "v_mfma_f32_16x16x32_bf16 a[156:159], v[124:127], v[16:19], a[156:159]\n\t"
    "v_mfma_f32_16x16x32_bf16 a[188:191], v[124:127], v[20:23], a[188:191]\n\t"
    "s_waitcnt vmcnt(10)\n\t"
    "v_cmp_ne_u32 vcc, v221, v209\n\t"
    "s_nop 1\n\t"
    "s_and_b32 s73, vcc_lo, 0xff\n\t"
    "s_cbranch_scc0 .LFLHOK_%=\n\t"
    "s_mov_b32 s74, 65536\n\t"
    ".LFLH_%=:\n\t"
    "buffer_load_dword v221, v218, s[24:27], 0 offen offset:0 sc1\n\t"
    "s_waitcnt vmcnt(0)\n\t"
    "v_cmp_ne_u32 vcc, v221, v209\n\t"
    "s_nop 1\n\t"
    "s_and_b32 s73, vcc_lo, 0xff\n\t"
    "s_cbranch_scc0 .LFLHOK_%=\n\t"
    "s_sleep 1\n\t"
    "s_sub_u32 s74, s74, 1\n\t"
    "s_cmp_lg_u32 s74, 0\n\t"
    "s_cbranch_scc1 .LFLH_%=\n\t"
    ".LFLHOK_%=:\n\t"
    "s_waitcnt vmcnt(21) lgkmcnt(0)\n\t"
    "s_barrier\n\t"
    "v_mfma_f32_16x16x32_bf16 a[0:3], v[128:131], v[24:27], a[0:3]\n\t"
    "ds_read_b64_tr_b16 v[96:97], v238 offset:16384\n\t"
    "s_add_u32 m0, s31, 0\n\t"
    "v_mfma_f32_16x16x32_bf16 a[32:35], v[128:131], v[28:31], a[32:35]\n\t"
    "v_mfma_f32_16x16x32_bf16 a[64:67], v[128:131], v[32:35], a[64:67]\n\t"
    "ds_read_b64_tr_b16 v[98:99], v238 offset:20480\n\t"
    "v_mfma_f32_16x16x32_bf16 a[96:99], v[128:131], v[36:39], a[96:99]\n\t"
    "buffer_load_dwordx4 v232, s[12:15], s28 offen offset:0 lds\n\t"
    "v_mfma_f32_16x16x32_bf16 a[128:131], v[128:131], v[40:43], a[128:131]\n\t"
    "ds_read_b64_tr_b16 v[100:101], v239 offset:16384\n\t"
    "v_mfma_f32_16x16x32_bf16 a[160:163], v[128:131], v[44:47], a[160:163]\n\t"
    "v_mfma_f32_16x16x32_bf16 a[4:7], v[132:135], v[24:27], a[4:7]\n\t"
    "ds_read_b64_tr_b16 v[102:103], v239 offset:20480\n\t"
    "buffer_load_dwordx4 v233, s[12:15], s28 offen offset:1024 lds\n\t"
    "v_mfma_f32_16x16x32_bf16 a[36:39], v[132:135], v[28:31], a[36:39]\n\t"
    "v_mfma_f32_16x16x32_bf16 a[68:71], v[132:135], v[32:35], a[68:71]\n\t"
    "v_mfma_f32_16x16x32_bf16 a[100:103], v[132:135], v[36:39], a[100:103]\n\t"
    "ds_read_b64_tr_b16 v[104:105], v238 offset:16640\n\t"
    "buffer_load_dwordx4 v234, s[12:15], s28 offen offset:2048 lds\n\t"
    "v_mfma_f32_16x16x32_bf16 a[132:135], v[132:135], v[40:43], a[132:135]\n\t"
    "v_mfma_f32_16x16x32_bf16 a[164:167], v[132:135], v[44:47], a[164:167]\n\t"
    "ds_read_b64_tr_b16 v[106:107], v238 offset:20736\n\t"
    "v_mfma_f32_16x16x32_bf16 a[8:11], v[136:139], v[24:27], a[8:11]\n\t"
    "buffer_load_dwordx4 v235, s[12:15], s28 offen offset:3072 lds\n\t"
    "v_mfma_f32_16x16x32_bf16 a[40:43], v[136:139], v[28:31], a[40:43]\n\t"
    "ds_read_b64_tr_b16 v[108:109], v239 offset:16640\n\t"
    "v_mfma_f32_16x16x32_bf16 a[72:75], v[136:139], v[32:35], a[72:75]\n\t"
    "v_mfma_f32_16x16x32_bf16 a[104:107], v[136:139], v[36:39], a[104:107]\n\t"
    "v_mfma_f32_16x16x32_bf16 a[136:139], v[136:139], v[40:43], a[136:139]\n\t"
    "ds_read_b64_tr_b16 v[110:111], v239 offset:20736\n\t"
    "s_add_u32 s28, s28, 65536\n\t"
    "v_mfma_f32_16x16x32_bf16 a[168:171], v[136:139], v[44:47], a[168:171]\n\t"
    "v_mfma_f32_16x16x32_bf16 a[12:15], v[140:143], v[24:27], a[12:15]\n\t"
    "ds_read_b64_tr_b16 v[112:113], v238 offset:16896\n\t"
    "v_mfma_f32_16x16x32_bf16 a[44:47], v[140:143], v[28:31], a[44:47]\n\t"
    "s_cmp_eq_u32 s95, s29\n\t"
    "s_addc_u32 s95, s95, 0\n\t"
    "v_mfma_f32_16x16x32_bf16 a[76:79], v[140:143], v[32:35], a[76:79]\n\t"
    "ds_read_b64_tr_b16 v[114:115], v238 offset:20992\n\t"
    "v_mfma_f32_16x16x32_bf16 a[108:111], v[140:143], v[36:39], a[108:111]\n\t"
    "v_mfma_f32_16x16x32_bf16 a[140:143], v[140:143], v[40:43], a[140:143]\n\t"
    "s_mul_i32 s30, s95, 49152\n\t"
    "v_mfma_f32_16x16x32_bf16 a[172:175], v[140:143], v[44:47], a[172:175]\n\t"
    "ds_read_b64_tr_b16 v[116:117], v239 offset:16896\n\t"
    "v_mfma_f32_16x16x32_bf16 a[16:19], v[144:147], v[24:27], a[16:19]\n\t"
    "v_mfma_f32_16x16x32_bf16 a[48:51], v[144:147], v[28:31], a[48:51]\n\t"
    "ds_read_b64_tr_b16 v[118:119], v239 offset:20992\n\t"
    "s_add_u32 s95, s95, 1\n\t"
    "v_mfma_f32_16x16x32_bf16 a[80:83], v[144:147], v[32:35], a[80:83]\n\t"
    "v_mfma_f32_16x16x32_bf16 a[112:115], v[144:147], v[36:39], a[112:115]\n\t"
    "ds_read_b64_tr_b16 v[120:121], v238 offset:17152\n\t"
    "v_mfma_f32_16x16x32_bf16 a[144:147], v[144:147], v[40:43], a[144:147]\n\t"
    "buffer_load_dwordx4 v[0:3], v236, s[16:19], s30 offen offset:0 sc1\n\t"
    "v_mfma_f32_16x16x32_bf16 a[176:179], v[144:147], v[44:47], a[176:179]\n\t"
    "v_mfma_f32_16x16x32_bf16 a[20:23], v[148:151], v[24:27], a[20:23]\n\t"
    "ds_read_b64_tr_b16 v[122:123], v238 offset:21248\n\t"
    "v_mfma_f32_16x16x32_bf16 a[52:55], v[148:151], v[28:31], a[52:55]\n\t"
    "v_mfma_f32_16x16x32_bf16 a[84:87], v[148:151], v[32:35], a[84:87]\n\t"
    "ds_read_b64_tr_b16 v[124:125], v239 offset:17152\n\t"
    "buffer_load_dwordx4 v[4:7], v236, s[16:19], s30 offen offset:1024 sc1\n\t"
    "v_mfma_f32_16x16x32_bf16 a[116:119], v[148:151], v[36:39], a[116:119]\n\t"
    "v_mfma_f32_16x16x32_bf16 a[148:151], v[148:151], v[40:43], a[148:151]\n\t"
    "ds_read_b64_tr_b16 v[126:127], v239 offset:21248\n\t"
    "v_mfma_f32_16x16x32_bf16 a[180:183], v[148:151], v[44:47], a[180:183]\n\t"
    "buffer_load_dwordx4 v[8:11], v236, s[16:19], s30 offen offset:2048 sc1\n\t"
    "v_mfma_f32_16x16x32_bf16 a[24:27], v[152:155], v[24:27], a[24:27]\n\t"
    "v_mfma_f32_16x16x32_bf16 a[56:59], v[152:155], v[28:31], a[56:59]\n\t"
    "v_mfma_f32_16x16x32_bf16 a[88:91], v[152:155], v[32:35], a[88:91]\n\t"
    "buffer_load_dwordx4 v[12:15], v237, s[16:19], s30 offen offset:0 sc1\n\t"
    "v_mfma_f32_16x16x32_bf16 a[120:123], v[152:155], v[36:39], a[120:123]\n\t"
    "v_mfma_f32_16x16x32_bf16 a[152:155], v[152:155], v[40:43], a[152:155]\n\t"
    "v_mfma_f32_16x16x32_bf16 a[184:187], v[152:155], v[44:47], a[184:187]\n\t"
    "buffer_load_dwordx4 v[16:19], v237, s[16:19], s30 offen offset:1024 sc1\n\t"
    "v_mfma_f32_16x16x32_bf16 a[28:31], v[156:159], v[24:27], a[28:31]\n\t"
    "v_mfma_f32_16x16x32_bf16 a[60:63], v[156:159], v[28:31], a[60:63]\n\t"
    "v_mfma_f32_16x16x32_bf16 a[92:95], v[156:159], v[32:35], a[92:95]\n\t"
    "buffer_load_dwordx4 v[20:23], v237, s[16:19], s30 offen offset:2048 sc1\n\t"
    "v_mfma_f32_16x16x32_bf16 a[124:127], v[156:159], v[36:39], a[124:127]\n\t"
    "v_mfma_f32_16x16x32_bf16 a[156:159], v[156:159], v[40:43], a[156:159]\n\t"
    "v_mfma_f32_16x16x32_bf16 a[188:191], v[156:159], v[44:47], a[188:191]\n\t"
    "s_waitcnt vmcnt(21) lgkmcnt(0)\n\t"
    "v_mfma_f32_16x16x32_bf16 a[0:3], v[96:99], v[48:51], a[0:3]\n\t"
    "ds_read_b64_tr_b16 v[128:129], v238 offset:24576\n\t"
    "s_add_u32 m0, s31, 8192\n\t"
    "v_mfma_f32_16x16x32_bf16 a[32:35], v[96:99], v[52:55], a[32:35]\n\t"
    "v_mfma_f32_16x16x32_bf16 a[64:67], v[96:99], v[56:59], a[64:67]\n\t"
    "ds_read_b64_tr_b16 v[130:131], v238 offset:28672\n\t"
    "v_mfma_f32_16x16x32_bf16 a[96:99], v[96:99], v[60:63], a[96:99]\n\t"
    "buffer_load_dwordx4 v232, s[12:15], s28 offen offset:0 lds\n\t"
    "v_mfma_f32_16x16x32_bf16 a[128:131], v[96:99], v[64:67], a[128:131]\n\t"
    "ds_read_b64_tr_b16 v[132:133], v239 offset:24576\n\t"
    "v_mfma_f32_16x16x32_bf16 a[160:163], v[96:99], v[68:71], a[160:163]\n\t"
    "v_mfma_f32_16x16x32_bf16 a[4:7], v[100:103], v[48:51], a[4:7]\n\t"
    "ds_read_b64_tr_b16 v[134:135], v239 offset:28672\n\t"
    "v_mfma_f32_16x16x32_bf16 a[36:39], v[100:103], v[52:55], a[36:39]\n\t"
    "buffer_load_dwordx4 v233, s[12:15], s28 offen offset:1024 lds\n\t"
    "v_mfma_f32_16x16x32_bf16 a[68:71], v[100:103], v[56:59], a[68:71]\n\t"
    "v_mfma_f32_16x16x32_bf16 a[100:103], v[100:103], v[60:63], a[100:103]\n\t"
    "ds_read_b64_tr_b16 v[136:137], v238 offset:24832\n\t"
    "v_mfma_f32_16x16x32_bf16 a[132:135], v[100:103], v[64:67], a[132:135]\n\t"
    "v_mfma_f32_16x16x32_bf16 a[164:167], v[100:103], v[68:71], a[164:167]\n\t"
    "ds_read_b64_tr_b16 v[138:139], v238 offset:28928\n\t"
    "buffer_load_dwordx4 v234, s[12:15], s28 offen offset:2048 lds\n\t"
    "v_mfma_f32_16x16x32_bf16 a[8:11], v[104:107], v[48:51], a[8:11]\n\t"
    "v_mfma_f32_16x16x32_bf16 a[40:43], v[104:107], v[52:55], a[40:43]\n\t"
    "ds_read_b64_tr_b16 v[140:141], v239 offset:24832\n\t"
    "v_mfma_f32_16x16x32_bf16 a[72:75], v[104:107], v[56:59], a[72:75]\n\t"
    "buffer_load_dwordx4 v235, s[12:15], s28 offen offset:3072 lds\n\t"
    "v_mfma_f32_16x16x32_bf16 a[104:107], v[104:107], v[60:63], a[104:107]\n\t"
    "v_mfma_f32_16x16x32_bf16 a[136:139], v[104:107], v[64:67], a[136:139]\n\t"
    "ds_read_b64_tr_b16 v[142:143], v239 offset:28928\n\t"
    "v_mfma_f32_16x16x32_bf16 a[168:171], v[104:107], v[68:71], a[168:171]\n\t"
    "v_mfma_f32_16x16x32_bf16 a[12:15], v[108:111], v[48:51], a[12:15]\n\t"
    "ds_read_b64_tr_b16 v[144:145], v238 offset:25088\n\t"
    "s_add_u32 s28, s28, 65536\n\t"
    "v_mfma_f32_16x16x32_bf16 a[44:47], v[108:111], v[52:55], a[44:47]\n\t"
    "v_mfma_f32_16x16x32_bf16 a[76:79], v[108:111], v[56:59], a[76:79]\n\t"
    "ds_read_b64_tr_b16 v[146:147], v238 offset:29184\n\t"
    "v_mfma_f32_16x16x32_bf16 a[108:111], v[108:111], v[60:63], a[108:111]\n\t"
    "v_mfma_f32_16x16x32_bf16 a[140:143], v[108:111], v[64:67], a[140:143]\n\t"
    "s_add_u32 s30, s30, 6144\n\t"
    "v_mfma_f32_16x16x32_bf16 a[172:175], v[108:111], v[68:71], a[172:175]\n\t"
    "ds_read_b64_tr_b16 v[148:149], v239 offset:25088\n\t"
    "v_mfma_f32_16x16x32_bf16 a[16:19], v[112:115], v[48:51], a[16:19]\n\t"
    "v_mfma_f32_16x16x32_bf16 a[48:51], v[112:115], v[52:55], a[48:51]\n\t"
    "ds_read_b64_tr_b16 v[150:151], v239 offset:29184\n\t"
    "buffer_load_dwordx4 v[24:27], v236, s[16:19], s30 offen offset:0 sc1\n\t"
    "v_mfma_f32_16x16x32_bf16 a[80:83], v[112:115], v[56:59], a[80:83]\n\t"
    "v_mfma_f32_16x16x32_bf16 a[112:115], v[112:115], v[60:63], a[112:115]\n\t"
    "ds_read_b64_tr_b16 v[152:153], v238 offset:25344\n\t"
    "v_mfma_f32_16x16x32_bf16 a[144:147], v[112:115], v[64:67], a[144:147]\n\t"
    "v_mfma_f32_16x16x32_bf16 a[176:179], v[112:115], v[68:71], a[176:179]\n\t"
    "buffer_load_dwordx4 v[28:31], v236, s[16:19], s30 offen offset:1024 sc1\n\t"
    "v_mfma_f32_16x16x32_bf16 a[20:23], v[116:119], v[48:51], a[20:23]\n\t"
    "ds_read_b64_tr_b16 v[154:155], v238 offset:29440\n\t"
    "v_mfma_f32_16x16x32_bf16 a[52:55], v[116:119], v[52:55], a[52:55]\n\t"
    "v_mfma_f32_16x16x32_bf16 a[84:87], v[116:119], v[56:59], a[84:87]\n\t"
    "ds_read_b64_tr_b16 v[156:157], v239 offset:25344\n\t"
    "v_mfma_f32_16x16x32_bf16 a[116:119], v[116:119], v[60:63], a[116:119]\n\t"
    "buffer_load_dwordx4 v[32:35], v236, s[16:19], s30 offen offset:2048 sc1\n\t"
    "v_mfma_f32_16x16x32_bf16 a[148:151], v[116:119], v[64:67], a[148:151]\n\t"
    "ds_read_b64_tr_b16 v[158:159], v239 offset:29440\n\t"
    "v_mfma_f32_16x16x32_bf16 a[180:183], v[116:119], v[68:71], a[180:183]\n\t"
    "v_mfma_f32_16x16x32_bf16 a[24:27], v[120:123], v[48:51], a[24:27]\n\t"
    "buffer_load_dwordx4 v[36:39], v237, s[16:19], s30 offen offset:0 sc1\n\t"
    "v_mfma_f32_16x16x32_bf16 a[56:59], v[120:123], v[52:55], a[56:59]\n\t"
    "v_mfma_f32_16x16x32_bf16 a[88:91], v[120:123], v[56:59], a[88:91]\n\t"
    "v_mfma_f32_16x16x32_bf16 a[120:123], v[120:123], v[60:63], a[120:123]\n\t"
    "v_mfma_f32_16x16x32_bf16 a[152:155], v[120:123], v[64:67], a[152:155]\n\t"
    "buffer_load_dwordx4 v[40:43], v237, s[16:19], s30 offen offset:1024 sc1\n\t"
    "v_mfma_f32_16x16x32_bf16 a[184:187], v[120:123], v[68:71], a[184:187]\n\t"
    "v_mfma_f32_16x16x32_bf16 a[28:31], v[124:127], v[48:51], a[28:31]\n\t"
    "v_mfma_f32_16x16x32_bf16 a[60:63], v[124:127], v[52:55], a[60:63]\n\t"
    "v_mfma_f32_16x16x32_bf16 a[92:95], v[124:127], v[56:59], a[92:95]\n\t"
    "buffer_load_dwordx4 v[44:47], v237, s[16:19], s30 offen offset:2048 sc1\n\t"
    "v_mfma_f32_16x16x32_bf16 a[124:127], v[124:127], v[60:63], a[124:127]\n\t"
    "v_mfma_f32_16x16x32_bf16 a[156:159], v[124:127], v[64:67], a[156:159]\n\t"
    "v_mfma_f32_16x16x32_bf16 a[188:191], v[124:127], v[68:71], a[188:191]\n\t"
    "s_waitcnt vmcnt(20) lgkmcnt(0)\n\t"
    "s_barrier\n\t"
    "v_mfma_f32_16x16x32_bf16 a[0:3], v[128:131], v[72:75], a[0:3]\n\t"
    "ds_read_b64_tr_b16 v[96:97], v238 offset:32768\n\t"
    "s_add_u32 m0, s31, 16384\n\t"
    "v_mfma_f32_16x16x32_bf16 a[32:35], v[128:131], v[76:79], a[32:35]\n\t"
    "v_mfma_f32_16x16x32_bf16 a[64:67], v[128:131], v[80:83], a[64:67]\n\t"
    "ds_read_b64_tr_b16 v[98:99], v238 offset:36864\n\t"
    "v_mfma_f32_16x16x32_bf16 a[96:99], v[128:131], v[84:87], a[96:99]\n\t"
    "buffer_load_dwordx4 v232, s[12:15], s28 offen offset:0 lds\n\t"
    "v_mfma_f32_16x16x32_bf16 a[128:131], v[128:131], v[88:91], a[128:131]\n\t"
    "ds_read_b64_tr_b16 v[100:101], v239 offset:32768\n\t"
    "v_mfma_f32_16x16x32_bf16 a[160:163], v[128:131], v[92:95], a[160:163]\n\t"
    "v_mfma_f32_16x16x32_bf16 a[4:7], v[132:135], v[72:75], a[4:7]\n\t"
    "ds_read_b64_tr_b16 v[102:103], v239 offset:36864\n\t"
    "v_mfma_f32_16x16x32_bf16 a[36:39], v[132:135], v[76:79], a[36:39]\n\t"
    "buffer_load_dwordx4 v233, s[12:15], s28 offen offset:1024 lds\n\t"
    "v_mfma_f32_16x16x32_bf16 a[68:71], v[132:135], v[80:83], a[68:71]\n\t"
    "v_mfma_f32_16x16x32_bf16 a[100:103], v[132:135], v[84:87], a[100:103]\n\t"
    "ds_read_b64_tr_b16 v[104:105], v238 offset:33024\n\t"
    "v_mfma_f32_16x16x32_bf16 a[132:135], v[132:135], v[88:91], a[132:135]\n\t"
    "v_mfma_f32_16x16x32_bf16 a[164:167], v[132:135], v[92:95], a[164:167]\n\t"
    "ds_read_b64_tr_b16 v[106:107], v238 offset:37120\n\t"
    "buffer_load_dwordx4 v234, s[12:15], s28 offen offset:2048 lds\n\t"
    "v_mfma_f32_16x16x32_bf16 a[8:11], v[136:139], v[72:75], a[8:11]\n\t"
    "v_mfma_f32_16x16x32_bf16 a[40:43], v[136:139], v[76:79], a[40:43]\n\t"
    "ds_read_b64_tr_b16 v[108:109], v239 offset:33024\n\t"
    "v_mfma_f32_16x16x32_bf16 a[72:75], v[136:139], v[80:83], a[72:75]\n\t"
    "buffer_load_dwordx4 v235, s[12:15], s28 offen offset:3072 lds\n\t"
    "v_mfma_f32_16x16x32_bf16 a[104:107], v[136:139], v[84:87], a[104:107]\n\t"
    "v_mfma_f32_16x16x32_bf16 a[136:139], v[136:139], v[88:91], a[136:139]\n\t"
    "ds_read_b64_tr_b16 v[110:111], v239 offset:37120\n\t"
    "v_mfma_f32_16x16x32_bf16 a[168:171], v[136:139], v[92:95], a[168:171]\n\t"
    "v_mfma_f32_16x16x32_bf16 a[12:15], v[140:143], v[72:75], a[12:15]\n\t"
    "ds_read_b64_tr_b16 v[112:113], v238 offset:33280\n\t"
    "s_add_u32 s28, s28, 65536\n\t"
    "v_mfma_f32_16x16x32_bf16 a[44:47], v[140:143], v[76:79], a[44:47]\n\t"
    "v_mfma_f32_16x16x32_bf16 a[76:79], v[140:143], v[80:83], a[76:79]\n\t"
    "ds_read_b64_tr_b16 v[114:115], v238 offset:37376\n\t"
    "v_mfma_f32_16x16x32_bf16 a[108:111], v[140:143], v[84:87], a[108:111]\n\t"
    "v_mfma_f32_16x16x32_bf16 a[140:143], v[140:143], v[88:91], a[140:143]\n\t"
    "s_add_u32 s30, s30, 6144\n\t"
    "v_mfma_f32_16x16x32_bf16 a[172:175], v[140:143], v[92:95], a[172:175]\n\t"
    "ds_read_b64_tr_b16 v[116:117], v239 offset:33280\n\t"
    "v_mfma_f32_16x16x32_bf16 a[16:19], v[144:147], v[72:75], a[16:19]\n\t"
    "v_mfma_f32_16x16x32_bf16 a[48:51], v[144:147], v[76:79], a[48:51]\n\t"
    "ds_read_b64_tr_b16 v[118:119], v239 offset:37376\n\t"
    "buffer_load_dwordx4 v[48:51], v236, s[16:19], s30 offen offset:0 sc1\n\t"
    "v_mfma_f32_16x16x32_bf16 a[80:83], v[144:147], v[80:83], a[80:83]\n\t"
    "v_mfma_f32_16x16x32_bf16 a[112:115], v[144:147], v[84:87], a[112:115]\n\t"
    "ds_read_b64_tr_b16 v[120:121], v238 offset:33536\n\t"
    "v_mfma_f32_16x16x32_bf16 a[144:147], v[144:147], v[88:91], a[144:147]\n\t"
    "v_mfma_f32_16x16x32_bf16 a[176:179], v[144:147], v[92:95], a[176:179]\n\t"
    "buffer_load_dwordx4 v[52:55], v236, s[16:19], s30 offen offset:1024 sc1\n\t"
    "v_mfma_f32_16x16x32_bf16 a[20:23], v[148:151], v[72:75], a[20:23]\n\t"
    "ds_read_b64_tr_b16 v[122:123], v238 offset:37632\n\t"
    "v_mfma_f32_16x16x32_bf16 a[52:55], v[148:151], v[76:79], a[52:55]\n\t"
    "v_mfma_f32_16x16x32_bf16 a[84:87], v[148:151], v[80:83], a[84:87]\n\t"
    "ds_read_b64_tr_b16 v[124:125], v239 offset:33536\n\t"
    "v_mfma_f32_16x16x32_bf16 a[116:119], v[148:151], v[84:87], a[116:119]\n\t"
    "buffer_load_dwordx4 v[56:59], v236, s[16:19], s30 offen offset:2048 sc1\n\t"
    "v_mfma_f32_16x16x32_bf16 a[148:151], v[148:151], v[88:91], a[148:151]\n\t"
    "ds_read_b64_tr_b16 v[126:127], v239 offset:37632\n\t"
    "v_mfma_f32_16x16x32_bf16 a[180:183], v[148:151], v[92:95], a[180:183]\n\t"
    "v_mfma_f32_16x16x32_bf16 a[24:27], v[152:155], v[72:75], a[24:27]\n\t"
    "buffer_load_dwordx4 v[60:63], v237, s[16:19], s30 offen offset:0 sc1\n\t"
    "v_mfma_f32_16x16x32_bf16 a[56:59], v[152:155], v[76:79], a[56:59]\n\t"
    "v_mfma_f32_16x16x32_bf16 a[88:91], v[152:155], v[80:83], a[88:91]\n\t"
    "v_mfma_f32_16x16x32_bf16 a[120:123], v[152:155], v[84:87], a[120:123]\n\t"
    "v_mfma_f32_16x16x32_bf16 a[152:155], v[152:155], v[88:91], a[152:155]\n\t"
    "buffer_load_dwordx4 v[64:67], v237, s[16:19], s30 offen offset:1024 sc1\n\t"
    "v_mfma_f32_16x16x32_bf16 a[184:187], v[152:155], v[92:95], a[184:187]\n\t"
    "v_mfma_f32_16x16x32_bf16 a[28:31], v[156:159], v[72:75], a[28:31]\n\t"
    "v_mfma_f32_16x16x32_bf16 a[60:63], v[156:159], v[76:79], a[60:63]\n\t"
    "v_mfma_f32_16x16x32_bf16 a[92:95], v[156:159], v[80:83], a[92:95]\n\t"
    "buffer_load_dwordx4 v[68:71], v237, s[16:19], s30 offen offset:2048 sc1\n\t"
    "v_mfma_f32_16x16x32_bf16 a[124:127], v[156:159], v[84:87], a[124:127]\n\t"
    "v_mfma_f32_16x16x32_bf16 a[156:159], v[156:159], v[88:91], a[156:159]\n\t"
    "v_mfma_f32_16x16x32_bf16 a[188:191], v[156:159], v[92:95], a[188:191]\n\t"
    "s_waitcnt vmcnt(20) lgkmcnt(0)\n\t"
    "v_mfma_f32_16x16x32_bf16 a[0:3], v[96:99], v[0:3], a[0:3]\n\t"
    "ds_read_b64_tr_b16 v[128:129], v238 offset:40960\n\t"
    "s_add_u32 m0, s31, 24576\n\t"
    "v_mfma_f32_16x16x32_bf16 a[32:35], v[96:99], v[4:7], a[32:35]\n\t"
    "v_mfma_f32_16x16x32_bf16 a[64:67], v[96:99], v[8:11], a[64:67]\n\t"
    "ds_read_b64_tr_b16 v[130:131], v238 offset:45056\n\t"
    "v_mfma_f32_16x16x32_bf16 a[96:99], v[96:99], v[12:15], a[96:99]\n\t"
    "buffer_load_dwordx4 v232, s[12:15], s28 offen offset:0 lds\n\t"
    "v_mfma_f32_16x16x32_bf16 a[128:131], v[96:99], v[16:19], a[128:131]\n\t"
    "ds_read_b64_tr_b16 v[132:133], v239 offset:40960\n\t"
    "v_mfma_f32_16x16x32_bf16 a[160:163], v[96:99], v[20:23], a[160:163]\n\t"
    "v_mfma_f32_16x16x32_bf16 a[4:7], v[100:103], v[0:3], a[4:7]\n\t"
    "ds_read_b64_tr_b16 v[134:135], v239 offset:45056\n\t"
    "buffer_load_dwordx4 v233, s[12:15], s28 offen offset:1024 lds\n\t"
    "v_mfma_f32_16x16x32_bf16 a[36:39], v[100:103], v[4:7], a[36:39]\n\t"
    "v_mfma_f32_16x16x32_bf16 a[68:71], v[100:103], v[8:11], a[68:71]\n\t"
    "v_mfma_f32_16x16x32_bf16 a[100:103], v[100:103], v[12:15], a[100:103]\n\t"
    "ds_read_b64_tr_b16 v[136:137], v238 offset:41216\n\t"
    "buffer_load_dwordx4 v234, s[12:15], s28 offen offset:2048 lds\n\t"
    "v_mfma_f32_16x16x32_bf16 a[132:135], v[100:103], v[16:19], a[132:135]\n\t"
    "v_mfma_f32_16x16x32_bf16 a[164:167], v[100:103], v[20:23], a[164:167]\n\t"
    "ds_read_b64_tr_b16 v[138:139], v238 offset:45312\n\t"
    "v_mfma_f32_16x16x32_bf16 a[8:11], v[104:107], v[0:3], a[8:11]\n\t"
    "buffer_load_dwordx4 v235, s[12:15], s28 offen offset:3072 lds\n\t"
    "v_mfma_f32_16x16x32_bf16 a[40:43], v[104:107], v[4:7], a[40:43]\n\t"
    "ds_read_b64_tr_b16 v[140:141], v239 offset:41216\n\t"
    "v_mfma_f32_16x16x32_bf16 a[72:75], v[104:107], v[8:11], a[72:75]\n\t"
    "v_mfma_f32_16x16x32_bf16 a[104:107], v[104:107], v[12:15], a[104:107]\n\t"
    "v_mfma_f32_16x16x32_bf16 a[136:139], v[104:107], v[16:19], a[136:139]\n\t"
    "ds_read_b64_tr_b16 v[142:143], v239 offset:45312\n\t"
    "s_cmp_eq_u32 s94, s29\n\t"
    "s_addc_u32 s94, s94, 0\n\t"
    "v_mfma_f32_16x16x32_bf16 a[168:171], v[104:107], v[20:23], a[168:171]\n\t"
    "v_mfma_f32_16x16x32_bf16 a[12:15], v[108:111], v[0:3], a[12:15]\n\t"
    "ds_read_b64_tr_b16 v[144:145], v238 offset:41472\n\t"
    "v_mfma_f32_16x16x32_bf16 a[44:47], v[108:111], v[4:7], a[44:47]\n\t"
    "s_mul_i32 s28, s94, 524288\n\t"
    "v_mfma_f32_16x16x32_bf16 a[76:79], v[108:111], v[8:11], a[76:79]\n\t"
    "ds_read_b64_tr_b16 v[146:147], v238 offset:45568\n\t"
    "v_mfma_f32_16x16x32_bf16 a[108:111], v[108:111], v[12:15], a[108:111]\n\t"
    "v_mfma_f32_16x16x32_bf16 a[140:143], v[108:111], v[16:19], a[140:143]\n\t"
    "s_add_u32 s94, s94, 1\n\t"
    "v_mfma_f32_16x16x32_bf16 a[172:175], v[108:111], v[20:23], a[172:175]\n\t"
    "ds_read_b64_tr_b16 v[148:149], v239 offset:41472\n\t"
    "v_mfma_f32_16x16x32_bf16 a[16:19], v[112:115], v[0:3], a[16:19]\n\t"
    "v_mfma_f32_16x16x32_bf16 a[48:51], v[112:115], v[4:7], a[48:51]\n\t"
    "ds_read_b64_tr_b16 v[150:151], v239 offset:45568\n\t"
    "s_add_u32 s30, s30, 6144\n\t"
    "v_mfma_f32_16x16x32_bf16 a[80:83], v[112:115], v[8:11], a[80:83]\n\t"
    "v_mfma_f32_16x16x32_bf16 a[112:115], v[112:115], v[12:15], a[112:115]\n\t"
    "ds_read_b64_tr_b16 v[152:153], v238 offset:41728\n\t"
    "v_mfma_f32_16x16x32_bf16 a[144:147], v[112:115], v[16:19], a[144:147]\n\t"
    "buffer_load_dwordx4 v[72:75], v236, s[16:19], s30 offen offset:0 sc1\n\t"
    "v_mfma_f32_16x16x32_bf16 a[176:179], v[112:115], v[20:23], a[176:179]\n\t"
    "v_mfma_f32_16x16x32_bf16 a[20:23], v[116:119], v[0:3], a[20:23]\n\t"
    "ds_read_b64_tr_b16 v[154:155], v238 offset:45824\n\t"
    "v_mfma_f32_16x16x32_bf16 a[52:55], v[116:119], v[4:7], a[52:55]\n\t"
    "v_mfma_f32_16x16x32_bf16 a[84:87], v[116:119], v[8:11], a[84:87]\n\t"
    "ds_read_b64_tr_b16 v[156:157], v239 offset:41728\n\t"
    "buffer_load_dwordx4 v[76:79], v236, s[16:19], s30 offen offset:1024 sc1\n\t"
    "v_mfma_f32_16x16x32_bf16 a[116:119], v[116:119], v[12:15], a[116:119]\n\t"
    "v_mfma_f32_16x16x32_bf16 a[148:151], v[116:119], v[16:19], a[148:151]\n\t"
    "ds_read_b64_tr_b16 v[158:159], v239 offset:45824\n\t"
    "v_mfma_f32_16x16x32_bf16 a[180:183], v[116:119], v[20:23], a[180:183]\n\t"
    "buffer_load_dwordx4 v[80:83], v236, s[16:19], s30 offen offset:2048 sc1\n\t"
    "v_mfma_f32_16x16x32_bf16 a[24:27], v[120:123], v[0:3], a[24:27]\n\t"
    "v_mfma_f32_16x16x32_bf16 a[56:59], v[120:123], v[4:7], a[56:59]\n\t"
    "v_mfma_f32_16x16x32_bf16 a[88:91], v[120:123], v[8:11], a[88:91]\n\t"
    "buffer_load_dwordx4 v[84:87], v237, s[16:19], s30 offen offset:0 sc1\n\t"
    "v_mfma_f32_16x16x32_bf16 a[120:123], v[120:123], v[12:15], a[120:123]\n\t"
    "v_mfma_f32_16x16x32_bf16 a[152:155], v[120:123], v[16:19], a[152:155]\n\t"
    "v_mfma_f32_16x16x32_bf16 a[184:187], v[120:123], v[20:23], a[184:187]\n\t"
    "buffer_load_dwordx4 v[88:91], v237, s[16:19], s30 offen offset:1024 sc1\n\t"
    "v_mfma_f32_16x16x32_bf16 a[28:31], v[124:127], v[0:3], a[28:31]\n\t"
    "v_mfma_f32_16x16x32_bf16 a[60:63], v[124:127], v[4:7], a[60:63]\n\t"
    "v_mfma_f32_16x16x32_bf16 a[92:95], v[124:127], v[8:11], a[92:95]\n\t"
    "buffer_load_dwordx4 v[92:95], v237, s[16:19], s30 offen offset:2048 sc1\n\t"
    "v_mfma_f32_16x16x32_bf16 a[124:127], v[124:127], v[12:15], a[124:127]\n\t"
    "v_mfma_f32_16x16x32_bf16 a[156:159], v[124:127], v[16:19], a[156:159]\n\t"
    "v_mfma_f32_16x16x32_bf16 a[188:191], v[124:127], v[20:23], a[188:191]\n\t"
    "s_waitcnt vmcnt(20) lgkmcnt(0)\n\t"
    "s_barrier\n\t"
    "v_mfma_f32_16x16x32_bf16 a[0:3], v[128:131], v[24:27], a[0:3]\n\t"
    "ds_read_b64_tr_b16 v[96:97], v238 offset:49152\n\t"
    "s_add_u32 m0, s31, 32768\n\t"
    "v_mfma_f32_16x16x32_bf16 a[32:35], v[128:131], v[28:31], a[32:35]\n\t"
    "v_mfma_f32_16x16x32_bf16 a[64:67], v[128:131], v[32:35], a[64:67]\n\t"
    "ds_read_b64_tr_b16 v[98:99], v238 offset:53248\n\t"
    "v_mfma_f32_16x16x32_bf16 a[96:99], v[128:131], v[36:39], a[96:99]\n\t"
    "buffer_load_dwordx4 v232, s[12:15], s28 offen offset:0 lds\n\t"
    "v_mfma_f32_16x16x32_bf16 a[128:131], v[128:131], v[40:43], a[128:131]\n\t"
    "ds_read_b64_tr_b16 v[100:101], v239 offset:49152\n\t"
    "v_mfma_f32_16x16x32_bf16 a[160:163], v[128:131], v[44:47], a[160:163]\n\t"
    "v_mfma_f32_16x16x32_bf16 a[4:7], v[132:135], v[24:27], a[4:7]\n\t"
    "ds_read_b64_tr_b16 v[102:103], v239 offset:53248\n\t"
    "buffer_load_dwordx4 v233, s[12:15], s28 offen offset:1024 lds\n\t"
    "v_mfma_f32_16x16x32_bf16 a[36:39], v[132:135], v[28:31], a[36:39]\n\t"
    "v_mfma_f32_16x16x32_bf16 a[68:71], v[132:135], v[32:35], a[68:71]\n\t"
    "v_mfma_f32_16x16x32_bf16 a[100:103], v[132:135], v[36:39], a[100:103]\n\t"
    "ds_read_b64_tr_b16 v[104:105], v238 offset:49408\n\t"
    "buffer_load_dwordx4 v234, s[12:15], s28 offen offset:2048 lds\n\t"
    "v_mfma_f32_16x16x32_bf16 a[132:135], v[132:135], v[40:43], a[132:135]\n\t"
    "v_mfma_f32_16x16x32_bf16 a[164:167], v[132:135], v[44:47], a[164:167]\n\t"
    "ds_read_b64_tr_b16 v[106:107], v238 offset:53504\n\t"
    "v_mfma_f32_16x16x32_bf16 a[8:11], v[136:139], v[24:27], a[8:11]\n\t"
    "buffer_load_dwordx4 v235, s[12:15], s28 offen offset:3072 lds\n\t"
    "v_mfma_f32_16x16x32_bf16 a[40:43], v[136:139], v[28:31], a[40:43]\n\t"
    "ds_read_b64_tr_b16 v[108:109], v239 offset:49408\n\t"
    "v_mfma_f32_16x16x32_bf16 a[72:75], v[136:139], v[32:35], a[72:75]\n\t"
    "v_mfma_f32_16x16x32_bf16 a[104:107], v[136:139], v[36:39], a[104:107]\n\t"
    "v_mfma_f32_16x16x32_bf16 a[136:139], v[136:139], v[40:43], a[136:139]\n\t"
    "ds_read_b64_tr_b16 v[110:111], v239 offset:53504\n\t"
    "s_add_u32 s28, s28, 65536\n\t"
    "v_mfma_f32_16x16x32_bf16 a[168:171], v[136:139], v[44:47], a[168:171]\n\t"
    "v_mfma_f32_16x16x32_bf16 a[12:15], v[140:143], v[24:27], a[12:15]\n\t"
    "ds_read_b64_tr_b16 v[112:113], v238 offset:49664\n\t"
    "v_mfma_f32_16x16x32_bf16 a[44:47], v[140:143], v[28:31], a[44:47]\n\t"
    "s_cmp_eq_u32 s95, s29\n\t"
    "s_addc_u32 s95, s95, 0\n\t"
    "v_mfma_f32_16x16x32_bf16 a[76:79], v[140:143], v[32:35], a[76:79]\n\t"
    "ds_read_b64_tr_b16 v[114:115], v238 offset:53760\n\t"
    "v_mfma_f32_16x16x32_bf16 a[108:111], v[140:143], v[36:39], a[108:111]\n\t"
    "v_mfma_f32_16x16x32_bf16 a[140:143], v[140:143], v[40:43], a[140:143]\n\t"
    "s_mul_i32 s30, s95, 49152\n\t"
    "v_mfma_f32_16x16x32_bf16 a[172:175], v[140:143], v[44:47], a[172:175]\n\t"
    "ds_read_b64_tr_b16 v[116:117], v239 offset:49664\n\t"
    "v_mfma_f32_16x16x32_bf16 a[16:19], v[144:147], v[24:27], a[16:19]\n\t"
    "v_mfma_f32_16x16x32_bf16 a[48:51], v[144:147], v[28:31], a[48:51]\n\t"
    "ds_read_b64_tr_b16 v[118:119], v239 offset:53760\n\t"
    "s_add_u32 s95, s95, 1\n\t"
    "v_mfma_f32_16x16x32_bf16 a[80:83], v[144:147], v[32:35], a[80:83]\n\t"
    "v_mfma_f32_16x16x32_bf16 a[112:115], v[144:147], v[36:39], a[112:115]\n\t"
    "ds_read_b64_tr_b16 v[120:121], v238 offset:49920\n\t"
    "v_mfma_f32_16x16x32_bf16 a[144:147], v[144:147], v[40:43], a[144:147]\n\t"
    "buffer_load_dwordx4 v[0:3], v236, s[16:19], s30 offen offset:0 sc1\n\t"
    "v_mfma_f32_16x16x32_bf16 a[176:179], v[144:147], v[44:47], a[176:179]\n\t"
    "v_mfma_f32_16x16x32_bf16 a[20:23], v[148:151], v[24:27], a[20:23]\n\t"
    "ds_read_b64_tr_b16 v[122:123], v238 offset:54016\n\t"
    "v_mfma_f32_16x16x32_bf16 a[52:55], v[148:151], v[28:31], a[52:55]\n\t"
    "v_mfma_f32_16x16x32_bf16 a[84:87], v[148:151], v[32:35], a[84:87]\n\t"
    "ds_read_b64_tr_b16 v[124:125], v239 offset:49920\n\t"
    "buffer_load_dwordx4 v[4:7], v236, s[16:19], s30 offen offset:1024 sc1\n\t"
    "v_mfma_f32_16x16x32_bf16 a[116:119], v[148:151], v[36:39], a[116:119]\n\t"
    "v_mfma_f32_16x16x32_bf16 a[148:151], v[148:151], v[40:43], a[148:151]\n\t"
    "ds_read_b64_tr_b16 v[126:127], v239 offset:54016\n\t"
    "v_mfma_f32_16x16x32_bf16 a[180:183], v[148:151], v[44:47], a[180:183]\n\t"
    "buffer_load_dwordx4 v[8:11], v236, s[16:19], s30 offen offset:2048 sc1\n\t"
    "v_mfma_f32_16x16x32_bf16 a[24:27], v[152:155], v[24:27], a[24:27]\n\t"
    "v_mfma_f32_16x16x32_bf16 a[56:59], v[152:155], v[28:31], a[56:59]\n\t"
    "v_mfma_f32_16x16x32_bf16 a[88:91], v[152:155], v[32:35], a[88:91]\n\t"
    "buffer_load_dwordx4 v[12:15], v237, s[16:19], s30 offen offset:0 sc1\n\t"
    "v_mfma_f32_16x16x32_bf16 a[120:123], v[152:155], v[36:39], a[120:123]\n\t"
    "v_mfma_f32_16x16x32_bf16 a[152:155], v[152:155], v[40:43], a[152:155]\n\t"
    "v_mfma_f32_16x16x32_bf16 a[184:187], v[152:155], v[44:47], a[184:187]\n\t"
    "buffer_load_dwordx4 v[16:19], v237, s[16:19], s30 offen offset:1024 sc1\n\t"
    "v_mfma_f32_16x16x32_bf16 a[28:31], v[156:159], v[24:27], a[28:31]\n\t"
    "v_mfma_f32_16x16x32_bf16 a[60:63], v[156:159], v[28:31], a[60:63]\n\t"
    "v_mfma_f32_16x16x32_bf16 a[92:95], v[156:159], v[32:35], a[92:95]\n\t"
    "buffer_load_dwordx4 v[20:23], v237, s[16:19], s30 offen offset:2048 sc1\n\t"
    "v_mfma_f32_16x16x32_bf16 a[124:127], v[156:159], v[36:39], a[124:127]\n\t"
    "v_mfma_f32_16x16x32_bf16 a[156:159], v[156:159], v[40:43], a[156:159]\n\t"
    "v_mfma_f32_16x16x32_bf16 a[188:191], v[156:159], v[44:47], a[188:191]\n\t"
    "s_waitcnt vmcnt(20) lgkmcnt(0)\n\t"
    "v_mfma_f32_16x16x32_bf16 a[0:3], v[96:99], v[48:51], a[0:3]\n\t"
    "ds_read_b64_tr_b16 v[128:129], v238 offset:57344\n\t"
    "s_add_u32 m0, s31, 40960\n\t"
    "v_mfma_f32_16x16x32_bf16 a[32:35], v[96:99], v[52:55], a[32:35]\n\t"
    "v_mfma_f32_16x16x32_bf16 a[64:67], v[96:99], v[56:59], a[64:67]\n\t"
    "ds_read_b64_tr_b16 v[130:131], v238 offset:61440\n\t"
    "v_mfma_f32_16x16x32_bf16 a[96:99], v[96:99], v[60:63], a[96:99]\n\t"
    "buffer_load_dwordx4 v232, s[12:15], s28 offen offset:0 lds\n\t"
    "v_mfma_f32_16x16x32_bf16 a[128:131], v[96:99], v[64:67], a[128:131]\n\t"
    "ds_read_b64_tr_b16 v[132:133], v239 offset:57344\n\t"
    "v_mfma_f32_16x16x32_bf16 a[160:163], v[96:99], v[68:71], a[160:163]\n\t"
    "v_mfma_f32_16x16x32_bf16 a[4:7], v[100:103], v[48:51], a[4:7]\n\t"
    "ds_read_b64_tr_b16 v[134:135], v239 offset:61440\n\t"
    "buffer_load_dwordx4 v233, s[12:15], s28 offen offset:1024 lds\n\t"
    "v_mfma_f32_16x16x32_bf16 a[36:39], v[100:103], v[52:55], a[36:39]\n\t"
    "v_mfma_f32_16x16x32_bf16 a[68:71], v[100:103], v[56:59], a[68:71]\n\t"
    "v_mfma_f32_16x16x32_bf16 a[100:103], v[100:103], v[60:63], a[100:103]\n\t"
    "ds_read_b64_tr_b16 v[136:137], v238 offset:57600\n\t"
    "v_mfma_f32_16x16x32_bf16 a[132:135], v[100:103], v[64:67], a[132:135]\n\t"
    "buffer_load_dwordx4 v234, s[12:15], s28 offen offset:2048 lds\n\t"
    "v_mfma_f32_16x16x32_bf16 a[164:167], v[100:103], v[68:71], a[164:167]\n\t"
    "ds_read_b64_tr_b16 v[138:139], v238 offset:61696\n\t"
    "v_mfma_f32_16x16x32_bf16 a[8:11], v[104:107], v[48:51], a[8:11]\n\t"
    "v_mfma_f32_16x16x32_bf16 a[40:43], v[104:107], v[52:55], a[40:43]\n\t"
    "ds_read_b64_tr_b16 v[140:141], v239 offset:57600\n\t"
    "buffer_load_dwordx4 v235, s[12:15], s28 offen offset:3072 lds\n\t"
    "v_mfma_f32_16x16x32_bf16 a[72:75], v[104:107], v[56:59], a[72:75]\n\t"
    "v_mfma_f32_16x16x32_bf16 a[104:107], v[104:107], v[60:63], a[104:107]\n\t"
    "v_mfma_f32_16x16x32_bf16 a[136:139], v[104:107], v[64:67], a[136:139]\n\t"
    "ds_read_b64_tr_b16 v[142:143], v239 offset:61696\n\t"
    "v_mfma_f32_16x16x32_bf16 a[168:171], v[104:107], v[68:71], a[168:171]\n\t"
    "s_add_u32 s28, s28, 65536\n\t"
    "v_mfma_f32_16x16x32_bf16 a[12:15], v[108:111], v[48:51], a[12:15]\n\t"
    "ds_read_b64_tr_b16 v[144:145], v238 offset:57856\n\t"
    "v_mfma_f32_16x16x32_bf16 a[44:47], v[108:111], v[52:55], a[44:47]\n\t"
    "v_mfma_f32_16x16x32_bf16 a[76:79], v[108:111], v[56:59], a[76:79]\n\t"
    "ds_read_b64_tr_b16 v[146:147], v238 offset:61952\n\t"
    "s_add_u32 s30, s30, 6144\n\t"
    "v_mfma_f32_16x16x32_bf16 a[108:111], v[108:111], v[60:63], a[108:111]\n\t"
    "v_mfma_f32_16x16x32_bf16 a[140:143], v[108:111], v[64:67], a[140:143]\n\t"
    "v_mfma_f32_16x16x32_bf16 a[172:175], v[108:111], v[68:71], a[172:175]\n\t"
    "ds_read_b64_tr_b16 v[148:149], v239 offset:57856\n\t"
    "v_mfma_f32_16x16x32_bf16 a[16:19], v[112:115], v[48:51], a[16:19]\n\t"
    "buffer_load_dwordx4 v[24:27], v236, s[16:19], s30 offen offset:0 sc1\n\t"
    "v_mfma_f32_16x16x32_bf16 a[48:51], v[112:115], v[52:55], a[48:51]\n\t"
    "ds_read_b64_tr_b16 v[150:151], v239 offset:61952\n\t"
    "v_mfma_f32_16x16x32_bf16 a[80:83], v[112:115], v[56:59], a[80:83]\n\t"
    "v_mfma_f32_16x16x32_bf16 a[112:115], v[112:115], v[60:63], a[112:115]\n\t"
    "ds_read_b64_tr_b16 v[152:153], v238 offset:58112\n\t"
    "buffer_load_dwordx4 v[28:31], v236, s[16:19], s30 offen offset:1024 sc1\n\t"
    "v_mfma_f32_16x16x32_bf16 a[144:147], v[112:115], v[64:67], a[144:147]\n\t"
    "v_mfma_f32_16x16x32_bf16 a[176:179], v[112:115], v[68:71], a[176:179]\n\t"
    "v_mfma_f32_16x16x32_bf16 a[20:23], v[116:119], v[48:51], a[20:23]\n\t"
    "ds_read_b64_tr_b16 v[154:155], v238 offset:62208\n\t"
    "buffer_load_dwordx4 v[32:35], v236, s[16:19], s30 offen offset:2048 sc1\n\t"
    "v_mfma_f32_16x16x32_bf16 a[52:55], v[116:119], v[52:55], a[52:55]\n\t"
    "v_mfma_f32_16x16x32_bf16 a[84:87], v[116:119], v[56:59], a[84:87]\n\t"
    "ds_read_b64_tr_b16 v[156:157], v239 offset:58112\n\t"
    "v_mfma_f32_16x16x32_bf16 a[116:119], v[116:119], v[60:63], a[116:119]\n\t"
    "v_mfma_f32_16x16x32_bf16 a[148:151], v[116:119], v[64:67], a[148:151]\n\t"
    "ds_read_b64_tr_b16 v[158:159], v239 offset:62208\n\t"
    "buffer_load_dwordx4 v[36:39], v237, s[16:19], s30 offen offset:0 sc1\n\t"
    "v_mfma_f32_16x16x32_bf16 a[180:183], v[116:119], v[68:71], a[180:183]\n\t"
    "v_mfma_f32_16x16x32_bf16 a[24:27], v[120:123], v[48:51], a[24:27]\n\t"
    "v_mfma_f32_16x16x32_bf16 a[56:59], v[120:123], v[52:55], a[56:59]\n\t"
    "buffer_load_dwordx4 v[40:43], v237, s[16:19], s30 offen offset:1024 sc1\n\t"
    "v_mfma_f32_16x16x32_bf16 a[88:91], v[120:123], v[56:59], a[88:91]\n\t"
    "v_mfma_f32_16x16x32_bf16 a[120:123], v[120:123], v[60:63], a[120:123]\n\t"
    "v_mfma_f32_16x16x32_bf16 a[152:155], v[120:123], v[64:67], a[152:155]\n\t"
    "v_mfma_f32_16x16x32_bf16 a[184:187], v[120:123], v[68:71], a[184:187]\n\t"
    "buffer_load_dwordx4 v[44:47], v237, s[16:19], s30 offen offset:2048 sc1\n\t"
    "v_mfma_f32_16x16x32_bf16 a[28:31], v[124:127], v[48:51], a[28:31]\n\t"
    "v_mfma_f32_16x16x32_bf16 a[60:63], v[124:127], v[52:55], a[60:63]\n\t"
    "v_mfma_f32_16x16x32_bf16 a[92:95], v[124:127], v[56:59], a[92:95]\n\t"
    "s_getpc_b64 s[68:69]\n\t"
    ".LCT6_%=:\n\t"
    "v_min_u32 v251, .LEND_%=-.LCT6_%=-4, v223\n\t"
    "global_load_dword a255, v251, s[68:69]\n\t"
    "v_mfma_f32_16x16x32_bf16 a[124:127], v[124:127], v[60:63], a[124:127]\n\t"
    "v_mfma_f32_16x16x32_bf16 a[156:159], v[124:127], v[64:67], a[156:159]\n\t"
    "v_mfma_f32_16x16x32_bf16 a[188:191], v[124:127], v[68:71], a[188:191]\n\t"
    "s_waitcnt vmcnt(21) lgkmcnt(0)\n\t"
    "s_barrier\n\t"
    "v_mfma_f32_16x16x32_bf16 a[0:3], v[128:131], v[72:75], a[0:3]\n\t"
    "ds_read_b64_tr_b16 v[96:97], v238 offset:0\n\t"
    "s_add_u32 m0, s31, 49152\n\t"
    "v_mfma_f32_16x16x32_bf16 a[32:35], v[128:131], v[76:79], a[32:35]\n\t"
    "v_mfma_f32_16x16x32_bf16 a[64:67], v[128:131], v[80:83], a[64:67]\n\t"
    "ds_read_b64_tr_b16 v[98:99], v238 offset:4096\n\t"
    "v_mfma_f32_16x16x32_bf16 a[96:99], v[128:131], v[84:87], a[96:99]\n\t"
    "buffer_load_dwordx4 v232, s[12:15], s28 offen offset:0 lds\n\t"
    "v_mfma_f32_16x16x32_bf16 a[128:131], v[128:131], v[88:91], a[128:131]\n\t"
    "ds_read_b64_tr_b16 v[100:101], v239 offset:0\n\t"
    "v_mfma_f32_16x16x32_bf16 a[160:163], v[128:131], v[92:95], a[160:163]\n\t"
    "v_mfma_f32_16x16x32_bf16 a[4:7], v[132:135], v[72:75], a[4:7]\n\t"
    "ds_read_b64_tr_b16 v[102:103], v239 offset:4096\n\t"
    "v_mfma_f32_16x16x32_bf16 a[36:39], v[132:135], v[76:79], a[36:39]\n\t"
    "buffer_load_dwordx4 v233, s[12:15], s28 offen offset:1024 lds\n\t"
    "v_mfma_f32_16x16x32_bf16 a[68:71], v[132:135], v[80:83], a[68:71]\n\t"
    "v_mfma_f32_16x16x32_bf16 a[100:103], v[132:135], v[84:87], a[100:103]\n\t"
    "ds_read_b64_tr_b16 v[104:105], v238 offset:256\n\t"
    "v_mfma_f32_16x16x32_bf16 a[132:135], v[132:135], v[88:91], a[132:135]\n\t"
    "v_mfma_f32_16x16x32_bf16 a[164:167], v[132:135], v[92:95], a[164:167]\n\t"
    "ds_read_b64_tr_b16 v[106:107], v238 offset:4352\n\t"
    "buffer_load_dwordx4 v234, s[12:15], s28 offen offset:2048 lds\n\t"
    "v_mfma_f32_16x16x32_bf16 a[8:11], v[136:139], v[72:75], a[8:11]\n\t"
    "v_mfma_f32_16x16x32_bf16 a[40:43], v[136:139], v[76:79], a[40:43]\n\t"
    "ds_read_b64_tr_b16 v[108:109], v239 offset:256\n\t"
    "v_mfma_f32_16x16x32_bf16 a[72:75], v[136:139], v[80:83], a[72:75]\n\t"
    "buffer_load_dwordx4 v235, s[12:15], s28 offen offset:3072 lds\n\t"
    "v_mfma_f32_16x16x32_bf16 a[104:107], v[136:139], v[84:87], a[104:107]\n\t"
    "v_mfma_f32_16x16x32_bf16 a[136:139], v[136:139], v[88:91], a[136:139]\n\t"
    "ds_read_b64_tr_b16 v[110:111], v239 offset:4352\n\t"
    "v_mfma_f32_16x16x32_bf16 a[168:171], v[136:139], v[92:95], a[168:171]\n\t"
    "v_mfma_f32_16x16x32_bf16 a[12:15], v[140:143], v[72:75], a[12:15]\n\t"
    "ds_read_b64_tr_b16 v[112:113], v238 offset:512\n\t"
    "s_add_u32 s28, s28, 65536\n\t"
    "v_mfma_f32_16x16x32_bf16 a[44:47], v[140:143], v[76:79], a[44:47]\n\t"
    "v_mfma_f32_16x16x32_bf16 a[76:79], v[140:143], v[80:83], a[76:79]\n\t"
    "ds_read_b64_tr_b16 v[114:115], v238 offset:4608\n\t"
    "v_mfma_f32_16x16x32_bf16 a[108:111], v[140:143], v[84:87], a[108:111]\n\t"
    "v_mfma_f32_16x16x32_bf16 a[140:143], v[140:143], v[88:91], a[140:143]\n\t"
    "s_add_u32 s30, s30, 6144\n\t"
    "v_mfma_f32_16x16x32_bf16 a[172:175], v[140:143], v[92:95], a[172:175]\n\t"
    "ds_read_b64_tr_b16 v[116:117], v239 offset:512\n\t"
    "v_mfma_f32_16x16x32_bf16 a[16:19], v[144:147], v[72:75], a[16:19]\n\t"
    "v_mfma_f32_16x16x32_bf16 a[48:51], v[144:147], v[76:79], a[48:51]\n\t"
    "ds_read_b64_tr_b16 v[118:119], v239 offset:4608\n\t"
    "buffer_load_dwordx4 v[48:51], v236, s[16:19], s30 offen offset:0 sc1\n\t"
    "v_mfma_f32_16x16x32_bf16 a[80:83], v[144:147], v[80:83], a[80:83]\n\t"
    "v_mfma_f32_16x16x32_bf16 a[112:115], v[144:147], v[84:87], a[112:115]\n\t"
    "ds_read_b64_tr_b16 v[120:121], v238 offset:768\n\t"
    "v_mfma_f32_16x16x32_bf16 a[144:147], v[144:147], v[88:91], a[144:147]\n\t"
    "v_mfma_f32_16x16x32_bf16 a[176:179], v[144:147], v[92:95], a[176:179]\n\t"
    "buffer_load_dwordx4 v[52:55], v236, s[16:19], s30 offen offset:1024 sc1\n\t"
    "v_mfma_f32_16x16x32_bf16 a[20:23], v[148:151], v[72:75], a[20:23]\n\t"
    "ds_read_b64_tr_b16 v[122:123], v238 offset:4864\n\t"
    "v_mfma_f32_16x16x32_bf16 a[52:55], v[148:151], v[76:79], a[52:55]\n\t"
    "v_mfma_f32_16x16x32_bf16 a[84:87], v[148:151], v[80:83], a[84:87]\n\t"
    "ds_read_b64_tr_b16 v[124:125], v239 offset:768\n\t"
    "v_mfma_f32_16x16x32_bf16 a[116:119], v[148:151], v[84:87], a[116:119]\n\t"
    "buffer_load_dwordx4 v[56:59], v236, s[16:19], s30 offen offset:2048 sc1\n\t"
    "v_mfma_f32_16x16x32_bf16 a[148:151], v[148:151], v[88:91], a[148:151]\n\t"
    "ds_read_b64_tr_b16 v[126:127], v239 offset:4864\n\t"
    "v_mfma_f32_16x16x32_bf16 a[180:183], v[148:151], v[92:95], a[180:183]\n\t"
    "v_mfma_f32_16x16x32_bf16 a[24:27], v[152:155], v[72:75], a[24:27]\n\t"
    "buffer_load_dwordx4 v[60:63], v237, s[16:19], s30 offen offset:0 sc1\n\t"
    "v_mfma_f32_16x16x32_bf16 a[56:59], v[152:155], v[76:79], a[56:59]\n\t"
    "v_mfma_f32_16x16x32_bf16 a[88:91], v[152:155], v[80:83], a[88:91]\n\t"
    "v_mfma_f32_16x16x32_bf16 a[120:123], v[152:155], v[84:87], a[120:123]\n\t"
    "v_mfma_f32_16x16x32_bf16 a[152:155], v[152:155], v[88:91], a[152:155]\n\t"
    "buffer_load_dwordx4 v[64:67], v237, s[16:19], s30 offen offset:1024 sc1\n\t"
    "v_mfma_f32_16x16x32_bf16 a[184:187], v[152:155], v[92:95], a[184:187]\n\t"
    "v_mfma_f32_16x16x32_bf16 a[28:31], v[156:159], v[72:75], a[28:31]\n\t"
    "v_mfma_f32_16x16x32_bf16 a[60:63], v[156:159], v[76:79], a[60:63]\n\t"
    "v_mfma_f32_16x16x32_bf16 a[92:95], v[156:159], v[80:83], a[92:95]\n\t"
    "buffer_load_dwordx4 v[68:71], v237, s[16:19], s30 offen offset:2048 sc1\n\t"
    "v_mfma_f32_16x16x32_bf16 a[124:127], v[156:159], v[84:87], a[124:127]\n\t"
    "v_mfma_f32_16x16x32_bf16 a[156:159], v[156:159], v[88:91], a[156:159]\n\t"
    "v_mfma_f32_16x16x32_bf16 a[188:191], v[156:159], v[92:95], a[188:191]\n\t"
    "s_waitcnt vmcnt(21) lgkmcnt(0)\n\t"
    "v_mfma_f32_16x16x32_bf16 a[0:3], v[96:99], v[0:3], a[0:3]\n\t"
    "ds_read_b64_tr_b16 v[128:129], v238 offset:8192\n\t"
    "s_add_u32 m0, s31, 57344\n\t"
    "v_mfma_f32_16x16x32_bf16 a[32:35], v[96:99], v[4:7], a[32:35]\n\t"
    "v_mfma_f32_16x16x32_bf16 a[64:67], v[96:99], v[8:11], a[64:67]\n\t"
    "ds_read_b64_tr_b16 v[130:131], v238 offset:12288\n\t"
    "v_mfma_f32_16x16x32_bf16 a[96:99], v[96:99], v[12:15], a[96:99]\n\t"
    "buffer_load_dwordx4 v232, s[12:15], s28 offen offset:0 lds\n\t"
    "v_mfma_f32_16x16x32_bf16 a[128:131], v[96:99], v[16:19], a[128:131]\n\t"
    "ds_read_b64_tr_b16 v[132:133], v239 offset:8192\n\t"
    "v_mfma_f32_16x16x32_bf16 a[160:163], v[96:99], v[20:23], a[160:163]\n\t"
    "v_mfma_f32_16x16x32_bf16 a[4:7], v[100:103], v[0:3], a[4:7]\n\t"
    "ds_read_b64_tr_b16 v[134:135], v239 offset:12288\n\t"
    "buffer_load_dwordx4 v233, s[12:15], s28 offen offset:1024 lds\n\t"
    "v_mfma_f32_16x16x32_bf16 a[36:39], v[100:103], v[4:7], a[36:39]\n\t"
    "v_mfma_f32_16x16x32_bf16 a[68:71], v[100:103], v[8:11], a[68:71]\n\t"
    "v_mfma_f32_16x16x32_bf16 a[100:103], v[100:103], v[12:15], a[100:103]\n\t"
    "ds_read_b64_tr_b16 v[136:137], v238 offset:8448\n\t"
    "buffer_load_dwordx4 v234, s[12:15], s28 offen offset:2048 lds\n\t"
    "v_mfma_f32_16x16x32_bf16 a[132:135], v[100:103], v[16:19], a[132:135]\n\t"
    "v_mfma_f32_16x16x32_bf16 a[164:167], v[100:103], v[20:23], a[164:167]\n\t"
    "ds_read_b64_tr_b16 v[138:139], v238 offset:12544\n\t"
    "v_mfma_f32_16x16x32_bf16 a[8:11], v[104:107], v[0:3], a[8:11]\n\t"
    "buffer_load_dwordx4 v235, s[12:15], s28 offen offset:3072 lds\n\t"
    "v_mfma_f32_16x16x32_bf16 a[40:43], v[104:107], v[4:7], a[40:43]\n\t"
    "ds_read_b64_tr_b16 v[140:141], v239 offset:8448\n\t"
    "v_mfma_f32_16x16x32_bf16 a[72:75], v[104:107], v[8:11], a[72:75]\n\t"
    "v_mfma_f32_16x16x32_bf16 a[104:107], v[104:107], v[12:15], a[104:107]\n\t"
    "v_mfma_f32_16x16x32_bf16 a[136:139], v[104:107], v[16:19], a[136:139]\n\t"
    "ds_read_b64_tr_b16 v[142:143], v239 offset:12544\n\t"
    "s_cmp_eq_u32 s94, s29\n\t"
    "s_addc_u32 s94, s94, 0\n\t"
    "v_mfma_f32_16x16x32_bf16 a[168:171], v[104:107], v[20:23], a[168:171]\n\t"
    "v_mfma_f32_16x16x32_bf16 a[12:15], v[108:111], v[0:3], a[12:15]\n\t"
    "ds_read_b64_tr_b16 v[144:145], v238 offset:8704\n\t"
    "v_mfma_f32_16x16x32_bf16 a[44:47], v[108:111], v[4:7], a[44:47]\n\t"
    "s_mul_i32 s28, s94, 524288\n\t"
    "v_mfma_f32_16x16x32_bf16 a[76:79], v[108:111], v[8:11], a[76:79]\n\t"
    "ds_read_b64_tr_b16 v[146:147], v238 offset:12800\n\t"
    "v_mfma_f32_16x16x32_bf16 a[108:111], v[108:111], v[12:15], a[108:111]\n\t"
    "v_mfma_f32_16x16x32_bf16 a[140:143], v[108:111], v[16:19], a[140:143]\n\t"
    "s_add_u32 s94, s94, 1\n\t"
    "v_mfma_f32_16x16x32_bf16 a[172:175], v[108:111], v[20:23], a[172:175]\n\t"
    "ds_read_b64_tr_b16 v[148:149], v239 offset:8704\n\t"
    "v_mfma_f32_16x16x32_bf16 a[16:19], v[112:115], v[0:3], a[16:19]\n\t"
    "v_mfma_f32_16x16x32_bf16 a[48:51], v[112:115], v[4:7], a[48:51]\n\t"
    "ds_read_b64_tr_b16 v[150:151], v239 offset:12800\n\t"
    "s_add_u32 s30, s30, 6144\n\t"
    "v_mfma_f32_16x16x32_bf16 a[80:83], v[112:115], v[8:11], a[80:83]\n\t"
    "v_mfma_f32_16x16x32_bf16 a[112:115], v[112:115], v[12:15], a[112:115]\n\t"
    "ds_read_b64_tr_b16 v[152:153], v238 offset:8960\n\t"
    "v_mfma_f32_16x16x32_bf16 a[144:147], v[112:115], v[16:19], a[144:147]\n\t"
    "buffer_load_dwordx4 v[72:75], v236, s[16:19], s30 offen offset:0 sc1\n\t"
    "v_mfma_f32_16x16x32_bf16 a[176:179], v[112:115], v[20:23], a[176:179]\n\t"
    "v_mfma_f32_16x16x32_bf16 a[20:23], v[116:119], v[0:3], a[20:23]\n\t"
    "ds_read_b64_tr_b16 v[154:155], v238 offset:13056\n\t"
    "v_mfma_f32_16x16x32_bf16 a[52:55], v[116:119], v[4:7], a[52:55]\n\t"
    "v_mfma_f32_16x16x32_bf16 a[84:87], v[116:119], v[8:11], a[84:87]\n\t"
    "ds_read_b64_tr_b16 v[156:157], v239 offset:8960\n\t"
    "buffer_load_dwordx4 v[76:79], v236, s[16:19], s30 offen offset:1024 sc1\n\t"
    "v_mfma_f32_16x16x32_bf16 a[116:119], v[116:119], v[12:15], a[116:119]\n\t"
    "v_mfma_f32_16x16x32_bf16 a[148:151], v[116:119], v[16:19], a[148:151]\n\t"
    "ds_read_b64_tr_b16 v[158:159], v239 offset:13056\n\t"
    "v_mfma_f32_16x16x32_bf16 a[180:183], v[116:119], v[20:23], a[180:183]\n\t"
    "buffer_load_dwordx4 v[80:83], v236, s[16:19], s30 offen offset:2048 sc1\n\t"
    "v_mfma_f32_16x16x32_bf16 a[24:27], v[120:123], v[0:3], a[24:27]\n\t"
    "v_mfma_f32_16x16x32_bf16 a[56:59], v[120:123], v[4:7], a[56:59]\n\t"
    "v_mfma_f32_16x16x32_bf16 a[88:91], v[120:123], v[8:11], a[88:91]\n\t"
    "buffer_load_dwordx4 v[84:87], v237, s[16:19], s30 offen offset:0 sc1\n\t"
    "v_mfma_f32_16x16x32_bf16 a[120:123], v[120:123], v[12:15], a[120:123]\n\t"
    "v_mfma_f32_16x16x32_bf16 a[152:155], v[120:123], v[16:19], a[152:155]\n\t"
    "v_mfma_f32_16x16x32_bf16 a[184:187], v[120:123], v[20:23], a[184:187]\n\t"
    "buffer_load_dwordx4 v[88:91], v237, s[16:19], s30 offen offset:1024 sc1\n\t"
    "v_mfma_f32_16x16x32_bf16 a[28:31], v[124:127], v[0:3], a[28:31]\n\t"
    "v_mfma_f32_16x16x32_bf16 a[60:63], v[124:127], v[4:7], a[60:63]\n\t"
    "v_mfma_f32_16x16x32_bf16 a[92:95], v[124:127], v[8:11], a[92:95]\n\t"
    "buffer_load_dwordx4 v[92:95], v237, s[16:19], s30 offen offset:2048 sc1\n\t"
    "v_mfma_f32_16x16x32_bf16 a[124:127], v[124:127], v[12:15], a[124:127]\n\t"
    "v_mfma_f32_16x16x32_bf16 a[156:159], v[124:127], v[16:19], a[156:159]\n\t"
    "v_mfma_f32_16x16x32_bf16 a[188:191], v[124:127], v[20:23], a[188:191]\n\t"
    "s_waitcnt vmcnt(21) lgkmcnt(0)\n\t"
    "s_barrier\n\t"
    "v_mfma_f32_16x16x32_bf16 a[0:3], v[128:131], v[24:27], a[0:3]\n\t"
    "ds_read_b64_tr_b16 v[96:97], v238 offset:16384\n\t"
    "s_add_u32 m0, s31, 0\n\t"
    "v_mfma_f32_16x16x32_bf16 a[32:35], v[128:131], v[28:31], a[32:35]\n\t"
    "v_mfma_f32_16x16x32_bf16 a[64:67], v[128:131], v[32:35], a[64:67]\n\t"
    "ds_read_b64_tr_b16 v[98:99], v238 offset:20480\n\t"
    "v_mfma_f32_16x16x32_bf16 a[96:99], v[128:131], v[36:39], a[96:99]\n\t"
    "buffer_load_dwordx4 v232, s[12:15], s28 offen offset:0 lds\n\t"
    "v_mfma_f32_16x16x32_bf16 a[128:131], v[128:131], v[40:43], a[128:131]\n\t"
    "ds_read_b64_tr_b16 v[100:101], v239 offset:16384\n\t"
    "v_mfma_f32_16x16x32_bf16 a[160:163], v[128:131], v[44:47], a[160:163]\n\t"
    "v_mfma_f32_16x16x32_bf16 a[4:7], v[132:135], v[24:27], a[4:7]\n\t"
    "ds_read_b64_tr_b16 v[102:103], v239 offset:20480\n\t"
    "buffer_load_dwordx4 v233, s[12:15], s28 offen offset:1024 lds\n\t"
    "v_mfma_f32_16x16x32_bf16 a[36:39], v[132:135], v[28:31], a[36:39]\n\t"
    "v_mfma_f32_16x16x32_bf16 a[68:71], v[132:135], v[32:35], a[68:71]\n\t"
    "v_mfma_f32_16x16x32_bf16 a[100:103], v[132:135], v[36:39], a[100:103]\n\t"
    "ds_read_b64_tr_b16 v[104:105], v238 offset:16640\n\t"
    "buffer_load_dwordx4 v234, s[12:15], s28 offen offset:2048 lds\n\t"
    "v_mfma_f32_16x16x32_bf16 a[132:135], v[132:135], v[40:43], a[132:135]\n\t"
    "v_mfma_f32_16x16x32_bf16 a[164:167], v[132:135], v[44:47], a[164:167]\n\t"
    "ds_read_b64_tr_b16 v[106:107], v238 offset:20736\n\t"
    "v_mfma_f32_16x16x32_bf16 a[8:11], v[136:139], v[24:27], a[8:11]\n\t"
    "buffer_load_dwordx4 v235, s[12:15], s28 offen offset:3072 lds\n\t"
    "v_mfma_f32_16x16x32_bf16 a[40:43], v[136:139], v[28:31], a[40:43]\n\t"
    "ds_read_b64_tr_b16 v[108:109], v239 offset:16640\n\t"
    "v_mfma_f32_16x16x32_bf16 a[72:75], v[136:139], v[32:35], a[72:75]\n\t"
    "v_mfma_f32_16x16x32_bf16 a[104:107], v[136:139], v[36:39], a[104:107]\n\t"
    "v_mfma_f32_16x16x32_bf16 a[136:139], v[136:139], v[40:43], a[136:139]\n\t"
    "ds_read_b64_tr_b16 v[110:111], v239 offset:20736\n\t"
    "s_add_u32 s28, s28, 65536\n\t"
    "v_mfma_f32_16x16x32_bf16 a[168:171], v[136:139], v[44:47], a[168:171]\n\t"
    "v_mfma_f32_16x16x32_bf16 a[12:15], v[140:143], v[24:27], a[12:15]\n\t"
    "ds_read_b64_tr_b16 v[112:113], v238 offset:16896\n\t"
    "v_mfma_f32_16x16x32_bf16 a[44:47], v[140:143], v[28:31], a[44:47]\n\t"
    "s_cmp_eq_u32 s95, s29\n\t"
    "s_addc_u32 s95, s95, 0\n\t"
    "v_mfma_f32_16x16x32_bf16 a[76:79], v[140:143], v[32:35], a[76:79]\n\t"
    "ds_read_b64_tr_b16 v[114:115], v238 offset:20992\n\t"
    "v_mfma_f32_16x16x32_bf16 a[108:111], v[140:143], v[36:39], a[108:111]\n\t"
    "v_mfma_f32_16x16x32_bf16 a[140:143], v[140:143], v[40:43], a[140:143]\n\t"
    "s_mul_i32 s30, s95, 49152\n\t"
    "v_mfma_f32_16x16x32_bf16 a[172:175], v[140:143], v[44:47], a[172:175]\n\t"
    "ds_read_b64_tr_b16 v[116:117], v239 offset:16896\n\t"
    "v_mfma_f32_16x16x32_bf16 a[16:19], v[144:147], v[24:27], a[16:19]\n\t"
    "v_mfma_f32_16x16x32_bf16 a[48:51], v[144:147], v[28:31], a[48:51]\n\t"
    "ds_read_b64_tr_b16 v[118:119], v239 offset:20992\n\t"
    "s_add_u32 s95, s95, 1\n\t"
    "v_mfma_f32_16x16x32_bf16 a[80:83], v[144:147], v[32:35], a[80:83]\n\t"
    "v_mfma_f32_16x16x32_bf16 a[112:115], v[144:147], v[36:39], a[112:115]\n\t"
    "ds_read_b64_tr_b16 v[120:121], v238 offset:17152\n\t"
    "v_mfma_f32_16x16x32_bf16 a[144:147], v[144:147], v[40:43], a[144:147]\n\t"
    "buffer_load_dwordx4 v[0:3], v236, s[16:19], s30 offen offset:0 sc1\n\t"
    "v_mfma_f32_16x16x32_bf16 a[176:179], v[144:147], v[44:47], a[176:179]\n\t"
    "v_mfma_f32_16x16x32_bf16 a[20:23], v[148:151], v[24:27], a[20:23]\n\t"
    "ds_read_b64_tr_b16 v[122:123], v238 offset:21248\n\t"
    "v_mfma_f32_16x16x32_bf16 a[52:55], v[148:151], v[28:31], a[52:55]\n\t"
    "v_mfma_f32_16x16x32_bf16 a[84:87], v[148:151], v[32:35], a[84:87]\n\t"
    "ds_read_b64_tr_b16 v[124:125], v239 offset:17152\n\t"
    "buffer_load_dwordx4 v[4:7], v236, s[16:19], s30 offen offset:1024 sc1\n\t"
    "v_mfma_f32_16x16x32_bf16 a[116:119], v[148:151], v[36:39], a[116:119]\n\t"
    "v_mfma_f32_16x16x32_bf16 a[148:151], v[148:151], v[40:43], a[148:151]\n\t"
    "ds_read_b64_tr_b16 v[126:127], v239 offset:21248\n\t"
    "v_mfma_f32_16x16x32_bf16 a[180:183], v[148:151], v[44:47], a[180:183]\n\t"
    "buffer_load_dwordx4 v[8:11], v236, s[16:19], s30 offen offset:2048 sc1\n\t"
    "v_mfma_f32_16x16x32_bf16 a[24:27], v[152:155], v[24:27], a[24:27]\n\t"
    "v_mfma_f32_16x16x32_bf16 a[56:59], v[152:155], v[28:31], a[56:59]\n\t"
    "v_mfma_f32_16x16x32_bf16 a[88:91], v[152:155], v[32:35], a[88:91]\n\t"
    "buffer_load_dwordx4 v[12:15], v237, s[16:19], s30 offen offset:0 sc1\n\t"
    "v_mfma_f32_16x16x32_bf16 a[120:123], v[152:155], v[36:39], a[120:123]\n\t"
    "v_mfma_f32_16x16x32_bf16 a[152:155], v[152:155], v[40:43], a[152:155]\n\t"
    "v_mfma_f32_16x16x32_bf16 a[184:187], v[152:155], v[44:47], a[184:187]\n\t"
    "buffer_load_dwordx4 v[16:19], v237, s[16:19], s30 offen offset:1024 sc1\n\t"
    "v_mfma_f32_16x16x32_bf16 a[28:31], v[156:159], v[24:27], a[28:31]\n\t"
    "v_mfma_f32_16x16x32_bf16 a[60:63], v[156:159], v[28:31], a[60:63]\n\t"
    "v_mfma_f32_16x16x32_bf16 a[92:95], v[156:159], v[32:35], a[92:95]\n\t"
    "buffer_load_dwordx4 v[20:23], v237, s[16:19], s30 offen offset:2048 sc1\n\t"
    "v_mfma_f32_16x16x32_bf16 a[124:127], v[156:159], v[36:39], a[124:127]\n\t"
    "v_mfma_f32_16x16x32_bf16 a[156:159], v[156:159], v[40:43], a[156:159]\n\t"
    "v_mfma_f32_16x16x32_bf16 a[188:191], v[156:159], v[44:47], a[188:191]\n\t"
    "s_waitcnt vmcnt(20) lgkmcnt(0)\n\t"
    "v_mfma_f32_16x16x32_bf16 a[0:3], v[96:99], v[48:51], a[0:3]\n\t"
    "ds_read_b64_tr_b16 v[128:129], v238 offset:24576\n\t"
    "s_add_u32 m0, s31, 8192\n\t"
    "v_mfma_f32_16x16x32_bf16 a[32:35], v[96:99], v[52:55], a[32:35]\n\t"
    "v_mfma_f32_16x16x32_bf16 a[64:67], v[96:99], v[56:59], a[64:67]\n\t"
    "ds_read_b64_tr_b16 v[130:131], v238 offset:28672\n\t"
    "v_mfma_f32_16x16x32_bf16 a[96:99], v[96:99], v[60:63], a[96:99]\n\t"
    "buffer_load_dwordx4 v232, s[12:15], s28 offen offset:0 lds\n\t"
    "v_mfma_f32_16x16x32_bf16 a[128:131], v[96:99], v[64:67], a[128:131]\n\t"
    "ds_read_b64_tr_b16 v[132:133], v239 offset:24576\n\t"
    "v_mfma_f32_16x16x32_bf16 a[160:163], v[96:99], v[68:71], a[160:163]\n\t"
    "v_mfma_f32_16x16x32_bf16 a[4:7], v[100:103], v[48:51], a[4:7]\n\t"
    "ds_read_b64_tr_b16 v[134:135], v239 offset:28672\n\t"
    "v_mfma_f32_16x16x32_bf16 a[36:39], v[100:103], v[52:55], a[36:39]\n\t"
    "buffer_load_dwordx4 v233, s[12:15], s28 offen offset:1024 lds\n\t"
    "v_mfma_f32_16x16x32_bf16 a[68:71], v[100:103], v[56:59], a[68:71]\n\t"
    "v_mfma_f32_16x16x32_bf16 a[100:103], v[100:103], v[60:63], a[100:103]\n\t"
    "ds_read_b64_tr_b16 v[136:137], v238 offset:24832\n\t"
    "v_mfma_f32_16x16x32_bf16 a[132:135], v[100:103], v[64:67], a[132:135]\n\t"
    "v_mfma_f32_16x16x32_bf16 a[164:167], v[100:103], v[68:71], a[164:167]\n\t"
    "ds_read_b64_tr_b16 v[138:139], v238 offset:28928\n\t"
    "buffer_load_dwordx4 v234, s[12:15], s28 offen offset:2048 lds\n\t"
    "v_mfma_f32_16x16x32_bf16 a[8:11], v[104:107], v[48:51], a[8:11]\n\t"
    "v_mfma_f32_16x16x32_bf16 a[40:43], v[104:107], v[52:55], a[40:43]\n\t"
    "ds_read_b64_tr_b16 v[140:141], v239 offset:24832\n\t"
    "v_mfma_f32_16x16x32_bf16 a[72:75], v[104:107], v[56:59], a[72:75]\n\t"
    "buffer_load_dwordx4 v235, s[12:15], s28 offen offset:3072 lds\n\t"
    "v_mfma_f32_16x16x32_bf16 a[104:107], v[104:107], v[60:63], a[104:107]\n\t"
    "v_mfma_f32_16x16x32_bf16 a[136:139], v[104:107], v[64:67], a[136:139]\n\t"
    "ds_read_b64_tr_b16 v[142:143], v239 offset:28928\n\t"
    "v_mfma_f32_16x16x32_bf16 a[168:171], v[104:107], v[68:71], a[168:171]\n\t"
    "v_mfma_f32_16x16x32_bf16 a[12:15], v[108:111], v[48:51], a[12:15]\n\t"
    "ds_read_b64_tr_b16 v[144:145], v238 offset:25088\n\t"
    "s_add_u32 s28, s28, 65536\n\t"
    "v_mfma_f32_16x16x32_bf16 a[44:47], v[108:111], v[52:55], a[44:47]\n\t"
    "v_mfma_f32_16x16x32_bf16 a[76:79], v[108:111], v[56:59], a[76:79]\n\t"
    "ds_read_b64_tr_b16 v[146:147], v238 offset:29184\n\t"
    "v_mfma_f32_16x16x32_bf16 a[108:111], v[108:111], v[60:63], a[108:111]\n\t"
    "v_mfma_f32_16x16x32_bf16 a[140:143], v[108:111], v[64:67], a[140:143]\n\t"
    "s_add_u32 s30, s30, 6144\n\t"
    "v_mfma_f32_16x16x32_bf16 a[172:175], v[108:111], v[68:71], a[172:175]\n\t"
    "ds_read_b64_tr_b16 v[148:149], v239 offset:25088\n\t"
    "v_mfma_f32_16x16x32_bf16 a[16:19], v[112:115], v[48:51], a[16:19]\n\t"
    "v_mfma_f32_16x16x32_bf16 a[48:51], v[112:115], v[52:55], a[48:51]\n\t"
    "ds_read_b64_tr_b16 v[150:151], v239 offset:29184\n\t"
    "buffer_load_dwordx4 v[24:27], v236, s[16:19], s30 offen offset:0 sc1\n\t"
    "v_mfma_f32_16x16x32_bf16 a[80:83], v[112:115], v[56:59], a[80:83]\n\t"
    "v_mfma_f32_16x16x32_bf16 a[112:115], v[112:115], v[60:63], a[112:115]\n\t"
    "ds_read_b64_tr_b16 v[152:153], v238 offset:25344\n\t"
    "v_mfma_f32_16x16x32_bf16 a[144:147], v[112:115], v[64:67], a[144:147]\n\t"
    "v_mfma_f32_16x16x32_bf16 a[176:179], v[112:115], v[68:71], a[176:179]\n\t"
    "buffer_load_dwordx4 v[28:31], v236, s[16:19], s30 offen offset:1024 sc1\n\t"
    "v_mfma_f32_16x16x32_bf16 a[20:23], v[116:119], v[48:51], a[20:23]\n\t"
    "ds_read_b64_tr_b16 v[154:155], v238 offset:29440\n\t"
    "v_mfma_f32_16x16x32_bf16 a[52:55], v[116:119], v[52:55], a[52:55]\n\t"
    "v_mfma_f32_16x16x32_bf16 a[84:87], v[116:119], v[56:59], a[84:87]\n\t"
    "ds_read_b64_tr_b16 v[156:157], v239 offset:25344\n\t"
    "v_mfma_f32_16x16x32_bf16 a[116:119], v[116:119], v[60:63], a[116:119]\n\t"
    "buffer_load_dwordx4 v[32:35], v236, s[16:19], s30 offen offset:2048 sc1\n\t"
    "v_mfma_f32_16x16x32_bf16 a[148:151], v[116:119], v[64:67], a[148:151]\n\t"
    "ds_read_b64_tr_b16 v[158:159], v239 offset:29440\n\t"
    "v_mfma_f32_16x16x32_bf16 a[180:183], v[116:119], v[68:71], a[180:183]\n\t"
    "v_mfma_f32_16x16x32_bf16 a[24:27], v[120:123], v[48:51], a[24:27]\n\t"
    "buffer_load_dwordx4 v[36:39], v237, s[16:19], s30 offen offset:0 sc1\n\t"
    "v_mfma_f32_16x16x32_bf16 a[56:59], v[120:123], v[52:55], a[56:59]\n\t"
    "v_mfma_f32_16x16x32_bf16 a[88:91], v[120:123], v[56:59], a[88:91]\n\t"
    "v_mfma_f32_16x16x32_bf16 a[120:123], v[120:123], v[60:63], a[120:123]\n\t"
    "v_mfma_f32_16x16x32_bf16 a[152:155], v[120:123], v[64:67], a[152:155]\n\t"
    "buffer_load_dwordx4 v[40:43], v237, s[16:19], s30 offen offset:1024 sc1\n\t"
    "v_mfma_f32_16x16x32_bf16 a[184:187], v[120:123], v[68:71], a[184:187]\n\t"
    "v_mfma_f32_16x16x32_bf16 a[28:31], v[124:127], v[48:51], a[28:31]\n\t"
    "v_mfma_f32_16x16x32_bf16 a[60:63], v[124:127], v[52:55], a[60:63]\n\t"
    "v_mfma_f32_16x16x32_bf16 a[92:95], v[124:127], v[56:59], a[92:95]\n\t"
    "buffer_load_dwordx4 v[44:47], v237, s[16:19], s30 offen offset:2048 sc1\n\t"
    "v_mfma_f32_16x16x32_bf16 a[124:127], v[124:127], v[60:63], a[124:127]\n\t"
    "v_mfma_f32_16x16x32_bf16 a[156:159], v[124:127], v[64:67], a[156:159]\n\t"
    "v_mfma_f32_16x16x32_bf16 a[188:191], v[124:127], v[68:71], a[188:191]\n\t"
    "s_waitcnt vmcnt(20) lgkmcnt(0)\n\t"
    "s_barrier\n\t"
    "v_mfma_f32_16x16x32_bf16 a[0:3], v[128:131], v[72:75], a[0:3]\n\t"
    "ds_read_b64_tr_b16 v[96:97], v238 offset:32768\n\t"
    "s_add_u32 m0, s31, 16384\n\t"
    "v_mfma_f32_16x16x32_bf16 a[32:35], v[128:131], v[76:79], a[32:35]\n\t"
    "v_mfma_f32_16x16x32_bf16 a[64:67], v[128:131], v[80:83], a[64:67]\n\t"
    "ds_read_b64_tr_b16 v[98:99], v238 offset:36864\n\t"
    "v_mfma_f32_16x16x32_bf16 a[96:99], v[128:131], v[84:87], a[96:99]\n\t"
    "buffer_load_dwordx4 v232, s[12:15], s28 offen offset:0 lds\n\t"
    "v_mfma_f32_16x16x32_bf16 a[128:131], v[128:131], v[88:91], a[128:131]\n\t"
    "ds_read_b64_tr_b16 v[100:101], v239 offset:32768\n\t"
    "v_mfma_f32_16x16x32_bf16 a[160:163], v[128:131], v[92:95], a[160:163]\n\t"
    "v_mfma_f32_16x16x32_bf16 a[4:7], v[132:135], v[72:75], a[4:7]\n\t"
    "ds_read_b64_tr_b16 v[102:103], v239 offset:36864\n\t"
    "v_mfma_f32_16x16x32_bf16 a[36:39], v[132:135], v[76:79], a[36:39]\n\t"
    "buffer_load_dwordx4 v233, s[12:15], s28 offen offset:1024 lds\n\t"
    "v_mfma_f32_16x16x32_bf16 a[68:71], v[132:135], v[80:83], a[68:71]\n\t"
    "v_mfma_f32_16x16x32_bf16 a[100:103], v[132:135], v[84:87], a[100:103]\n\t"
    "ds_read_b64_tr_b16 v[104:105], v238 offset:33024\n\t"
    "v_mfma_f32_16x16x32_bf16 a[132:135], v[132:135], v[88:91], a[132:135]\n\t"
    "v_mfma_f32_16x16x32_bf16 a[164:167], v[132:135], v[92:95], a[164:167]\n\t"
    "ds_read_b64_tr_b16 v[106:107], v238 offset:37120\n\t"
    "buffer_load_dwordx4 v234, s[12:15], s28 offen offset:2048 lds\n\t"
    "v_mfma_f32_16x16x32_bf16 a[8:11], v[136:139], v[72:75], a[8:11]\n\t"
    "v_mfma_f32_16x16x32_bf16 a[40:43], v[136:139], v[76:79], a[40:43]\n\t"
    "ds_read_b64_tr_b16 v[108:109], v239 offset:33024\n\t"
    "v_mfma_f32_16x16x32_bf16 a[72:75], v[136:139], v[80:83], a[72:75]\n\t"
    "buffer_load_dwordx4 v235, s[12:15], s28 offen offset:3072 lds\n\t"
    "v_mfma_f32_16x16x32_bf16 a[104:107], v[136:139], v[84:87], a[104:107]\n\t"
    "v_mfma_f32_16x16x32_bf16 a[136:139], v[136:139], v[88:91], a[136:139]\n\t"
    "ds_read_b64_tr_b16 v[110:111], v239 offset:37120\n\t"
    "v_mfma_f32_16x16x32_bf16 a[168:171], v[136:139], v[92:95], a[168:171]\n\t"
    "v_mfma_f32_16x16x32_bf16 a[12:15], v[140:143], v[72:75], a[12:15]\n\t"
    "ds_read_b64_tr_b16 v[112:113], v238 offset:33280\n\t"
    "s_add_u32 s28, s28, 65536\n\t"
    "v_mfma_f32_16x16x32_bf16 a[44:47], v[140:143], v[76:79], a[44:47]\n\t"
    "v_mfma_f32_16x16x32_bf16 a[76:79], v[140:143], v[80:83], a[76:79]\n\t"
    "ds_read_b64_tr_b16 v[114:115], v238 offset:37376\n\t"
    "v_mfma_f32_16x16x32_bf16 a[108:111], v[140:143], v[84:87], a[108:111]\n\t"
    "v_mfma_f32_16x16x32_bf16 a[140:143], v[140:143], v[88:91], a[140:143]\n\t"
    "s_add_u32 s30, s30, 6144\n\t"
    "v_mfma_f32_16x16x32_bf16 a[172:175], v[140:143], v[92:95], a[172:175]\n\t"
    "ds_read_b64_tr_b16 v[116:117], v239 offset:33280\n\t"
    "v_mfma_f32_16x16x32_bf16 a[16:19], v[144:147], v[72:75], a[16:19]\n\t"
    "v_mfma_f32_16x16x32_bf16 a[48:51], v[144:147], v[76:79], a[48:51]\n\t"
    "ds_read_b64_tr_b16 v[118:119], v239 offset:37376\n\t"
    "buffer_load_dwordx4 v[48:51], v236, s[16:19], s30 offen offset:0 sc1\n\t"
    "v_mfma_f32_16x16x32_bf16 a[80:83], v[144:147], v[80:83], a[80:83]\n\t"
    "v_mfma_f32_16x16x32_bf16 a[112:115], v[144:147], v[84:87], a[112:115]\n\t"
    "ds_read_b64_tr_b16 v[120:121], v238 offset:33536\n\t"
    "v_mfma_f32_16x16x32_bf16 a[144:147], v[144:147], v[88:91], a[144:147]\n\t"
    "v_mfma_f32_16x16x32_bf16 a[176:179], v[144:147], v[92:95], a[176:179]\n\t"
    "buffer_load_dwordx4 v[52:55], v236, s[16:19], s30 offen offset:1024 sc1\n\t"
    "v_mfma_f32_16x16x32_bf16 a[20:23], v[148:151], v[72:75], a[20:23]\n\t"
    "ds_read_b64_tr_b16 v[122:123], v238 offset:37632\n\t"
    "v_mfma_f32_16x16x32_bf16 a[52:55], v[148:151], v[76:79], a[52:55]\n\t"
    "v_mfma_f32_16x16x32_bf16 a[84:87], v[148:151], v[80:83], a[84:87]\n\t"
    "ds_read_b64_tr_b16 v[124:125], v239 offset:33536\n\t"
    "v_mfma_f32_16x16x32_bf16 a[116:119], v[148:151], v[84:87], a[116:119]\n\t"
    "buffer_load_dwordx4 v[56:59], v236, s[16:19], s30 offen offset:2048 sc1\n\t"
    "v_mfma_f32_16x16x32_bf16 a[148:151], v[148:151], v[88:91], a[148:151]\n\t"
    "ds_read_b64_tr_b16 v[126:127], v239 offset:37632\n\t"
    "v_mfma_f32_16x16x32_bf16 a[180:183], v[148:151], v[92:95], a[180:183]\n\t"
    "v_mfma_f32_16x16x32_bf16 a[24:27], v[152:155], v[72:75], a[24:27]\n\t"
    "buffer_load_dwordx4 v[60:63], v237, s[16:19], s30 offen offset:0 sc1\n\t"
    "v_mfma_f32_16x16x32_bf16 a[56:59], v[152:155], v[76:79], a[56:59]\n\t"
    "v_mfma_f32_16x16x32_bf16 a[88:91], v[152:155], v[80:83], a[88:91]\n\t"
    "v_mfma_f32_16x16x32_bf16 a[120:123], v[152:155], v[84:87], a[120:123]\n\t"
    "v_mfma_f32_16x16x32_bf16 a[152:155], v[152:155], v[88:91], a[152:155]\n\t"
    "buffer_load_dwordx4 v[64:67], v237, s[16:19], s30 offen offset:1024 sc1\n\t"
    "v_mfma_f32_16x16x32_bf16 a[184:187], v[152:155], v[92:95], a[184:187]\n\t"
    "v_mfma_f32_16x16x32_bf16 a[28:31], v[156:159], v[72:75], a[28:31]\n\t"
    "v_mfma_f32_16x16x32_bf16 a[60:63], v[156:159], v[76:79], a[60:63]\n\t"
    "v_mfma_f32_16x16x32_bf16 a[92:95], v[156:159], v[80:83], a[92:95]\n\t"
    "buffer_load_dwordx4 v[68:71], v237, s[16:19], s30 offen offset:2048 sc1\n\t"
    "v_mfma_f32_16x16x32_bf16 a[124:127], v[156:159], v[84:87], a[124:127]\n\t"
    "v_mfma_f32_16x16x32_bf16 a[156:159], v[156:159], v[88:91], a[156:159]\n\t"
    "v_mfma_f32_16x16x32_bf16 a[188:191], v[156:159], v[92:95], a[188:191]\n\t"
    "s_waitcnt vmcnt(20) lgkmcnt(0)\n\t"
    "v_mfma_f32_16x16x32_bf16 a[0:3], v[96:99], v[0:3], a[0:3]\n\t"
    "ds_read_b64_tr_b16 v[128:129], v238 offset:40960\n\t"
    "s_add_u32 m0, s31, 24576\n\t"
    "v_mfma_f32_16x16x32_bf16 a[32:35], v[96:99], v[4:7], a[32:35]\n\t"
    "v_mfma_f32_16x16x32_bf16 a[64:67], v[96:99], v[8:11], a[64:67]\n\t"
    "ds_read_b64_tr_b16 v[130:131], v238 offset:45056\n\t"
    "v_mfma_f32_16x16x32_bf16 a[96:99], v[96:99], v[12:15], a[96:99]\n\t"
    "buffer_load_dwordx4 v232, s[12:15], s28 offen offset:0 lds\n\t"
    "v_mfma_f32_16x16x32_bf16 a[128:131], v[96:99], v[16:19], a[128:131]\n\t"
    "ds_read_b64_tr_b16 v[132:133], v239 offset:40960\n\t"
    "v_mfma_f32_16x16x32_bf16 a[160:163], v[96:99], v[20:23], a[160:163]\n\t"
    "v_mfma_f32_16x16x32_bf16 a[4:7], v[100:103], v[0:3], a[4:7]\n\t"
    "ds_read_b64_tr_b16 v[134:135], v239 offset:45056\n\t"
    "buffer_load_dwordx4 v233, s[12:15], s28 offen offset:1024 lds\n\t"
    "v_mfma_f32_16x16x32_bf16 a[36:39], v[100:103], v[4:7], a[36:39]\n\t"
    "v_mfma_f32_16x16x32_bf16 a[68:71], v[100:103], v[8:11], a[68:71]\n\t"
    "v_mfma_f32_16x16x32_bf16 a[100:103], v[100:103], v[12:15], a[100:103]\n\t"
    "ds_read_b64_tr_b16 v[136:137], v238 offset:41216\n\t"
    "buffer_load_dwordx4 v234, s[12:15], s28 offen offset:2048 lds\n\t"
    "v_mfma_f32_16x16x32_bf16 a[132:135], v[100:103], v[16:19], a[132:135]\n\t"
    "v_mfma_f32_16x16x32_bf16 a[164:167], v[100:103], v[20:23], a[164:167]\n\t"
    "ds_read_b64_tr_b16 v[138:139], v238 offset:45312\n\t"
    "v_mfma_f32_16x16x32_bf16 a[8:11], v[104:107], v[0:3], a[8:11]\n\t"
    "buffer_load_dwordx4 v235, s[12:15], s28 offen offset:3072 lds\n\t"
    "v_mfma_f32_16x16x32_bf16 a[40:43], v[104:107], v[4:7], a[40:43]\n\t"
    "ds_read_b64_tr_b16 v[140:141], v239 offset:41216\n\t"
    "v_mfma_f32_16x16x32_bf16 a[72:75], v[104:107], v[8:11], a[72:75]\n\t"
    "v_mfma_f32_16x16x32_bf16 a[104:107], v[104:107], v[12:15], a[104:107]\n\t"
    "v_mfma_f32_16x16x32_bf16 a[136:139], v[104:107], v[16:19], a[136:139]\n\t"
    "ds_read_b64_tr_b16 v[142:143], v239 offset:45312\n\t"
    "s_cmp_eq_u32 s94, s29\n\t"
    "s_addc_u32 s94, s94, 0\n\t"
    "v_mfma_f32_16x16x32_bf16 a[168:171], v[104:107], v[20:23], a[168:171]\n\t"
    "v_mfma_f32_16x16x32_bf16 a[12:15], v[108:111], v[0:3], a[12:15]\n\t"
    "ds_read_b64_tr_b16 v[144:145], v238 offset:41472\n\t"
    "v_mfma_f32_16x16x32_bf16 a[44:47], v[108:111], v[4:7], a[44:47]\n\t"
    "s_mul_i32 s28, s94, 524288\n\t"
    "v_mfma_f32_16x16x32_bf16 a[76:79], v[108:111], v[8:11], a[76:79]\n\t"
    "ds_read_b64_tr_b16 v[146:147], v238 offset:45568\n\t"
    "v_mfma_f32_16x16x32_bf16 a[108:111], v[108:111], v[12:15], a[108:111]\n\t"
    "v_mfma_f32_16x16x32_bf16 a[140:143], v[108:111], v[16:19], a[140:143]\n\t"
    "s_add_u32 s94, s94, 1\n\t"
    "v_mfma_f32_16x16x32_bf16 a[172:175], v[108:111], v[20:23], a[172:175]\n\t"
    "ds_read_b64_tr_b16 v[148:149], v239 offset:41472\n\t"
    "v_mfma_f32_16x16x32_bf16 a[16:19], v[112:115], v[0:3], a[16:19]\n\t"
    "v_mfma_f32_16x16x32_bf16 a[48:51], v[112:115], v[4:7], a[48:51]\n\t"
    "ds_read_b64_tr_b16 v[150:151], v239 offset:45568\n\t"
    "s_add_u32 s30, s30, 6144\n\t"
    "v_mfma_f32_16x16x32_bf16 a[80:83], v[112:115], v[8:11], a[80:83]\n\t"
    "v_mfma_f32_16x16x32_bf16 a[112:115], v[112:115], v[12:15], a[112:115]\n\t"
    "ds_read_b64_tr_b16 v[152:153], v238 offset:41728\n\t"
    "v_mfma_f32_16x16x32_bf16 a[144:147], v[112:115], v[16:19], a[144:147]\n\t"
    "buffer_load_dwordx4 v[72:75], v236, s[16:19], s30 offen offset:0 sc1\n\t"
    "v_mfma_f32_16x16x32_bf16 a[176:179], v[112:115], v[20:23], a[176:179]\n\t"
    "v_mfma_f32_16x16x32_bf16 a[20:23], v[116:119], v[0:3], a[20:23]\n\t"
    "ds_read_b64_tr_b16 v[154:155], v238 offset:45824\n\t"
    "v_mfma_f32_16x16x32_bf16 a[52:55], v[116:119], v[4:7], a[52:55]\n\t"
    "v_mfma_f32_16x16x32_bf16 a[84:87], v[116:119], v[8:11], a[84:87]\n\t"
    "ds_read_b64_tr_b16 v[156:157], v239 offset:41728\n\t"
    "buffer_load_dwordx4 v[76:79], v236, s[16:19], s30 offen offset:1024 sc1\n\t"
    "v_mfma_f32_16x16x32_bf16 a[116:119], v[116:119], v[12:15], a[116:119]\n\t"
    "v_mfma_f32_16x16x32_bf16 a[148:151], v[116:119], v[16:19], a[148:151]\n\t"
    "ds_read_b64_tr_b16 v[158:159], v239 offset:45824\n\t"
    "v_mfma_f32_16x16x32_bf16 a[180:183], v[116:119], v[20:23], a[180:183]\n\t"
    "buffer_load_dwordx4 v[80:83], v236, s[16:19], s30 offen offset:2048 sc1\n\t"
    "v_mfma_f32_16x16x32_bf16 a[24:27], v[120:123], v[0:3], a[24:27]\n\t"
    "v_mfma_f32_16x16x32_bf16 a[56:59], v[120:123], v[4:7], a[56:59]\n\t"
    "v_mfma_f32_16x16x32_bf16 a[88:91], v[120:123], v[8:11], a[88:91]\n\t"
    "buffer_load_dwordx4 v[84:87], v237, s[16:19], s30 offen offset:0 sc1\n\t"
    "v_mfma_f32_16x16x32_bf16 a[120:123], v[120:123], v[12:15], a[120:123]\n\t"
    "v_mfma_f32_16x16x32_bf16 a[152:155], v[120:123], v[16:19], a[152:155]\n\t"
    "v_mfma_f32_16x16x32_bf16 a[184:187], v[120:123], v[20:23], a[184:187]\n\t"
    "buffer_load_dwordx4 v[88:91], v237, s[16:19], s30 offen offset:1024 sc1\n\t"
    "v_mfma_f32_16x16x32_bf16 a[28:31], v[124:127], v[0:3], a[28:31]\n\t"
    "v_mfma_f32_16x16x32_bf16 a[60:63], v[124:127], v[4:7], a[60:63]\n\t"
    "v_mfma_f32_16x16x32_bf16 a[92:95], v[124:127], v[8:11], a[92:95]\n\t"
    "buffer_load_dwordx4 v[92:95], v237, s[16:19], s30 offen offset:2048 sc1\n\t"
    "v_mfma_f32_16x16x32_bf16 a[124:127], v[124:127], v[12:15], a[124:127]\n\t"
    "v_mfma_f32_16x16x32_bf16 a[156:159], v[124:127], v[16:19], a[156:159]\n\t"
    "v_mfma_f32_16x16x32_bf16 a[188:191], v[124:127], v[20:23], a[188:191]\n\t"
    "s_waitcnt vmcnt(20) lgkmcnt(0)\n\t"
    "s_barrier\n\t"
    "v_mfma_f32_16x16x32_bf16 a[0:3], v[128:131], v[24:27], a[0:3]\n\t"
    "ds_read_b64_tr_b16 v[96:97], v238 offset:49152\n\t"
    "s_add_u32 m0, s31, 32768\n\t"
    "v_mfma_f32_16x16x32_bf16 a[32:35], v[128:131], v[28:31], a[32:35]\n\t"
    "v_mfma_f32_16x16x32_bf16 a[64:67], v[128:131], v[32:35], a[64:67]\n\t"
    "ds_read_b64_tr_b16 v[98:99], v238 offset:53248\n\t"
    "buffer_load_dwordx4 v232, s[12:15], s28 offen offset:0 lds\n\t"
    "v_mfma_f32_16x16x32_bf16 a[96:99], v[128:131], v[36:39], a[96:99]\n\t"
    "v_mfma_f32_16x16x32_bf16 a[128:131], v[128:131], v[40:43], a[128:131]\n\t"
    "ds_read_b64_tr_b16 v[100:101], v239 offset:49152\n\t"
    "buffer_load_dwordx4 v233, s[12:15], s28 offen offset:1024 lds\n\t"
    "v_mfma_f32_16x16x32_bf16 a[160:163], v[128:131], v[44:47], a[160:163]\n\t"
    "v_mfma_f32_16x16x32_bf16 a[4:7], v[132:135], v[24:27], a[4:7]\n\t"
    "ds_read_b64_tr_b16 v[102:103], v239 offset:53248\n\t"
    "v_mfma_f32_16x16x32_bf16 a[36:39], v[132:135], v[28:31], a[36:39]\n\t"
    "buffer_load_dwordx4 v234, s[12:15], s28 offen offset:2048 lds\n\t"
    "v_mfma_f32_16x16x32_bf16 a[68:71], v[132:135], v[32:35], a[68:71]\n\t"
    "v_mfma_f32_16x16x32_bf16 a[100:103], v[132:135], v[36:39], a[100:103]\n\t"
    "ds_read_b64_tr_b16 v[104:105], v238 offset:49408\n\t"
    "buffer_load_dwordx4 v235, s[12:15], s28 offen offset:3072 lds\n\t"
    "v_mfma_f32_16x16x32_bf16 a[132:135], v[132:135], v[40:43], a[132:135]\n\t"
    "v_mfma_f32_16x16x32_bf16 a[164:167], v[132:135], v[44:47], a[164:167]\n\t"
    "ds_read_b64_tr_b16 v[106:107], v238 offset:53504\n\t"
    "v_mfma_f32_16x16x32_bf16 a[8:11], v[136:139], v[24:27], a[8:11]\n\t"
    "s_add_u32 s28, s28, 65536\n\t"
    "v_mfma_f32_16x16x32_bf16 a[40:43], v[136:139], v[28:31], a[40:43]\n\t"
    "ds_read_b64_tr_b16 v[108:109], v239 offset:49408\n\t"
    "v_mfma_f32_16x16x32_bf16 a[72:75], v[136:139], v[32:35], a[72:75]\n\t"
    "s_cmp_eq_u32 s95, s29\n\t"
    "s_addc_u32 s95, s95, 0\n\t"
    "v_mfma_f32_16x16x32_bf16 a[104:107], v[136:139], v[36:39], a[104:107]\n\t"
    "v_mfma_f32_16x16x32_bf16 a[136:139], v[136:139], v[40:43], a[136:139]\n\t"
    "ds_read_b64_tr_b16 v[110:111], v239 offset:53504\n\t"
    "s_mul_i32 s70, s95, 49152\n\t"
    "v_mfma_f32_16x16x32_bf16 a[168:171], v[136:139], v[44:47], a[168:171]\n\t"
    "v_mfma_f32_16x16x32_bf16 a[12:15], v[140:143], v[24:27], a[12:15]\n\t"
    "ds_read_b64_tr_b16 v[112:113], v238 offset:49664\n\t"
    "v_mfma_f32_16x16x32_bf16 a[44:47], v[140:143], v[28:31], a[44:47]\n\t"
    "s_add_u32 s95, s95, 1\n\t"
    "v_mfma_f32_16x16x32_bf16 a[76:79], v[140:143], v[32:35], a[76:79]\n\t"
    "ds_read_b64_tr_b16 v[114:115], v238 offset:53760\n\t"
    "v_mfma_f32_16x16x32_bf16 a[108:111], v[140:143], v[36:39], a[108:111]\n\t"
    "s_cmp_eq_u32 s95, s29\n\t"
    "s_addc_u32 s95, s95, 0\n\t"
    "v_mfma_f32_16x16x32_bf16 a[140:143], v[140:143], v[40:43], a[140:143]\n\t"
    "v_mfma_f32_16x16x32_bf16 a[172:175], v[140:143], v[44:47], a[172:175]\n\t"
    "ds_read_b64_tr_b16 v[116:117], v239 offset:49664\n\t"
    "v_mfma_f32_16x16x32_bf16 a[16:19], v[144:147], v[24:27], a[16:19]\n\t"
    "s_mul_i32 s71, s95, 49152\n\t"
    "v_mfma_f32_16x16x32_bf16 a[48:51], v[144:147], v[28:31], a[48:51]\n\t"
    "ds_read_b64_tr_b16 v[118:119], v239 offset:53760\n\t"
    "v_mfma_f32_16x16x32_bf16 a[80:83], v[144:147], v[32:35], a[80:83]\n\t"
    "s_add_u32 s95, s95, 1\n\t"
    "v_mfma_f32_16x16x32_bf16 a[112:115], v[144:147], v[36:39], a[112:115]\n\t"
    "ds_read_b64_tr_b16 v[120:121], v238 offset:49920\n\t"
    "v_mfma_f32_16x16x32_bf16 a[144:147], v[144:147], v[40:43], a[144:147]\n\t"
    "s_add_u32 s30, s70, 0\n\t"
    "v_mfma_f32_16x16x32_bf16 a[176:179], v[144:147], v[44:47], a[176:179]\n\t"
    "v_mfma_f32_16x16x32_bf16 a[20:23], v[148:151], v[24:27], a[20:23]\n\t"
    "ds_read_b64_tr_b16 v[122:123], v238 offset:54016\n\t"
    "v_mfma_f32_16x16x32_bf16 a[52:55], v[148:151], v[28:31], a[52:55]\n\t"
    "buffer_load_dwordx4 v[0:3], v237, s[16:19], s30 offen offset:0 sc1\n\t"
    "v_mfma_f32_16x16x32_bf16 a[84:87], v[148:151], v[32:35], a[84:87]\n\t"
    "ds_read_b64_tr_b16 v[124:125], v239 offset:49920\n\t"
    "v_mfma_f32_16x16x32_bf16 a[116:119], v[148:151], v[36:39], a[116:119]\n\t"
    "buffer_load_dwordx4 v[4:7], v237, s[16:19], s30 offen offset:1024 sc1\n\t"
    "v_mfma_f32_16x16x32_bf16 a[148:151], v[148:151], v[40:43], a[148:151]\n\t"
    "ds_read_b64_tr_b16 v[126:127], v239 offset:54016\n\t"
    "v_mfma_f32_16x16x32_bf16 a[180:183], v[148:151], v[44:47], a[180:183]\n\t"
    "v_mfma_f32_16x16x32_bf16 a[24:27], v[152:155], v[24:27], a[24:27]\n\t"
    "buffer_load_dwordx4 v[8:11], v237, s[16:19], s30 offen offset:2048 sc1\n\t"
    "v_mfma_f32_16x16x32_bf16 a[56:59], v[152:155], v[28:31], a[56:59]\n\t"
    "v_mfma_f32_16x16x32_bf16 a[88:91], v[152:155], v[32:35], a[88:91]\n\t"
    "s_add_u32 s30, s70, 6144\n\t"
    "v_mfma_f32_16x16x32_bf16 a[120:123], v[152:155], v[36:39], a[120:123]\n\t"
    "v_mfma_f32_16x16x32_bf16 a[152:155], v[152:155], v[40:43], a[152:155]\n\t"
    "buffer_load_dwordx4 v[12:15], v237, s[16:19], s30 offen offset:0 sc1\n\t"
    "v_mfma_f32_16x16x32_bf16 a[184:187], v[152:155], v[44:47], a[184:187]\n\t"
    "v_mfma_f32_16x16x32_bf16 a[28:31], v[156:159], v[24:27], a[28:31]\n\t"
    "v_mfma_f32_16x16x32_bf16 a[60:63], v[156:159], v[28:31], a[60:63]\n\t"
    "buffer_load_dwordx4 v[16:19], v237, s[16:19], s30 offen offset:1024 sc1\n\t"
    "v_mfma_f32_16x16x32_bf16 a[92:95], v[156:159], v[32:35], a[92:95]\n\t"
    "v_mfma_f32_16x16x32_bf16 a[124:127], v[156:159], v[36:39], a[124:127]\n\t"
    "buffer_load_dwordx4 v[20:23], v237, s[16:19], s30 offen offset:2048 sc1\n\t"
    "v_mfma_f32_16x16x32_bf16 a[156:159], v[156:159], v[40:43], a[156:159]\n\t"
    "v_mfma_f32_16x16x32_bf16 a[188:191], v[156:159], v[44:47], a[188:191]\n\t"
    "s_waitcnt vmcnt(20) lgkmcnt(0)\n\t"
    "v_mfma_f32_16x16x32_bf16 a[0:3], v[96:99], v[48:51], a[0:3]\n\t"
    "ds_read_b64_tr_b16 v[128:129], v238 offset:57344\n\t"
    "s_add_u32 m0, s31, 40960\n\t"
    "v_mfma_f32_16x16x32_bf16 a[32:35], v[96:99], v[52:55], a[32:35]\n\t"
    "v_mfma_f32_16x16x32_bf16 a[64:67], v[96:99], v[56:59], a[64:67]\n\t"
    "ds_read_b64_tr_b16 v[130:131], v238 offset:61440\n\t"
    "v_mfma_f32_16x16x32_bf16 a[96:99], v[96:99], v[60:63], a[96:99]\n\t"
    "buffer_load_dwordx4 v232, s[12:15], s28 offen offset:0 lds\n\t"
    "v_mfma_f32_16x16x32_bf16 a[128:131], v[96:99], v[64:67], a[128:131]\n\t"
    "ds_read_b64_tr_b16 v[132:133], v239 offset:57344\n\t"
    "v_mfma_f32_16x16x32_bf16 a[160:163], v[96:99], v[68:71], a[160:163]\n\t"
    "v_mfma_f32_16x16x32_bf16 a[4:7], v[100:103], v[48:51], a[4:7]\n\t"
    "ds_read_b64_tr_b16 v[134:135], v239 offset:61440\n\t"
    "buffer_load_dwordx4 v233, s[12:15], s28 offen offset:1024 lds\n\t"
    "v_mfma_f32_16x16x32_bf16 a[36:39], v[100:103], v[52:55], a[36:39]\n\t"
    "v_mfma_f32_16x16x32_bf16 a[68:71], v[100:103], v[56:59], a[68:71]\n\t"
    "v_mfma_f32_16x16x32_bf16 a[100:103], v[100:103], v[60:63], a[100:103]\n\t"
    "ds_read_b64_tr_b16 v[136:137], v238 offset:57600\n\t"
    "v_mfma_f32_16x16x32_bf16 a[132:135], v[100:103], v[64:67], a[132:135]\n\t"
    "buffer_load_dwordx4 v234, s[12:15], s28 offen offset:2048 lds\n\t"
    "v_mfma_f32_16x16x32_bf16 a[164:167], v[100:103], v[68:71], a[164:167]\n\t"
    "ds_read_b64_tr_b16 v[138:139], v238 offset:61696\n\t"
    "v_mfma_f32_16x16x32_bf16 a[8:11], v[104:107], v[48:51], a[8:11]\n\t"
    "v_mfma_f32_16x16x32_bf16 a[40:43], v[104:107], v[52:55], a[40:43]\n\t"
    "ds_read_b64_tr_b16 v[140:141], v239 offset:57600\n\t"
    "buffer_load_dwordx4 v235, s[12:15], s28 offen offset:3072 lds\n\t"
    "v_mfma_f32_16x16x32_bf16 a[72:75], v[104:107], v[56:59], a[72:75]\n\t"
    "v_mfma_f32_16x16x32_bf16 a[104:107], v[104:107], v[60:63], a[104:107]\n\t"
    "v_mfma_f32_16x16x32_bf16 a[136:139], v[104:107], v[64:67], a[136:139]\n\t"
    "ds_read_b64_tr_b16 v[142:143], v239 offset:61696\n\t"
    "v_mfma_f32_16x16x32_bf16 a[168:171], v[104:107], v[68:71], a[168:171]\n\t"
    "s_add_u32 s28, s28, 65536\n\t"
    "v_mfma_f32_16x16x32_bf16 a[12:15], v[108:111], v[48:51], a[12:15]\n\t"
    "ds_read_b64_tr_b16 v[144:145], v238 offset:57856\n\t"
    "v_mfma_f32_16x16x32_bf16 a[44:47], v[108:111], v[52:55], a[44:47]\n\t"
    "v_mfma_f32_16x16x32_bf16 a[76:79], v[108:111], v[56:59], a[76:79]\n\t"
    "ds_read_b64_tr_b16 v[146:147], v238 offset:61952\n\t"
    "s_add_u32 s30, s70, 12288\n\t"
    "v_mfma_f32_16x16x32_bf16 a[108:111], v[108:111], v[60:63], a[108:111]\n\t"
    "v_mfma_f32_16x16x32_bf16 a[140:143], v[108:111], v[64:67], a[140:143]\n\t"
    "v_mfma_f32_16x16x32_bf16 a[172:175], v[108:111], v[68:71], a[172:175]\n\t"
    "ds_read_b64_tr_b16 v[148:149], v239 offset:57856\n\t"
    "v_mfma_f32_16x16x32_bf16 a[16:19], v[112:115], v[48:51], a[16:19]\n\t"
    "buffer_load_dwordx4 v[24:27], v237, s[16:19], s30 offen offset:0 sc1\n\t"
    "v_mfma_f32_16x16x32_bf16 a[48:51], v[112:115], v[52:55], a[48:51]\n\t"
    "ds_read_b64_tr_b16 v[150:151], v239 offset:61952\n\t"
    "v_mfma_f32_16x16x32_bf16 a[80:83], v[112:115], v[56:59], a[80:83]\n\t"
    "v_mfma_f32_16x16x32_bf16 a[112:115], v[112:115], v[60:63], a[112:115]\n\t"
    "ds_read_b64_tr_b16 v[152:153], v238 offset:58112\n\t"
    "buffer_load_dwordx4 v[28:31], v237, s[16:19], s30 offen offset:1024 sc1\n\t"
    "v_mfma_f32_16x16x32_bf16 a[144:147], v[112:115], v[64:67], a[144:147]\n\t"
    "v_mfma_f32_16x16x32_bf16 a[176:179], v[112:115], v[68:71], a[176:179]\n\t"
    "v_mfma_f32_16x16x32_bf16 a[20:23], v[116:119], v[48:51], a[20:23]\n\t"
    "ds_read_b64_tr_b16 v[154:155], v238 offset:62208\n\t"
    "buffer_load_dwordx4 v[32:35], v237, s[16:19], s30 offen offset:2048 sc1\n\t"
    "v_mfma_f32_16x16x32_bf16 a[52:55], v[116:119], v[52:55], a[52:55]\n\t"
    "v_mfma_f32_16x16x32_bf16 a[84:87], v[116:119], v[56:59], a[84:87]\n\t"
    "ds_read_b64_tr_b16 v[156:157], v239 offset:58112\n\t"
    "v_mfma_f32_16x16x32_bf16 a[116:119], v[116:119], v[60:63], a[116:119]\n\t"
    "v_mfma_f32_16x16x32_bf16 a[148:151], v[116:119], v[64:67], a[148:151]\n\t"
    "ds_read_b64_tr_b16 v[158:159], v239 offset:62208\n\t"
    "s_add_u32 s30, s70, 18432\n\t"
    "v_mfma_f32_16x16x32_bf16 a[180:183], v[116:119], v[68:71], a[180:183]\n\t"
    "v_mfma_f32_16x16x32_bf16 a[24:27], v[120:123], v[48:51], a[24:27]\n\t"
    "v_mfma_f32_16x16x32_bf16 a[56:59], v[120:123], v[52:55], a[56:59]\n\t"
    "buffer_load_dwordx4 v[36:39], v237, s[16:19], s30 offen offset:0 sc1\n\t"
    "v_mfma_f32_16x16x32_bf16 a[88:91], v[120:123], v[56:59], a[88:91]\n\t"
    "v_mfma_f32_16x16x32_bf16 a[120:123], v[120:123], v[60:63], a[120:123]\n\t"
    "v_mfma_f32_16x16x32_bf16 a[152:155], v[120:123], v[64:67], a[152:155]\n\t"
    "v_mfma_f32_16x16x32_bf16 a[184:187], v[120:123], v[68:71], a[184:187]\n\t"
    "buffer_load_dwordx4 v[40:43], v237, s[16:19], s30 offen offset:1024 sc1\n\t"
    "v_mfma_f32_16x16x32_bf16 a[28:31], v[124:127], v[48:51], a[28:31]\n\t"
    "v_mfma_f32_16x16x32_bf16 a[60:63], v[124:127], v[52:55], a[60:63]\n\t"
    "v_mfma_f32_16x16x32_bf16 a[92:95], v[124:127], v[56:59], a[92:95]\n\t"
    "buffer_load_dwordx4 v[44:47], v237, s[16:19], s30 offen offset:2048 sc1\n\t"
    "v_mfma_f32_16x16x32_bf16 a[124:127], v[124:127], v[60:63], a[124:127]\n\t"
    "v_mfma_f32_16x16x32_bf16 a[156:159], v[124:127], v[64:67], a[156:159]\n\t"
    "v_mfma_f32_16x16x32_bf16 a[188:191], v[124:127], v[68:71], a[188:191]\n\t"
    "s_waitcnt vmcnt(20) lgkmcnt(0)\n\t"
    "s_barrier\n\t"
    "v_mfma_f32_16x16x32_bf16 a[0:3], v[128:131], v[72:75], a[0:3]\n\t"
    "ds_read_b64_tr_b16 v[96:97], v238 offset:0\n\t"
    "s_add_u32 m0, s31, 49152\n\t"
    "v_mfma_f32_16x16x32_bf16 a[32:35], v[128:131], v[76:79], a[32:35]\n\t"
    "v_mfma_f32_16x16x32_bf16 a[64:67], v[128:131], v[80:83], a[64:67]\n\t"
    "ds_read_b64_tr_b16 v[98:99], v238 offset:4096\n\t"
    "v_mfma_f32_16x16x32_bf16 a[96:99], v[128:131], v[84:87], a[96:99]\n\t"
    "buffer_load_dwordx4 v232, s[12:15], s28 offen offset:0 lds\n\t"
    "v_mfma_f32_16x16x32_bf16 a[128:131], v[128:131], v[88:91], a[128:131]\n\t"
    "ds_read_b64_tr_b16 v[100:101], v239 offset:0\n\t"
    "v_mfma_f32_16x16x32_bf16 a[160:163], v[128:131], v[92:95], a[160:163]\n\t"
    "v_mfma_f32_16x16x32_bf16 a[4:7], v[132:135], v[72:75], a[4:7]\n\t"
    "ds_read_b64_tr_b16 v[102:103], v239 offset:4096\n\t"
    "buffer_load_dwordx4 v233, s[12:15], s28 offen offset:1024 lds\n\t"
    "v_mfma_f32_16x16x32_bf16 a[36:39], v[132:135], v[76:79], a[36:39]\n\t"
    "v_mfma_f32_16x16x32_bf16 a[68:71], v[132:135], v[80:83], a[68:71]\n\t"
    "v_mfma_f32_16x16x32_bf16 a[100:103], v[132:135], v[84:87], a[100:103]\n\t"
    "ds_read_b64_tr_b16 v[104:105], v238 offset:256\n\t"
    "v_mfma_f32_16x16x32_bf16 a[132:135], v[132:135], v[88:91], a[132:135]\n\t"
    "buffer_load_dwordx4 v234, s[12:15], s28 offen offset:2048 lds\n\t"
    "v_mfma_f32_16x16x32_bf16 a[164:167], v[132:135], v[92:95], a[164:167]\n\t"
    "ds_read_b64_tr_b16 v[106:107], v238 offset:4352\n\t"
    "v_mfma_f32_16x16x32_bf16 a[8:11], v[136:139], v[72:75], a[8:11]\n\t"
    "v_mfma_f32_16x16x32_bf16 a[40:43], v[136:139], v[76:79], a[40:43]\n\t"
    "ds_read_b64_tr_b16 v[108:109], v239 offset:256\n\t"
    "buffer_load_dwordx4 v235, s[12:15], s28 offen offset:3072 lds\n\t"
    "v_mfma_f32_16x16x32_bf16 a[72:75], v[136:139], v[80:83], a[72:75]\n\t"
    "v_mfma_f32_16x16x32_bf16 a[104:107], v[136:139], v[84:87], a[104:107]\n\t"
    "v_mfma_f32_16x16x32_bf16 a[136:139], v[136:139], v[88:91], a[136:139]\n\t"
    "ds_read_b64_tr_b16 v[110:111], v239 offset:4352\n\t"
    "v_mfma_f32_16x16x32_bf16 a[168:171], v[136:139], v[92:95], a[168:171]\n\t"
    "s_add_u32 s28, s28, 65536\n\t"
    "v_mfma_f32_16x16x32_bf16 a[12:15], v[140:143], v[72:75], a[12:15]\n\t"
    "ds_read_b64_tr_b16 v[112:113], v238 offset:512\n\t"
    "v_mfma_f32_16x16x32_bf16 a[44:47], v[140:143], v[76:79], a[44:47]\n\t"
    "v_mfma_f32_16x16x32_bf16 a[76:79], v[140:143], v[80:83], a[76:79]\n\t"
    "ds_read_b64_tr_b16 v[114:115], v238 offset:4608\n\t"
    "s_add_u32 s30, s71, 0\n\t"
    "v_mfma_f32_16x16x32_bf16 a[108:111], v[140:143], v[84:87], a[108:111]\n\t"
    "v_mfma_f32_16x16x32_bf16 a[140:143], v[140:143], v[88:91], a[140:143]\n\t"
    "v_mfma_f32_16x16x32_bf16 a[172:175], v[140:143], v[92:95], a[172:175]\n\t"
    "ds_read_b64_tr_b16 v[116:117], v239 offset:512\n\t"
    "v_mfma_f32_16x16x32_bf16 a[16:19], v[144:147], v[72:75], a[16:19]\n\t"
    "buffer_load_dwordx4 v[48:51], v237, s[16:19], s30 offen offset:0 sc1\n\t"
    "v_mfma_f32_16x16x32_bf16 a[48:51], v[144:147], v[76:79], a[48:51]\n\t"
    "ds_read_b64_tr_b16 v[118:119], v239 offset:4608\n\t"
    "v_mfma_f32_16x16x32_bf16 a[80:83], v[144:147], v[80:83], a[80:83]\n\t"
    "v_mfma_f32_16x16x32_bf16 a[112:115], v[144:147], v[84:87], a[112:115]\n\t"
    "ds_read_b64_tr_b16 v[120:121], v238 offset:768\n\t"
    "buffer_load_dwordx4 v[52:55], v237, s[16:19], s30 offen offset:1024 sc1\n\t"
    "v_mfma_f32_16x16x32_bf16 a[144:147], v[144:147], v[88:91], a[144:147]\n\t"
    "v_mfma_f32_16x16x32_bf16 a[176:179], v[144:147], v[92:95], a[176:179]\n\t"
    "v_mfma_f32_16x16x32_bf16 a[20:23], v[148:151], v[72:75], a[20:23]\n\t"
    "ds_read_b64_tr_b16 v[122:123], v238 offset:4864\n\t"
    "buffer_load_dwordx4 v[56:59], v237, s[16:19], s30 offen offset:2048 sc1\n\t"
    "v_mfma_f32_16x16x32_bf16 a[52:55], v[148:151], v[76:79], a[52:55]\n\t"
    "v_mfma_f32_16x16x32_bf16 a[84:87], v[148:151], v[80:83], a[84:87]\n\t"
    "ds_read_b64_tr_b16 v[124:125], v239 offset:768\n\t"
    "v_mfma_f32_16x16x32_bf16 a[116:119], v[148:151], v[84:87], a[116:119]\n\t"
    "v_mfma_f32_16x16x32_bf16 a[148:151], v[148:151], v[88:91], a[148:151]\n\t"
    "ds_read_b64_tr_b16 v[126:127], v239 offset:4864\n\t"
    "s_add_u32 s30, s71, 6144\n\t"
    "v_mfma_f32_16x16x32_bf16 a[180:183], v[148:151], v[92:95], a[180:183]\n\t"
    "v_mfma_f32_16x16x32_bf16 a[24:27], v[152:155], v[72:75], a[24:27]\n\t"
    "v_mfma_f32_16x16x32_bf16 a[56:59], v[152:155], v[76:79], a[56:59]\n\t"
    "buffer_load_dwordx4 v[60:63], v237, s[16:19], s30 offen offset:0 sc1\n\t"
    "v_mfma_f32_16x16x32_bf16 a[88:91], v[152:155], v[80:83], a[88:91]\n\t"
    "v_mfma_f32_16x16x32_bf16 a[120:123], v[152:155], v[84:87], a[120:123]\n\t"
    "v_mfma_f32_16x16x32_bf16 a[152:155], v[152:155], v[88:91], a[152:155]\n\t"
    "v_mfma_f32_16x16x32_bf16 a[184:187], v[152:155], v[92:95], a[184:187]\n\t"
    "buffer_load_dwordx4 v[64:67], v237, s[16:19], s30 offen offset:1024 sc1\n\t"
    "v_mfma_f32_16x16x32_bf16 a[28:31], v[156:159], v[72:75], a[28:31]\n\t"
    "v_mfma_f32_16x16x32_bf16 a[60:63], v[156:159], v[76:79], a[60:63]\n\t"
    "v_mfma_f32_16x16x32_bf16 a[92:95], v[156:159], v[80:83], a[92:95]\n\t"
    "buffer_load_dwordx4 v[68:71], v237, s[16:19], s30 offen offset:2048 sc1\n\t"
    "v_mfma_f32_16x16x32_bf16 a[124:127], v[156:159], v[84:87], a[124:127]\n\t"
    "v_mfma_f32_16x16x32_bf16 a[156:159], v[156:159], v[88:91], a[156:159]\n\t"
    "v_mfma_f32_16x16x32_bf16 a[188:191], v[156:159], v[92:95], a[188:191]\n\t"
    "s_waitcnt vmcnt(20) lgkmcnt(0)\n\t"
    "s_barrier\n\t"
    "v_mfma_f32_16x16x32_bf16 a[96:99], v[96:99], v[0:3], a[96:99]\n\t"
    "ds_read_b64_tr_b16 v[128:129], v238 offset:8192\n\t"
    "s_add_u32 m0, s31, 57344\n\t"
    "v_mfma_f32_16x16x32_bf16 a[128:131], v[96:99], v[4:7], a[128:131]\n\t"
    "ds_read_b64_tr_b16 v[130:131], v238 offset:12288\n\t"
    "v_mfma_f32_16x16x32_bf16 a[160:163], v[96:99], v[8:11], a[160:163]\n\t"
    "ds_read_b64_tr_b16 v[132:133], v239 offset:8192\n\t"
    "v_mfma_f32_16x16x32_bf16 a[100:103], v[100:103], v[0:3], a[100:103]\n\t"
    "ds_read_b64_tr_b16 v[134:135], v239 offset:12288\n\t"
    "buffer_load_dwordx4 v232, s[12:15], s28 offen offset:0 lds\n\t"
    "v_mfma_f32_16x16x32_bf16 a[132:135], v[100:103], v[4:7], a[132:135]\n\t"
    "ds_read_b64_tr_b16 v[136:137], v238 offset:8448\n\t"
    "v_mfma_f32_16x16x32_bf16 a[164:167], v[100:103], v[8:11], a[164:167]\n\t"
    "ds_read_b64_tr_b16 v[138:139], v238 offset:12544\n\t"
    "v_mfma_f32_16x16x32_bf16 a[104:107], v[104:107], v[0:3], a[104:107]\n\t"
    "ds_read_b64_tr_b16 v[140:141], v239 offset:8448\n\t"
    "buffer_load_dwordx4 v233, s[12:15], s28 offen offset:1024 lds\n\t"
    "v_mfma_f32_16x16x32_bf16 a[136:139], v[104:107], v[4:7], a[136:139]\n\t"
    "ds_read_b64_tr_b16 v[142:143], v239 offset:12544\n\t"
    "v_mfma_f32_16x16x32_bf16 a[168:171], v[104:107], v[8:11], a[168:171]\n\t"
    "ds_read_b64_tr_b16 v[144:145], v238 offset:8704\n\t"
    "v_mfma_f32_16x16x32_bf16 a[108:111], v[108:111], v[0:3], a[108:111]\n\t"
    "ds_read_b64_tr_b16 v[146:147], v238 offset:12800\n\t"
    "buffer_load_dwordx4 v234, s[12:15], s28 offen offset:2048 lds\n\t"
    "v_mfma_f32_16x16x32_bf16 a[140:143], v[108:111], v[4:7], a[140:143]\n\t"
    "ds_read_b64_tr_b16 v[148:149], v239 offset:8704\n\t"
    "v_mfma_f32_16x16x32_bf16 a[172:175], v[108:111], v[8:11], a[172:175]\n\t"
    "ds_read_b64_tr_b16 v[150:151], v239 offset:12800\n\t"
    "v_mfma_f32_16x16x32_bf16 a[112:115], v[112:115], v[0:3], a[112:115]\n\t"
    "ds_read_b64_tr_b16 v[152:153], v238 offset:8960\n\t"
    "buffer_load_dwordx4 v235, s[12:15], s28 offen offset:3072 lds\n\t"
    "v_mfma_f32_16x16x32_bf16 a[144:147], v[112:115], v[4:7], a[144:147]\n\t"
    "ds_read_b64_tr_b16 v[154:155], v238 offset:13056\n\t"
    "v_mfma_f32_16x16x32_bf16 a[176:179], v[112:115], v[8:11], a[176:179]\n\t"
    "ds_read_b64_tr_b16 v[156:157], v239 offset:8960\n\t"
    "v_mfma_f32_16x16x32_bf16 a[116:119], v[116:119], v[0:3], a[116:119]\n\t"
    "ds_read_b64_tr_b16 v[158:159], v239 offset:13056\n\t"
    "s_cmp_eq_u32 s94, s29\n\t"
    "s_addc_u32 s94, s94, 0\n\t"
    "v_mfma_f32_16x16x32_bf16 a[148:151], v[116:119], v[4:7], a[148:151]\n\t"
    "v_mfma_f32_16x16x32_bf16 a[180:183], v[116:119], v[8:11], a[180:183]\n\t"
    "v_mfma_f32_16x16x32_bf16 a[120:123], v[120:123], v[0:3], a[120:123]\n\t"
    "s_mul_i32 s28, s94, 524288\n\t"
    "v_mfma_f32_16x16x32_bf16 a[152:155], v[120:123], v[4:7], a[152:155]\n\t"
    "v_mfma_f32_16x16x32_bf16 a[184:187], v[120:123], v[8:11], a[184:187]\n\t"
    "v_mfma_f32_16x16x32_bf16 a[124:127], v[124:127], v[0:3], a[124:127]\n\t"
    "s_add_u32 s94, s94, 1\n\t"
    "v_mfma_f32_16x16x32_bf16 a[156:159], v[124:127], v[4:7], a[156:159]\n\t"
    "v_mfma_f32_16x16x32_bf16 a[188:191], v[124:127], v[8:11], a[188:191]\n\t"
    "s_waitcnt lgkmcnt(0)\n\t"
    "v_mfma_f32_16x16x32_bf16 a[96:99], v[128:131], v[12:15], a[96:99]\n\t"
    "ds_read_b64_tr_b16 v[96:97], v238 offset:16384\n\t"
    "s_add_u32 s30, s71, 12288\n\t"
    "v_mfma_f32_16x16x32_bf16 a[128:131], v[128:131], v[16:19], a[128:131]\n\t"
    "ds_read_b64_tr_b16 v[98:99], v238 offset:20480\n\t"
    "v_mfma_f32_16x16x32_bf16 a[160:163], v[128:131], v[20:23], a[160:163]\n\t"
    "ds_read_b64_tr_b16 v[100:101], v239 offset:16384\n\t"
    "v_mfma_f32_16x16x32_bf16 a[100:103], v[132:135], v[12:15], a[100:103]\n\t"
    "ds_read_b64_tr_b16 v[102:103], v239 offset:20480\n\t"
    "buffer_load_dwordx4 v[72:75], v237, s[16:19], s30 offen offset:0 sc1\n\t"
    "v_mfma_f32_16x16x32_bf16 a[132:135], v[132:135], v[16:19], a[132:135]\n\t"
    "ds_read_b64_tr_b16 v[104:105], v238 offset:16640\n\t"
    "v_mfma_f32_16x16x32_bf16 a[164:167], v[132:135], v[20:23], a[164:167]\n\t"
    "ds_read_b64_tr_b16 v[106:107], v238 offset:20736\n\t"
    "v_mfma_f32_16x16x32_bf16 a[104:107], v[136:139], v[12:15], a[104:107]\n\t"
    "ds_read_b64_tr_b16 v[108:109], v239 offset:16640\n\t"
    "buffer_load_dwordx4 v[76:79], v237, s[16:19], s30 offen offset:1024 sc1\n\t"
    "v_mfma_f32_16x16x32_bf16 a[136:139], v[136:139], v[16:19], a[136:139]\n\t"
    "ds_read_b64_tr_b16 v[110:111], v239 offset:20736\n\t"
    "v_mfma_f32_16x16x32_bf16 a[168:171], v[136:139], v[20:23], a[168:171]\n\t"
    "ds_read_b64_tr_b16 v[112:113], v238 offset:16896\n\t"
    "v_mfma_f32_16x16x32_bf16 a[108:111], v[140:143], v[12:15], a[108:111]\n\t"
    "ds_read_b64_tr_b16 v[114:115], v238 offset:20992\n\t"
    "buffer_load_dwordx4 v[80:83], v237, s[16:19], s30 offen offset:2048 sc1\n\t"
    "v_mfma_f32_16x16x32_bf16 a[140:143], v[140:143], v[16:19], a[140:143]\n\t"
    "ds_read_b64_tr_b16 v[116:117], v239 offset:16896\n\t"
    "v_mfma_f32_16x16x32_bf16 a[172:175], v[140:143], v[20:23], a[172:175]\n\t"
    "ds_read_b64_tr_b16 v[118:119], v239 offset:20992\n\t"
    "v_mfma_f32_16x16x32_bf16 a[112:115], v[144:147], v[12:15], a[112:115]\n\t"
    "ds_read_b64_tr_b16 v[120:121], v238 offset:17152\n\t"
    "s_add_u32 s30, s71, 18432\n\t"
    "v_mfma_f32_16x16x32_bf16 a[144:147], v[144:147], v[16:19], a[144:147]\n\t"
    "ds_read_b64_tr_b16 v[122:123], v238 offset:21248\n\t"
    "v_mfma_f32_16x16x32_bf16 a[176:179], v[144:147], v[20:23], a[176:179]\n\t"
    "ds_read_b64_tr_b16 v[124:125], v239 offset:17152\n\t"
    "v_mfma_f32_16x16x32_bf16 a[116:119], v[148:151], v[12:15], a[116:119]\n\t"
    "ds_read_b64_tr_b16 v[126:127], v239 offset:21248\n\t"
    "buffer_load_dwordx4 v[84:87], v237, s[16:19], s30 offen offset:0 sc1\n\t"
    "v_mfma_f32_16x16x32_bf16 a[148:151], v[148:151], v[16:19], a[148:151]\n\t"
    "v_mfma_f32_16x16x32_bf16 a[180:183], v[148:151], v[20:23], a[180:183]\n\t"
    "v_mfma_f32_16x16x32_bf16 a[120:123], v[152:155], v[12:15], a[120:123]\n\t"
    "buffer_load_dwordx4 v[88:91], v237, s[16:19], s30 offen offset:1024 sc1\n\t"
    "v_mfma_f32_16x16x32_bf16 a[152:155], v[152:155], v[16:19], a[152:155]\n\t"
    "v_mfma_f32_16x16x32_bf16 a[184:187], v[152:155], v[20:23], a[184:187]\n\t"
    "v_mfma_f32_16x16x32_bf16 a[124:127], v[156:159], v[12:15], a[124:127]\n\t"
    "buffer_load_dwordx4 v[92:95], v237, s[16:19], s30 offen offset:2048 sc1\n\t"
    "v_mfma_f32_16x16x32_bf16 a[156:159], v[156:159], v[16:19], a[156:159]\n\t"
    "v_mfma_f32_16x16x32_bf16 a[188:191], v[156:159], v[20:23], a[188:191]\n\t"
    "s_waitcnt vmcnt(20) lgkmcnt(0)\n\t"
    "s_barrier\n\t"
    "v_mfma_f32_16x16x32_bf16 a[96:99], v[96:99], v[24:27], a[96:99]\n\t"
    "ds_read_b64_tr_b16 v[128:129], v238 offset:24576\n\t"
    "s_add_u32 s30, s70, 0\n\t"
    "v_mfma_f32_16x16x32_bf16 a[128:131], v[96:99], v[28:31], a[128:131]\n\t"
    "ds_read_b64_tr_b16 v[130:131], v238 offset:28672\n\t"
    "v_mfma_f32_16x16x32_bf16 a[160:163], v[96:99], v[32:35], a[160:163]\n\t"
    "ds_read_b64_tr_b16 v[132:133], v239 offset:24576\n\t"
    "v_mfma_f32_16x16x32_bf16 a[100:103], v[100:103], v[24:27], a[100:103]\n\t"
    "ds_read_b64_tr_b16 v[134:135], v239 offset:28672\n\t"
    "v_mfma_f32_16x16x32_bf16 a[132:135], v[100:103], v[28:31], a[132:135]\n\t"
    "ds_read_b64_tr_b16 v[136:137], v238 offset:24832\n\t"
    "v_mfma_f32_16x16x32_bf16 a[164:167], v[100:103], v[32:35], a[164:167]\n\t"
    "ds_read_b64_tr_b16 v[138:139], v238 offset:28928\n\t"
    "v_mfma_f32_16x16x32_bf16 a[104:107], v[104:107], v[24:27], a[104:107]\n\t"
    "ds_read_b64_tr_b16 v[140:141], v239 offset:24832\n\t"
    "buffer_load_dwordx4 v[0:3], v236, s[16:19], s30 offen offset:0 sc1\n\t"
    "v_mfma_f32_16x16x32_bf16 a[136:139], v[104:107], v[28:31], a[136:139]\n\t"
    "ds_read_b64_tr_b16 v[142:143], v239 offset:28928\n\t"
    "v_mfma_f32_16x16x32_bf16 a[168:171], v[104:107], v[32:35], a[168:171]\n\t"
    "ds_read_b64_tr_b16 v[144:145], v238 offset:25088\n\t"
    "v_mfma_f32_16x16x32_bf16 a[108:111], v[108:111], v[24:27], a[108:111]\n\t"
    "ds_read_b64_tr_b16 v[146:147], v238 offset:29184\n\t"
    "v_mfma_f32_16x16x32_bf16 a[140:143], v[108:111], v[28:31], a[140:143]\n\t"
    "ds_read_b64_tr_b16 v[148:149], v239 offset:25088\n\t"
    "v_mfma_f32_16x16x32_bf16 a[172:175], v[108:111], v[32:35], a[172:175]\n\t"
    "ds_read_b64_tr_b16 v[150:151], v239 offset:29184\n\t"
    "v_mfma_f32_16x16x32_bf16 a[112:115], v[112:115], v[24:27], a[112:115]\n\t"
    "ds_read_b64_tr_b16 v[152:153], v238 offset:25344\n\t"
    "buffer_load_dwordx4 v[4:7], v236, s[16:19], s30 offen offset:1024 sc1\n\t"
    "v_mfma_f32_16x16x32_bf16 a[144:147], v[112:115], v[28:31], a[144:147]\n\t"
    "ds_read_b64_tr_b16 v[154:155], v238 offset:29440\n\t"
    "v_mfma_f32_16x16x32_bf16 a[176:179], v[112:115], v[32:35], a[176:179]\n\t"
    "ds_read_b64_tr_b16 v[156:157], v239 offset:25344\n\t"
    "v_mfma_f32_16x16x32_bf16 a[116:119], v[116:119], v[24:27], a[116:119]\n\t"
    "ds_read_b64_tr_b16 v[158:159], v239 offset:29440\n\t"
    "v_mfma_f32_16x16x32_bf16 a[148:151], v[116:119], v[28:31], a[148:151]\n\t"
    "v_mfma_f32_16x16x32_bf16 a[180:183], v[116:119], v[32:35], a[180:183]\n\t"
    "v_mfma_f32_16x16x32_bf16 a[120:123], v[120:123], v[24:27], a[120:123]\n\t"
    "buffer_load_dwordx4 v[8:11], v236, s[16:19], s30 offen offset:2048 sc1\n\t"
    "v_mfma_f32_16x16x32_bf16 a[152:155], v[120:123], v[28:31], a[152:155]\n\t"
    "v_mfma_f32_16x16x32_bf16 a[184:187], v[120:123], v[32:35], a[184:187]\n\t"
    "v_mfma_f32_16x16x32_bf16 a[124:127], v[124:127], v[24:27], a[124:127]\n\t"
    "v_mfma_f32_16x16x32_bf16 a[156:159], v[124:127], v[28:31], a[156:159]\n\t"
    "v_mfma_f32_16x16x32_bf16 a[188:191], v[124:127], v[32:35], a[188:191]\n\t"
    "s_waitcnt lgkmcnt(0)\n\t"
    "v_mfma_f32_16x16x32_bf16 a[96:99], v[128:131], v[36:39], a[96:99]\n\t"
    "ds_read_b64_tr_b16 v[96:97], v238 offset:32768\n\t"
    "s_add_u32 s30, s70, 6144\n\t"
    "v_mfma_f32_16x16x32_bf16 a[128:131], v[128:131], v[40:43], a[128:131]\n\t"
    "ds_read_b64_tr_b16 v[98:99], v238 offset:36864\n\t"
    "v_mfma_f32_16x16x32_bf16 a[160:163], v[128:131], v[44:47], a[160:163]\n\t"
    "ds_read_b64_tr_b16 v[100:101], v239 offset:32768\n\t"
    "v_mfma_f32_16x16x32_bf16 a[100:103], v[132:135], v[36:39], a[100:103]\n\t"
    "ds_read_b64_tr_b16 v[102:103], v239 offset:36864\n\t"
    "v_mfma_f32_16x16x32_bf16 a[132:135], v[132:135], v[40:43], a[132:135]\n\t"
    "ds_read_b64_tr_b16 v[104:105], v238 offset:33024\n\t"
    "v_mfma_f32_16x16x32_bf16 a[164:167], v[132:135], v[44:47], a[164:167]\n\t"
    "ds_read_b64_tr_b16 v[106:107], v238 offset:37120\n\t"
    "v_mfma_f32_16x16x32_bf16 a[104:107], v[136:139], v[36:39], a[104:107]\n\t"
    "ds_read_b64_tr_b16 v[108:109], v239 offset:33024\n\t"
    "buffer_load_dwordx4 v[12:15], v236, s[16:19], s30 offen offset:0 sc1\n\t"
    "v_mfma_f32_16x16x32_bf16 a[136:139], v[136:139], v[40:43], a[136:139]\n\t"
    "ds_read_b64_tr_b16 v[110:111], v239 offset:37120\n\t"
    "v_mfma_f32_16x16x32_bf16 a[168:171], v[136:139], v[44:47], a[168:171]\n\t"
    "ds_read_b64_tr_b16 v[112:113], v238 offset:33280\n\t"
    "v_mfma_f32_16x16x32_bf16 a[108:111], v[140:143], v[36:39], a[108:111]\n\t"
    "ds_read_b64_tr_b16 v[114:115], v238 offset:37376\n\t"
    "v_mfma_f32_16x16x32_bf16 a[140:143], v[140:143], v[40:43], a[140:143]\n\t"
    "ds_read_b64_tr_b16 v[116:117], v239 offset:33280\n\t"
    "v_mfma_f32_16x16x32_bf16 a[172:175], v[140:143], v[44:47], a[172:175]\n\t"
    "ds_read_b64_tr_b16 v[118:119], v239 offset:37376\n\t"
    "v_mfma_f32_16x16x32_bf16 a[112:115], v[144:147], v[36:39], a[112:115]\n\t"
    "ds_read_b64_tr_b16 v[120:121], v238 offset:33536\n\t"
    "buffer_load_dwordx4 v[16:19], v236, s[16:19], s30 offen offset:1024 sc1\n\t"
    "v_mfma_f32_16x16x32_bf16 a[144:147], v[144:147], v[40:43], a[144:147]\n\t"
    "ds_read_b64_tr_b16 v[122:123], v238 offset:37632\n\t"
    "v_mfma_f32_16x16x32_bf16 a[176:179], v[144:147], v[44:47], a[176:179]\n\t"
    "ds_read_b64_tr_b16 v[124:125], v239 offset:33536\n\t"
    "v_mfma_f32_16x16x32_bf16 a[116:119], v[148:151], v[36:39], a[116:119]\n\t"
    "ds_read_b64_tr_b16 v[126:127], v239 offset:37632\n\t"
    "v_mfma_f32_16x16x32_bf16 a[148:151], v[148:151], v[40:43], a[148:151]\n\t"
    "v_mfma_f32_16x16x32_bf16 a[180:183], v[148:151], v[44:47], a[180:183]\n\t"
    "v_mfma_f32_16x16x32_bf16 a[120:123], v[152:155], v[36:39], a[120:123]\n\t"
    "buffer_load_dwordx4 v[20:23], v236, s[16:19], s30 offen offset:2048 sc1\n\t"
    "v_mfma_f32_16x16x32_bf16 a[152:155], v[152:155], v[40:43], a[152:155]\n\t"
    "v_mfma_f32_16x16x32_bf16 a[184:187], v[152:155], v[44:47], a[184:187]\n\t"
    "v_mfma_f32_16x16x32_bf16 a[124:127], v[156:159], v[36:39], a[124:127]\n\t"
    "v_mfma_f32_16x16x32_bf16 a[156:159], v[156:159], v[40:43], a[156:159]\n\t"
    "v_mfma_f32_16x16x32_bf16 a[188:191], v[156:159], v[44:47], a[188:191]\n\t"
    "s_waitcnt vmcnt(16) lgkmcnt(0)\n\t"
    "s_barrier\n\t"
    "v_mfma_f32_16x16x32_bf16 a[96:99], v[96:99], v[48:51], a[96:99]\n\t"
    "ds_read_b64_tr_b16 v[128:129], v238 offset:40960\n\t"
    "s_add_u32 s30, s70, 12288\n\t"
    "v_mfma_f32_16x16x32_bf16 a[128:131], v[96:99], v[52:55], a[128:131]\n\t"
    "ds_read_b64_tr_b16 v[130:131], v238 offset:45056\n\t"
    "v_mfma_f32_16x16x32_bf16 a[160:163], v[96:99], v[56:59], a[160:163]\n\t"
    "ds_read_b64_tr_b16 v[132:133], v239 offset:40960\n\t"
    "v_mfma_f32_16x16x32_bf16 a[100:103], v[100:103], v[48:51], a[100:103]\n\t"
    "ds_read_b64_tr_b16 v[134:135], v239 offset:45056\n\t"
    "v_mfma_f32_16x16x32_bf16 a[132:135], v[100:103], v[52:55], a[132:135]\n\t"
    "ds_read_b64_tr_b16 v[136:137], v238 offset:41216\n\t"
    "v_mfma_f32_16x16x32_bf16 a[164:167], v[100:103], v[56:59], a[164:167]\n\t"
    "ds_read_b64_tr_b16 v[138:139], v238 offset:45312\n\t"
    "v_mfma_f32_16x16x32_bf16 a[104:107], v[104:107], v[48:51], a[104:107]\n\t"
    "ds_read_b64_tr_b16 v[140:141], v239 offset:41216\n\t"
    "buffer_load_dwordx4 v[24:27], v236, s[16:19], s30 offen offset:0 sc1\n\t"
    "v_mfma_f32_16x16x32_bf16 a[136:139], v[104:107], v[52:55], a[136:139]\n\t"
    "ds_read_b64_tr_b16 v[142:143], v239 offset:45312\n\t"
    "v_mfma_f32_16x16x32_bf16 a[168:171], v[104:107], v[56:59], a[168:171]\n\t"
    "ds_read_b64_tr_b16 v[144:145], v238 offset:41472\n\t"
    "v_mfma_f32_16x16x32_bf16 a[108:111], v[108:111], v[48:51], a[108:111]\n\t"
    "ds_read_b64_tr_b16 v[146:147], v238 offset:45568\n\t"
    "v_mfma_f32_16x16x32_bf16 a[140:143], v[108:111], v[52:55], a[140:143]\n\t"
    "ds_read_b64_tr_b16 v[148:149], v239 offset:41472\n\t"
    "v_mfma_f32_16x16x32_bf16 a[172:175], v[108:111], v[56:59], a[172:175]\n\t"
    "ds_read_b64_tr_b16 v[150:151], v239 offset:45568\n\t"
    "v_mfma_f32_16x16x32_bf16 a[112:115], v[112:115], v[48:51], a[112:115]\n\t"
    "ds_read_b64_tr_b16 v[152:153], v238 offset:41728\n\t"
    "buffer_load_dwordx4 v[28:31], v236, s[16:19], s30 offen offset:1024 sc1\n\t"
    "v_mfma_f32_16x16x32_bf16 a[144:147], v[112:115], v[52:55], a[144:147]\n\t"
    "ds_read_b64_tr_b16 v[154:155], v238 offset:45824\n\t"
    "v_mfma_f32_16x16x32_bf16 a[176:179], v[112:115], v[56:59], a[176:179]\n\t"
    "ds_read_b64_tr_b16 v[156:157], v239 offset:41728\n\t"
    "v_mfma_f32_16x16x32_bf16 a[116:119], v[116:119], v[48:51], a[116:119]\n\t"
    "ds_read_b64_tr_b16 v[158:159], v239 offset:45824\n\t"
    "v_mfma_f32_16x16x32_bf16 a[148:151], v[116:119], v[52:55], a[148:151]\n\t"
    "v_mfma_f32_16x16x32_bf16 a[180:183], v[116:119], v[56:59], a[180:183]\n\t"
    "v_mfma_f32_16x16x32_bf16 a[120:123], v[120:123], v[48:51], a[120:123]\n\t"
    "buffer_load_dwordx4 v[32:35], v236, s[16:19], s30 offen offset:2048 sc1\n\t"
    "v_mfma_f32_16x16x32_bf16 a[152:155], v[120:123], v[52:55], a[152:155]\n\t"
    "v_mfma_f32_16x16x32_bf16 a[184:187], v[120:123], v[56:59], a[184:187]\n\t"
    "v_mfma_f32_16x16x32_bf16 a[124:127], v[124:127], v[48:51], a[124:127]\n\t"
    "v_mfma_f32_16x16x32_bf16 a[156:159], v[124:127], v[52:55], a[156:159]\n\t"
    "v_mfma_f32_16x16x32_bf16 a[188:191], v[124:127], v[56:59], a[188:191]\n\t"
    "s_waitcnt lgkmcnt(0)\n\t"
    "v_mfma_f32_16x16x32_bf16 a[96:99], v[128:131], v[60:63], a[96:99]\n\t"
    "ds_read_b64_tr_b16 v[96:97], v238 offset:49152\n\t"
    "s_add_u32 s30, s70, 18432\n\t"
    "v_mfma_f32_16x16x32_bf16 a[128:131], v[128:131], v[64:67], a[128:131]\n\t"
    "ds_read_b64_tr_b16 v[98:99], v238 offset:53248\n\t"
    "v_mfma_f32_16x16x32_bf16 a[160:163], v[128:131], v[68:71], a[160:163]\n\t"
    "ds_read_b64_tr_b16 v[100:101], v239 offset:49152\n\t"
    "v_mfma_f32_16x16x32_bf16 a[100:103], v[132:135], v[60:63], a[100:103]\n\t"
    "ds_read_b64_tr_b16 v[102:103], v239 offset:53248\n\t"
    "v_mfma_f32_16x16x32_bf16 a[132:135], v[132:135], v[64:67], a[132:135]\n\t"
    "ds_read_b64_tr_b16 v[104:105], v238 offset:49408\n\t"
    "v_mfma_f32_16x16x32_bf16 a[164:167], v[132:135], v[68:71], a[164:167]\n\t"
    "ds_read_b64_tr_b16 v[106:107], v238 offset:53504\n\t"
    "v_mfma_f32_16x16x32_bf16 a[104:107], v[136:139], v[60:63], a[104:107]\n\t"
    "ds_read_b64_tr_b16 v[108:109], v239 offset:49408\n\t"
    "buffer_load_dwordx4 v[36:39], v236, s[16:19], s30 offen offset:0 sc1\n\t"
    "v_mfma_f32_16x16x32_bf16 a[136:139], v[136:139], v[64:67], a[136:139]\n\t"
    "ds_read_b64_tr_b16 v[110:111], v239 offset:53504\n\t"
    "v_mfma_f32_16x16x32_bf16 a[168:171], v[136:139], v[68:71], a[168:171]\n\t"
    "ds_read_b64_tr_b16 v[112:113], v238 offset:49664\n\t"
    "v_mfma_f32_16x16x32_bf16 a[108:111], v[140:143], v[60:63], a[108:111]\n\t"
    "ds_read_b64_tr_b16 v[114:115], v238 offset:53760\n\t"
    "v_mfma_f32_16x16x32_bf16 a[140:143], v[140:143], v[64:67], a[140:143]\n\t"
    "ds_read_b64_tr_b16 v[116:117], v239 offset:49664\n\t"
    "v_mfma_f32_16x16x32_bf16 a[172:175], v[140:143], v[68:71], a[172:175]\n\t"
    "ds_read_b64_tr_b16 v[118:119], v239 offset:53760\n\t"
    "v_mfma_f32_16x16x32_bf16 a[112:115], v[144:147], v[60:63], a[112:115]\n\t"
    "ds_read_b64_tr_b16 v[120:121], v238 offset:49920\n\t"
    "buffer_load_dwordx4 v[40:43], v236, s[16:19], s30 offen offset:1024 sc1\n\t"
    "v_mfma_f32_16x16x32_bf16 a[144:147], v[144:147], v[64:67], a[144:147]\n\t"
    "ds_read_b64_tr_b16 v[122:123], v238 offset:54016\n\t"
    "v_mfma_f32_16x16x32_bf16 a[176:179], v[144:147], v[68:71], a[176:179]\n\t"
    "ds_read_b64_tr_b16 v[124:125], v239 offset:49920\n\t"
    "v_mfma_f32_16x16x32_bf16 a[116:119], v[148:151], v[60:63], a[116:119]\n\t"
    "ds_read_b64_tr_b16 v[126:127], v239 offset:54016\n\t"
    "v_mfma_f32_16x16x32_bf16 a[148:151], v[148:151], v[64:67], a[148:151]\n\t"
    "v_mfma_f32_16x16x32_bf16 a[180:183], v[148:151], v[68:71], a[180:183]\n\t"
    "v_mfma_f32_16x16x32_bf16 a[120:123], v[152:155], v[60:63], a[120:123]\n\t"
    "buffer_load_dwordx4 v[44:47], v236, s[16:19], s30 offen offset:2048 sc1\n\t"
    "v_mfma_f32_16x16x32_bf16 a[152:155], v[152:155], v[64:67], a[152:155]\n\t"
    "v_mfma_f32_16x16x32_bf16 a[184:187], v[152:155], v[68:71], a[184:187]\n\t"
    "v_mfma_f32_16x16x32_bf16 a[124:127], v[156:159], v[60:63], a[124:127]\n\t"
    "v_mfma_f32_16x16x32_bf16 a[156:159], v[156:159], v[64:67], a[156:159]\n\t"
    "v_mfma_f32_16x16x32_bf16 a[188:191], v[156:159], v[68:71], a[188:191]\n\t"
    "s_waitcnt vmcnt(12) lgkmcnt(0)\n\t"
    "s_barrier\n\t"
    "v_mfma_f32_16x16x32_bf16 a[96:99], v[96:99], v[72:75], a[96:99]\n\t"
    "ds_read_b64_tr_b16 v[128:129], v238 offset:57344\n\t"
    "s_add_u32 s30, s71, 0\n\t"
    "v_mfma_f32_16x16x32_bf16 a[128:131], v[96:99], v[76:79], a[128:131]\n\t"
    "ds_read_b64_tr_b16 v[130:131], v238 offset:61440\n\t"
    "v_mfma_f32_16x16x32_bf16 a[160:163], v[96:99], v[80:83], a[160:163]\n\t"
    "ds_read_b64_tr_b16 v[132:133], v239 offset:57344\n\t"
    "v_mfma_f32_16x16x32_bf16 a[100:103], v[100:103], v[72:75], a[100:103]\n\t"
    "ds_read_b64_tr_b16 v[134:135], v239 offset:61440\n\t"
    "v_mfma_f32_16x16x32_bf16 a[132:135], v[100:103], v[76:79], a[132:135]\n\t"
    "ds_read_b64_tr_b16 v[136:137], v238 offset:57600\n\t"
    "v_mfma_f32_16x16x32_bf16 a[164:167], v[100:103], v[80:83], a[164:167]\n\t"
    "ds_read_b64_tr_b16 v[138:139], v238 offset:61696\n\t"
    "v_mfma_f32_16x16x32_bf16 a[104:107], v[104:107], v[72:75], a[104:107]\n\t"
    "ds_read_b64_tr_b16 v[140:141], v239 offset:57600\n\t"
    "buffer_load_dwordx4 v[48:51], v236, s[16:19], s30 offen offset:0 sc1\n\t"
    "v_mfma_f32_16x16x32_bf16 a[136:139], v[104:107], v[76:79], a[136:139]\n\t"
    "ds_read_b64_tr_b16 v[142:143], v239 offset:61696\n\t"
    "v_mfma_f32_16x16x32_bf16 a[168:171], v[104:107], v[80:83], a[168:171]\n\t"
    "ds_read_b64_tr_b16 v[144:145], v238 offset:57856\n\t"
    "v_mfma_f32_16x16x32_bf16 a[108:111], v[108:111], v[72:75], a[108:111]\n\t"
    "ds_read_b64_tr_b16 v[146:147], v238 offset:61952\n\t"
    "v_mfma_f32_16x16x32_bf16 a[140:143], v[108:111], v[76:79], a[140:143]\n\t"
    "ds_read_b64_tr_b16 v[148:149], v239 offset:57856\n\t"
    "v_mfma_f32_16x16x32_bf16 a[172:175], v[108:111], v[80:83], a[172:175]\n\t"
    "ds_read_b64_tr_b16 v[150:151], v239 offset:61952\n\t"
    "v_mfma_f32_16x16x32_bf16 a[112:115], v[112:115], v[72:75], a[112:115]\n\t"
    "ds_read_b64_tr_b16 v[152:153], v238 offset:58112\n\t"
    "buffer_load_dwordx4 v[52:55], v236, s[16:19], s30 offen offset:1024 sc1\n\t"
    "v_mfma_f32_16x16x32_bf16 a[144:147], v[112:115], v[76:79], a[144:147]\n\t"
    "ds_read_b64_tr_b16 v[154:155], v238 offset:62208\n\t"
    "v_mfma_f32_16x16x32_bf16 a[176:179], v[112:115], v[80:83], a[176:179]\n\t"
    "ds_read_b64_tr_b16 v[156:157], v239 offset:58112\n\t"
    "v_mfma_f32_16x16x32_bf16 a[116:119], v[116:119], v[72:75], a[116:119]\n\t"
    "ds_read_b64_tr_b16 v[158:159], v239 offset:62208\n\t"
    "v_mfma_f32_16x16x32_bf16 a[148:151], v[116:119], v[76:79], a[148:151]\n\t"
    "v_mfma_f32_16x16x32_bf16 a[180:183], v[116:119], v[80:83], a[180:183]\n\t"
    "v_mfma_f32_16x16x32_bf16 a[120:123], v[120:123], v[72:75], a[120:123]\n\t"
    "buffer_load_dwordx4 v[56:59], v236, s[16:19], s30 offen offset:2048 sc1\n\t"
    "v_mfma_f32_16x16x32_bf16 a[152:155], v[120:123], v[76:79], a[152:155]\n\t"
    "v_mfma_f32_16x16x32_bf16 a[184:187], v[120:123], v[80:83], a[184:187]\n\t"
    "v_mfma_f32_16x16x32_bf16 a[124:127], v[124:127], v[72:75], a[124:127]\n\t"
    "v_mfma_f32_16x16x32_bf16 a[156:159], v[124:127], v[76:79], a[156:159]\n\t"
    "v_mfma_f32_16x16x32_bf16 a[188:191], v[124:127], v[80:83], a[188:191]\n\t"
    "s_waitcnt lgkmcnt(0)\n\t"
    "v_mfma_f32_16x16x32_bf16 a[112:115], v[144:147], v[84:87], a[112:115]\n\t"
    "ds_read_b64_tr_b16 v[96:97], v238 offset:0\n\t"
    "s_add_u32 s30, s71, 6144\n\t"
    "v_mfma_f32_16x16x32_bf16 a[116:119], v[148:151], v[84:87], a[116:119]\n\t"
    "ds_read_b64_tr_b16 v[98:99], v238 offset:4096\n\t"
    "v_mfma_f32_16x16x32_bf16 a[120:123], v[152:155], v[84:87], a[120:123]\n\t"
    "ds_read_b64_tr_b16 v[100:101], v239 offset:0\n\t"
    "v_mfma_f32_16x16x32_bf16 a[124:127], v[156:159], v[84:87], a[124:127]\n\t"
    "ds_read_b64_tr_b16 v[102:103], v239 offset:4096\n\t"
    "v_mfma_f32_16x16x32_bf16 a[144:147], v[144:147], v[88:91], a[144:147]\n\t"
    "ds_read_b64_tr_b16 v[104:105], v238 offset:256\n\t"
    "v_mfma_f32_16x16x32_bf16 a[148:151], v[148:151], v[88:91], a[148:151]\n\t"
    "ds_read_b64_tr_b16 v[106:107], v238 offset:4352\n\t"
    "v_mfma_f32_16x16x32_bf16 a[152:155], v[152:155], v[88:91], a[152:155]\n\t"
    "ds_read_b64_tr_b16 v[108:109], v239 offset:256\n\t"
    "ds_write_b128 v240, a[112:115] offset:0\n\t"
    "buffer_load_dwordx4 v[60:63], v236, s[16:19], s30 offen offset:0 sc1\n\t"
    "v_mfma_f32_16x16x32_bf16 a[156:159], v[156:159], v[88:91], a[156:159]\n\t"
    "ds_read_b64_tr_b16 v[110:111], v239 offset:4352\n\t"
    "v_mfma_f32_16x16x32_bf16 a[176:179], v[144:147], v[92:95], a[176:179]\n\t"
    "ds_read_b64_tr_b16 v[112:113], v238 offset:512\n\t"
    "ds_write_b128 v240, a[116:119] offset:1024\n\t"
    "v_mfma_f32_16x16x32_bf16 a[180:183], v[148:151], v[92:95], a[180:183]\n\t"
    "ds_read_b64_tr_b16 v[114:115], v238 offset:4608\n\t"
    "v_mfma_f32_16x16x32_bf16 a[184:187], v[152:155], v[92:95], a[184:187]\n\t"
    "ds_read_b64_tr_b16 v[116:117], v239 offset:512\n\t"
    "ds_write_b128 v240, a[120:123] offset:2048\n\t"
    "v_mfma_f32_16x16x32_bf16 a[188:191], v[156:159], v[92:95], a[188:191]\n\t"
    "ds_read_b64_tr_b16 v[118:119], v239 offset:4608\n\t"
    "v_mfma_f32_16x16x32_bf16 a[96:99], v[128:131], v[84:87], a[96:99]\n\t"
    "ds_read_b64_tr_b16 v[120:121], v238 offset:768\n\t"
    "ds_write_b128 v240, a[124:127] offset:3072\n\t"
    "buffer_load_dwordx4 v[64:67], v236, s[16:19], s30 offen offset:1024 sc1\n\t"
    "v_mfma_f32_16x16x32_bf16 a[100:103], v[132:135], v[84:87], a[100:103]\n\t"
    "ds_read_b64_tr_b16 v[122:123], v238 offset:4864\n\t"
    "v_mfma_f32_16x16x32_bf16 a[104:107], v[136:139], v[84:87], a[104:107]\n\t"
    "ds_read_b64_tr_b16 v[124:125], v239 offset:768\n\t"
    "ds_write_b128 v240, a[144:147] offset:16384\n\t"
    "v_mfma_f32_16x16x32_bf16 a[108:111], v[140:143], v[84:87], a[108:111]\n\t"
    "ds_read_b64_tr_b16 v[126:127], v239 offset:4864\n\t"
    "v_mfma_f32_16x16x32_bf16 a[128:131], v[128:131], v[88:91], a[128:131]\n\t"
    "ds_write_b128 v240, a[148:151] offset:17408\n\t"
    "v_mfma_f32_16x16x32_bf16 a[132:135], v[132:135], v[88:91], a[132:135]\n\t"
    "v_mfma_f32_16x16x32_bf16 a[136:139], v[136:139], v[88:91], a[136:139]\n\t"
    "ds_write_b128 v240, a[152:155] offset:18432\n\t"
    "buffer_load_dwordx4 v[68:71], v236, s[16:19], s30 offen offset:2048 sc1\n\t"
    "v_mfma_f32_16x16x32_bf16 a[140:143], v[140:143], v[88:91], a[140:143]\n\t"
    "v_mfma_f32_16x16x32_bf16 a[160:163], v[128:131], v[92:95], a[160:163]\n\t"
    "ds_write_b128 v240, a[156:159] offset:19456\n\t"
    "v_mfma_f32_16x16x32_bf16 a[164:167], v[132:135], v[92:95], a[164:167]\n\t"
    "v_mfma_f32_16x16x32_bf16 a[168:171], v[136:139], v[92:95], a[168:171]\n\t"
    "v_mfma_f32_16x16x32_bf16 a[172:175], v[140:143], v[92:95], a[172:175]\n\t"
    "s_waitcnt vmcnt(12) lgkmcnt(0)\n\t"
    "s_barrier\n\t"
    "v_mfma_f32_16x16x32_bf16 a[0:3], v[96:99], v[0:3], a[0:3]\n\t"
    "ds_read_b64_tr_b16 v[128:129], v238 offset:8192\n\t"
    "s_add_u32 s30, s71, 12288\n\t"
    "ds_read_b128 v[160:163], v241 offset:0\n\t"
    "ds_read_b128 v[164:167], v241 offset:1024\n\t"
    "v_mfma_f32_16x16x32_bf16 a[32:35], v[96:99], v[4:7], a[32:35]\n\t"
    "ds_read_b64_tr_b16 v[130:131], v238 offset:12288\n\t"
    "ds_read_b128 v[168:171], v241 offset:2048\n\t"
    "v_mfma_f32_16x16x32_bf16 a[64:67], v[96:99], v[8:11], a[64:67]\n\t"
    "ds_read_b64_tr_b16 v[132:133], v239 offset:8192\n\t"
    "ds_read_b128 v[172:175], v241 offset:3072\n\t"
    "v_mfma_f32_16x16x32_bf16 a[4:7], v[100:103], v[0:3], a[4:7]\n\t"
    "ds_read_b64_tr_b16 v[134:135], v239 offset:12288\n\t"
    "v_accvgpr_read_b32 v252, a96\n\t"
    "v_mfma_f32_16x16x32_bf16 a[36:39], v[100:103], v[4:7], a[36:39]\n\t"
    "ds_read_b64_tr_b16 v[136:137], v238 offset:8448\n\t"
    "v_accvgpr_read_b32 v253, a97\n\t"
    "v_mfma_f32_16x16x32_bf16 a[68:71], v[100:103], v[8:11], a[68:71]\n\t"
    "ds_read_b64_tr_b16 v[138:139], v238 offset:12544\n\t"
    "v_accvgpr_read_b32 v254, a98\n\t"
    "v_mfma_f32_16x16x32_bf16 a[8:11], v[104:107], v[0:3], a[8:11]\n\t"
    "ds_read_b64_tr_b16 v[140:141], v239 offset:8448\n\t"
    "buffer_load_dwordx4 v[72:75], v236, s[16:19], s30 offen offset:0 sc1\n\t"
    "v_accvgpr_read_b32 v255, a99\n\t"
    "s_waitcnt lgkmcnt(9)\n\t"
    "v_pk_add_f32 v[160:161], v[160:161], v[252:253]\n\t"
    "v_mfma_f32_16x16x32_bf16 a[40:43], v[104:107], v[4:7], a[40:43]\n\t"
    "ds_read_b64_tr_b16 v[142:143], v239 offset:12544\n\t"
    "v_pk_add_f32 v[162:163], v[162:163], v[254:255]\n\t"
    "v_mfma_f32_16x16x32_bf16 a[72:75], v[104:107], v[8:11], a[72:75]\n\t"
    "ds_read_b64_tr_b16 v[144:145], v238 offset:8704\n\t"
    "v_mfma_f32_16x16x32_bf16 a[12:15], v[108:111], v[0:3], a[12:15]\n\t"
    "s_add_u32 s72, s59, 0\n\t"
    "buffer_store_dwordx4 v[160:163], v216, s[24:27], s72 offen offset:0 sc1\n\t"
    "ds_read_b64_tr_b16 v[146:147], v238 offset:12800\n\t"
    "v_accvgpr_read_b32 v252, a100\n\t"
    "v_mfma_f32_16x16x32_bf16 a[44:47], v[108:111], v[4:7], a[44:47]\n\t"
    "ds_read_b64_tr_b16 v[148:149], v239 offset:8704\n\t"
    "v_accvgpr_read_b32 v253, a101\n\t"
    "v_mfma_f32_16x16x32_bf16 a[76:79], v[108:111], v[8:11], a[76:79]\n\t"
    "ds_read_b64_tr_b16 v[150:151], v239 offset:12800\n\t"
    "v_accvgpr_read_b32 v254, a102\n\t"
    "v_mfma_f32_16x16x32_bf16 a[16:19], v[112:115], v[0:3], a[16:19]\n\t"
    "ds_read_b64_tr_b16 v[152:153], v238 offset:8960\n\t"
    "buffer_load_dwordx4 v[76:79], v236, s[16:19], s30 offen offset:1024 sc1\n\t"
    "v_accvgpr_read_b32 v255, a103\n\t"
    "v_mfma_f32_16x16x32_bf16 a[48:51], v[112:115], v[4:7], a[48:51]\n\t"
    "ds_read_b64_tr_b16 v[154:155], v238 offset:13056\n\t"
    "s_waitcnt lgkmcnt(14)\n\t"
    "v_pk_add_f32 v[164:165], v[164:165], v[252:253]\n\t"
    "v_pk_add_f32 v[166:167], v[166:167], v[254:255]\n\t"
    "v_mfma_f32_16x16x32_bf16 a[80:83], v[112:115], v[8:11], a[80:83]\n\t"
    "ds_read_b64_tr_b16 v[156:157], v239 offset:8960\n\t"
    "v_mfma_f32_16x16x32_bf16 a[20:23], v[116:119], v[0:3], a[20:23]\n\t"
    "ds_read_b64_tr_b16 v[158:159], v239 offset:13056\n\t"
    "v_accvgpr_read_b32 v252, a104\n\t"
    "v_mfma_f32_16x16x32_bf16 a[52:55], v[116:119], v[4:7], a[52:55]\n\t"
    "v_accvgpr_read_b32 v253, a105\n\t"
    "v_mfma_f32_16x16x32_bf16 a[84:87], v[116:119], v[8:11], a[84:87]\n\t"
    "v_accvgpr_read_b32 v254, a106\n\t"
    "v_mfma_f32_16x16x32_bf16 a[24:27], v[120:123], v[0:3], a[24:27]\n\t"
    "s_add_u32 s72, s59, 0\n\t"
    "buffer_store_dwordx4 v[164:167], v216, s[24:27], s72 offen offset:1024 sc1\n\t"
    "buffer_load_dwordx4 v[80:83], v236, s[16:19], s30 offen offset:2048 sc1\n\t"
    "v_accvgpr_read_b32 v255, a107\n\t"
    "v_mfma_f32_16x16x32_bf16 a[56:59], v[120:123], v[4:7], a[56:59]\n\t"
    "s_waitcnt lgkmcnt(14)\n\t"
    "v_pk_add_f32 v[168:169], v[168:169], v[252:253]\n\t"
    "v_mfma_f32_16x16x32_bf16 a[88:91], v[120:123], v[8:11], a[88:91]\n\t"
    "v_pk_add_f32 v[170:171], v[170:171], v[254:255]\n\t"
    "v_mfma_f32_16x16x32_bf16 a[28:31], v[124:127], v[0:3], a[28:31]\n\t"
    "v_accvgpr_read_b32 v252, a108\n\t"
    "v_mfma_f32_16x16x32_bf16 a[60:63], v[124:127], v[4:7], a[60:63]\n\t"
    "v_accvgpr_read_b32 v253, a109\n\t"
    "v_mfma_f32_16x16x32_bf16 a[92:95], v[124:127], v[8:11], a[92:95]\n\t"
    "s_waitcnt lgkmcnt(0)\n\t"
    "v_accvgpr_read_b32 v254, a110\n\t"
    "v_mfma_f32_16x16x32_bf16 a[0:3], v[128:131], v[12:15], a[0:3]\n\t"
    "ds_read_b64_tr_b16 v[96:97], v238 offset:16384\n\t"
    "s_add_u32 s30, s71, 18432\n\t"
    "v_accvgpr_read_b32 v255, a111\n\t"
    "v_mfma_f32_16x16x32_bf16 a[32:35], v[128:131], v[16:19], a[32:35]\n\t"
    "ds_read_b64_tr_b16 v[98:99], v238 offset:20480\n\t"
    "v_pk_add_f32 v[172:173], v[172:173], v[252:253]\n\t"
    "v_mfma_f32_16x16x32_bf16 a[64:67], v[128:131], v[20:23], a[64:67]\n\t"
    "ds_read_b64_tr_b16 v[100:101], v239 offset:16384\n\t"
    "v_pk_add_f32 v[174:175], v[174:175], v[254:255]\n\t"
    "v_mfma_f32_16x16x32_bf16 a[4:7], v[132:135], v[12:15], a[4:7]\n\t"
    "ds_read_b64_tr_b16 v[102:103], v239 offset:20480\n\t"
    "s_add_u32 s72, s59, 0\n\t"
    "buffer_store_dwordx4 v[168:171], v216, s[24:27], s72 offen offset:2048 sc1\n\t"
    "ds_write_b128 v204, a[176:179] offset:0\n\t"
    "v_mfma_f32_16x16x32_bf16 a[36:39], v[132:135], v[16:19], a[36:39]\n\t"
    "ds_read_b64_tr_b16 v[104:105], v238 offset:16640\n\t"
    "ds_write_b128 v204, a[180:183] offset:1024\n\t"
    "v_mfma_f32_16x16x32_bf16 a[68:71], v[132:135], v[20:23], a[68:71]\n\t"
    "ds_read_b64_tr_b16 v[106:107], v238 offset:20736\n\t"
    "ds_write_b128 v204, a[184:187] offset:2048\n\t"
    "v_mfma_f32_16x16x32_bf16 a[8:11], v[136:139], v[12:15], a[8:11]\n\t"
    "ds_read_b64_tr_b16 v[108:109], v239 offset:16640\n\t"
    "buffer_load_dwordx4 v[84:87], v236, s[16:19], s30 offen offset:0 sc1\n\t"
    "ds_write_b128 v204, a[188:191] offset:3072\n\t"
    "v_mfma_f32_16x16x32_bf16 a[40:43], v[136:139], v[16:19], a[40:43]\n\t"
    "ds_read_b64_tr_b16 v[110:111], v239 offset:20736\n\t"
    "ds_read_b128 v[176:179], v241 offset:16384\n\t"
    "v_mfma_f32_16x16x32_bf16 a[72:75], v[136:139], v[20:23], a[72:75]\n\t"
    "ds_read_b64_tr_b16 v[112:113], v238 offset:16896\n\t"
    "ds_read_b128 v[180:183], v241 offset:17408\n\t"
    "v_mfma_f32_16x16x32_bf16 a[12:15], v[140:143], v[12:15], a[12:15]\n\t"
    "ds_read_b64_tr_b16 v[114:115], v238 offset:20992\n\t"
    "ds_read_b128 v[184:187], v241 offset:18432\n\t"
    "ds_read_b128 v[188:191], v241 offset:19456\n\t"
    "v_mfma_f32_16x16x32_bf16 a[44:47], v[140:143], v[16:19], a[44:47]\n\t"
    "ds_read_b64_tr_b16 v[116:117], v239 offset:16896\n\t"
    "v_accvgpr_read_b32 v252, a128\n\t"
    "v_mfma_f32_16x16x32_bf16 a[76:79], v[140:143], v[20:23], a[76:79]\n\t"
    "ds_read_b64_tr_b16 v[118:119], v239 offset:20992\n\t"
    "v_accvgpr_read_b32 v253, a129\n\t"
    "v_mfma_f32_16x16x32_bf16 a[16:19], v[144:147], v[12:15], a[16:19]\n\t"
    "ds_read_b64_tr_b16 v[120:121], v238 offset:17152\n\t"
    "s_add_u32 s72, s59, 0\n\t"
    "buffer_store_dwordx4 v[172:175], v216, s[24:27], s72 offen offset:3072 sc1\n\t"
    "buffer_load_dwordx4 v[88:91], v236, s[16:19], s30 offen offset:1024 sc1\n\t"
    "v_accvgpr_read_b32 v254, a130\n\t"
    "v_mfma_f32_16x16x32_bf16 a[48:51], v[144:147], v[16:19], a[48:51]\n\t"
    "ds_read_b64_tr_b16 v[122:123], v238 offset:21248\n\t"
    "v_accvgpr_read_b32 v255, a131\n\t"
    "v_mfma_f32_16x16x32_bf16 a[80:83], v[144:147], v[20:23], a[80:83]\n\t"
    "ds_read_b64_tr_b16 v[124:125], v239 offset:17152\n\t"
    "s_waitcnt lgkmcnt(10)\n\t"
    "v_pk_add_f32 v[176:177], v[176:177], v[252:253]\n\t"
    "v_mfma_f32_16x16x32_bf16 a[20:23], v[148:151], v[12:15], a[20:23]\n\t"
    "ds_read_b64_tr_b16 v[126:127], v239 offset:21248\n\t"
    "v_pk_add_f32 v[178:179], v[178:179], v[254:255]\n\t"
    "v_mfma_f32_16x16x32_bf16 a[52:55], v[148:151], v[16:19], a[52:55]\n\t"
    "v_accvgpr_read_b32 v252, a132\n\t"
    "v_mfma_f32_16x16x32_bf16 a[84:87], v[148:151], v[20:23], a[84:87]\n\t"
    "v_accvgpr_read_b32 v253, a133\n\t"
    "v_mfma_f32_16x16x32_bf16 a[24:27], v[152:155], v[12:15], a[24:27]\n\t"
    "buffer_load_dwordx4 v[92:95], v236, s[16:19], s30 offen offset:2048 sc1\n\t"
    "v_accvgpr_read_b32 v254, a134\n\t"
    "v_mfma_f32_16x16x32_bf16 a[56:59], v[152:155], v[16:19], a[56:59]\n\t"
    "v_accvgpr_read_b32 v255, a135\n\t"
    "v_mfma_f32_16x16x32_bf16 a[88:91], v[152:155], v[20:23], a[88:91]\n\t"
    "s_waitcnt lgkmcnt(9)\n\t"
    "v_pk_add_f32 v[180:181], v[180:181], v[252:253]\n\t"
    "v_mfma_f32_16x16x32_bf16 a[28:31], v[156:159], v[12:15], a[28:31]\n\t"
    "v_pk_add_f32 v[182:183], v[182:183], v[254:255]\n\t"
    "v_mfma_f32_16x16x32_bf16 a[60:63], v[156:159], v[16:19], a[60:63]\n\t"
    "s_add_u32 s72, s59, 4096\n\t"
    "buffer_store_dwordx4 v[176:179], v216, s[24:27], s72 offen offset:0 sc1\n\t"
    "v_mfma_f32_16x16x32_bf16 a[92:95], v[156:159], v[20:23], a[92:95]\n\t"
    "s_waitcnt vmcnt(17) lgkmcnt(0)\n\t"
    "s_barrier\n\t"
    "v_mfma_f32_16x16x32_bf16 a[0:3], v[96:99], v[24:27], a[0:3]\n\t"
    "ds_read_b64_tr_b16 v[128:129], v238 offset:24576\n\t"
    "ds_read_b128 v[192:195], v205 offset:0\n\t"
    "v_mfma_f32_16x16x32_bf16 a[32:35], v[96:99], v[28:31], a[32:35]\n\t"
    "ds_read_b64_tr_b16 v[130:131], v238 offset:28672\n\t"
    "ds_read_b128 v[196:199], v205 offset:1024\n\t"
    "v_mfma_f32_16x16x32_bf16 a[64:67], v[96:99], v[32:35], a[64:67]\n\t"
    "ds_read_b64_tr_b16 v[132:133], v239 offset:24576\n\t"
    "ds_read_b128 v[200:203], v205 offset:2048\n\t"
    "v_mfma_f32_16x16x32_bf16 a[4:7], v[100:103], v[24:27], a[4:7]\n\t"
    "ds_read_b64_tr_b16 v[134:135], v239 offset:28672\n\t"
    "ds_read_b128 v[248:251], v205 offset:3072\n\t"
    "v_mfma_f32_16x16x32_bf16 a[36:39], v[100:103], v[28:31], a[36:39]\n\t"
    "ds_read_b64_tr_b16 v[136:137], v238 offset:24832\n\t"
    "v_accvgpr_read_b32 v252, a136\n\t"
    "v_mfma_f32_16x16x32_bf16 a[68:71], v[100:103], v[32:35], a[68:71]\n\t"
    "ds_read_b64_tr_b16 v[138:139], v238 offset:28928\n\t"
    "v_accvgpr_read_b32 v253, a137\n\t"
    "v_mfma_f32_16x16x32_bf16 a[8:11], v[104:107], v[24:27], a[8:11]\n\t"
    "ds_read_b64_tr_b16 v[140:141], v239 offset:24832\n\t"
    "v_accvgpr_read_b32 v254, a138\n\t"
    "v_mfma_f32_16x16x32_bf16 a[40:43], v[104:107], v[28:31], a[40:43]\n\t"
    "ds_read_b64_tr_b16 v[142:143], v239 offset:28928\n\t"
    "v_accvgpr_read_b32 v255, a139\n\t"
    "v_mfma_f32_16x16x32_bf16 a[72:75], v[104:107], v[32:35], a[72:75]\n\t"
    "s_add_u32 s72, s59, 4096\n\t"
    "buffer_store_dwordx4 v[180:183], v216, s[24:27], s72 offen offset:1024 sc1\n\t"
    "ds_read_b64_tr_b16 v[144:145], v238 offset:25088\n\t"
    "v_pk_add_f32 v[184:185], v[184:185], v[252:253]\n\t"
    "v_mfma_f32_16x16x32_bf16 a[12:15], v[108:111], v[24:27], a[12:15]\n\t"
    "ds_read_b64_tr_b16 v[146:147], v238 offset:29184\n\t"
    "v_pk_add_f32 v[186:187], v[186:187], v[254:255]\n\t"
    "v_mfma_f32_16x16x32_bf16 a[44:47], v[108:111], v[28:31], a[44:47]\n\t"
    "ds_read_b64_tr_b16 v[148:149], v239 offset:25088\n\t"
    "v_mfma_f32_16x16x32_bf16 a[76:79], v[108:111], v[32:35], a[76:79]\n\t"
    "ds_read_b64_tr_b16 v[150:151], v239 offset:29184\n\t"
    "v_accvgpr_read_b32 v252, a140\n\t"
    "v_mfma_f32_16x16x32_bf16 a[16:19], v[112:115], v[24:27], a[16:19]\n\t"
    "ds_read_b64_tr_b16 v[152:153], v238 offset:25344\n\t"
    "v_accvgpr_read_b32 v253, a141\n\t"
    "v_mfma_f32_16x16x32_bf16 a[48:51], v[112:115], v[28:31], a[48:51]\n\t"
    "ds_read_b64_tr_b16 v[154:155], v238 offset:29440\n\t"
    "v_accvgpr_read_b32 v254, a142\n\t"
    "v_mfma_f32_16x16x32_bf16 a[80:83], v[112:115], v[32:35], a[80:83]\n\t"
    "ds_read_b64_tr_b16 v[156:157], v239 offset:25344\n\t"
    "v_accvgpr_read_b32 v255, a143\n\t"
    "v_mfma_f32_16x16x32_bf16 a[20:23], v[116:119], v[24:27], a[20:23]\n\t"
    "ds_read_b64_tr_b16 v[158:159], v239 offset:29440\n\t"
    "v_pk_add_f32 v[188:189], v[188:189], v[252:253]\n\t"
    "v_mfma_f32_16x16x32_bf16 a[52:55], v[116:119], v[28:31], a[52:55]\n\t"
    "v_pk_add_f32 v[190:191], v[190:191], v[254:255]\n\t"
    "v_mfma_f32_16x16x32_bf16 a[84:87], v[116:119], v[32:35], a[84:87]\n\t"
    "v_mfma_f32_16x16x32_bf16 a[24:27], v[120:123], v[24:27], a[24:27]\n\t"
    "s_add_u32 s72, s59, 4096\n\t"
    "buffer_store_dwordx4 v[184:187], v216, s[24:27], s72 offen offset:2048 sc1\n\t"
    "v_accvgpr_read_b32 v252, a160\n\t"
    "v_mfma_f32_16x16x32_bf16 a[56:59], v[120:123], v[28:31], a[56:59]\n\t"
    "v_accvgpr_read_b32 v253, a161\n\t"
    "v_mfma_f32_16x16x32_bf16 a[88:91], v[120:123], v[32:35], a[88:91]\n\t"
    "v_accvgpr_read_b32 v254, a162\n\t"
    "v_mfma_f32_16x16x32_bf16 a[28:31], v[124:127], v[24:27], a[28:31]\n\t"
    "v_accvgpr_read_b32 v255, a163\n\t"
    "v_mfma_f32_16x16x32_bf16 a[60:63], v[124:127], v[28:31], a[60:63]\n\t"
    "s_waitcnt lgkmcnt(14)\n\t"
    "v_pk_add_f32 v[192:193], v[192:193], v[252:253]\n\t"
    "v_mfma_f32_16x16x32_bf16 a[92:95], v[124:127], v[32:35], a[92:95]\n\t"
    "s_waitcnt lgkmcnt(0)\n\t"
    "v_pk_add_f32 v[194:195], v[194:195], v[254:255]\n\t"
    "v_mfma_f32_16x16x32_bf16 a[0:3], v[128:131], v[36:39], a[0:3]\n\t"
    "ds_read_b64_tr_b16 v[96:97], v238 offset:32768\n\t"
    "v_mfma_f32_16x16x32_bf16 a[32:35], v[128:131], v[40:43], a[32:35]\n\t"
    "ds_read_b64_tr_b16 v[98:99], v238 offset:36864\n\t"
    "v_accvgpr_read_b32 v252, a164\n\t"
    "v_mfma_f32_16x16x32_bf16 a[64:67], v[128:131], v[44:47], a[64:67]\n\t"
    "ds_read_b64_tr_b16 v[100:101], v239 offset:32768\n\t"
    "v_accvgpr_read_b32 v253, a165\n\t"
    "v_mfma_f32_16x16x32_bf16 a[4:7], v[132:135], v[36:39], a[4:7]\n\t"
    "ds_read_b64_tr_b16 v[102:103], v239 offset:36864\n\t"
    "v_accvgpr_read_b32 v254, a166\n\t"
    "v_mfma_f32_16x16x32_bf16 a[36:39], v[132:135], v[40:43], a[36:39]\n\t"
    "s_add_u32 s72, s59, 4096\n\t"
    "buffer_store_dwordx4 v[188:191], v216, s[24:27], s72 offen offset:3072 sc1\n\t"
    "ds_read_b64_tr_b16 v[104:105], v238 offset:33024\n\t"
    "v_accvgpr_read_b32 v255, a167\n\t"
    "v_mfma_f32_16x16x32_bf16 a[68:71], v[132:135], v[44:47], a[68:71]\n\t"
    "ds_read_b64_tr_b16 v[106:107], v238 offset:37120\n\t"
    "v_pk_add_f32 v[196:197], v[196:197], v[252:253]\n\t"
    "v_mfma_f32_16x16x32_bf16 a[8:11], v[136:139], v[36:39], a[8:11]\n\t"
    "ds_read_b64_tr_b16 v[108:109], v239 offset:33024\n\t"
    "v_pk_add_f32 v[198:199], v[198:199], v[254:255]\n\t"
    "v_mfma_f32_16x16x32_bf16 a[40:43], v[136:139], v[40:43], a[40:43]\n\t"
    "ds_read_b64_tr_b16 v[110:111], v239 offset:37120\n\t"
    "v_mfma_f32_16x16x32_bf16 a[72:75], v[136:139], v[44:47], a[72:75]\n\t"
    "ds_read_b64_tr_b16 v[112:113], v238 offset:33280\n\t"
    "v_accvgpr_read_b32 v252, a168\n\t"
    "v_mfma_f32_16x16x32_bf16 a[12:15], v[140:143], v[36:39], a[12:15]\n\t"
    "ds_read_b64_tr_b16 v[114:115], v238 offset:37376\n\t"
    "v_accvgpr_read_b32 v253, a169\n\t"
    "v_mfma_f32_16x16x32_bf16 a[44:47], v[140:143], v[40:43], a[44:47]\n\t"
    "ds_read_b64_tr_b16 v[116:117], v239 offset:33280\n\t"
    "v_accvgpr_read_b32 v254, a170\n\t"
    "v_mfma_f32_16x16x32_bf16 a[76:79], v[140:143], v[44:47], a[76:79]\n\t"
    "ds_read_b64_tr_b16 v[118:119], v239 offset:37376\n\t"
    "v_accvgpr_read_b32 v255, a171\n\t"
    "v_mfma_f32_16x16x32_bf16 a[16:19], v[144:147], v[36:39], a[16:19]\n\t"
    "ds_read_b64_tr_b16 v[120:121], v238 offset:33536\n\t"
    "v_pk_add_f32 v[200:201], v[200:201], v[252:253]\n\t"
    "v_mfma_f32_16x16x32_bf16 a[48:51], v[144:147], v[40:43], a[48:51]\n\t"
    "ds_read_b64_tr_b16 v[122:123], v238 offset:37632\n\t"
    "v_pk_add_f32 v[202:203], v[202:203], v[254:255]\n\t"
    "s_add_u32 s72, s59, 8192\n\t"
    "buffer_store_dwordx4 v[192:195], v216, s[24:27], s72 offen offset:0 sc1\n\t"
    "v_mfma_f32_16x16x32_bf16 a[80:83], v[144:147], v[44:47], a[80:83]\n\t"
    "ds_read_b64_tr_b16 v[124:125], v239 offset:33536\n\t"
    "v_mfma_f32_16x16x32_bf16 a[20:23], v[148:151], v[36:39], a[20:23]\n\t"
    "ds_read_b64_tr_b16 v[126:127], v239 offset:37632\n\t"
    "v_accvgpr_read_b32 v252, a172\n\t"
    "v_mfma_f32_16x16x32_bf16 a[52:55], v[148:151], v[40:43], a[52:55]\n\t"
    "v_accvgpr_read_b32 v253, a173\n\t"
    "v_mfma_f32_16x16x32_bf16 a[84:87], v[148:151], v[44:47], a[84:87]\n\t"
    "v_accvgpr_read_b32 v254, a174\n\t"
    "v_mfma_f32_16x16x32_bf16 a[24:27], v[152:155], v[36:39], a[24:27]\n\t"
    "v_accvgpr_read_b32 v255, a175\n\t"
    "v_mfma_f32_16x16x32_bf16 a[56:59], v[152:155], v[40:43], a[56:59]\n\t"
    "v_pk_add_f32 v[248:249], v[248:249], v[252:253]\n\t"
    "v_mfma_f32_16x16x32_bf16 a[88:91], v[152:155], v[44:47], a[88:91]\n\t"
    "v_pk_add_f32 v[250:251], v[250:251], v[254:255]\n\t"
    "v_mfma_f32_16x16x32_bf16 a[28:31], v[156:159], v[36:39], a[28:31]\n\t"
    "v_mfma_f32_16x16x32_bf16 a[60:63], v[156:159], v[40:43], a[60:63]\n\t"
    "v_mfma_f32_16x16x32_bf16 a[92:95], v[156:159], v[44:47], a[92:95]\n\t"
    "s_add_u32 s72, s59, 8192\n\t"
    "buffer_store_dwordx4 v[196:199], v216, s[24:27], s72 offen offset:1024 sc1\n\t"
    "s_waitcnt vmcnt(16) lgkmcnt(0)\n\t"
    "v_mfma_f32_16x16x32_bf16 a[0:3], v[96:99], v[48:51], a[0:3]\n\t"
    "ds_read_b64_tr_b16 v[128:129], v238 offset:40960\n\t"
    "v_mfma_f32_16x16x32_bf16 a[32:35], v[96:99], v[52:55], a[32:35]\n\t"
    "ds_read_b64_tr_b16 v[130:131], v238 offset:45056\n\t"
    "v_mfma_f32_16x16x32_bf16 a[64:67], v[96:99], v[56:59], a[64:67]\n\t"
    "ds_read_b64_tr_b16 v[132:133], v239 offset:40960\n\t"
    "v_mfma_f32_16x16x32_bf16 a[4:7], v[100:103], v[48:51], a[4:7]\n\t"
    "ds_read_b64_tr_b16 v[134:135], v239 offset:45056\n\t"
    "v_mfma_f32_16x16x32_bf16 a[36:39], v[100:103], v[52:55], a[36:39]\n\t"
    "ds_read_b64_tr_b16 v[136:137], v238 offset:41216\n\t"
    "v_mfma_f32_16x16x32_bf16 a[68:71], v[100:103], v[56:59], a[68:71]\n\t"
    "ds_read_b64_tr_b16 v[138:139], v238 offset:45312\n\t"
    "v_mfma_f32_16x16x32_bf16 a[8:11], v[104:107], v[48:51], a[8:11]\n\t"
    "ds_read_b64_tr_b16 v[140:141], v239 offset:41216\n\t"
    "v_mfma_f32_16x16x32_bf16 a[40:43], v[104:107], v[52:55], a[40:43]\n\t"
    "ds_read_b64_tr_b16 v[142:143], v239 offset:45312\n\t"
    "v_mfma_f32_16x16x32_bf16 a[72:75], v[104:107], v[56:59], a[72:75]\n\t"
    "ds_read_b64_tr_b16 v[144:145], v238 offset:41472\n\t"
    "v_mfma_f32_16x16x32_bf16 a[12:15], v[108:111], v[48:51], a[12:15]\n\t"
    "s_add_u32 s72, s59, 8192\n\t"
    "buffer_store_dwordx4 v[200:203], v216, s[24:27], s72 offen offset:2048 sc1\n\t"
    "ds_read_b64_tr_b16 v[146:147], v238 offset:45568\n\t"
    "v_mfma_f32_16x16x32_bf16 a[44:47], v[108:111], v[52:55], a[44:47]\n\t"
    "ds_read_b64_tr_b16 v[148:149], v239 offset:41472\n\t"
    "v_mfma_f32_16x16x32_bf16 a[76:79], v[108:111], v[56:59], a[76:79]\n\t"
    "ds_read_b64_tr_b16 v[150:151], v239 offset:45568\n\t"
    "v_mfma_f32_16x16x32_bf16 a[16:19], v[112:115], v[48:51], a[16:19]\n\t"
    "ds_read_b64_tr_b16 v[152:153], v238 offset:41728\n\t"
    "v_mfma_f32_16x16x32_bf16 a[48:51], v[112:115], v[52:55], a[48:51]\n\t"
    "ds_read_b64_tr_b16 v[154:155], v238 offset:45824\n\t"
    "v_mfma_f32_16x16x32_bf16 a[80:83], v[112:115], v[56:59], a[80:83]\n\t"
    "ds_read_b64_tr_b16 v[156:157], v239 offset:41728\n\t"
    "v_mfma_f32_16x16x32_bf16 a[20:23], v[116:119], v[48:51], a[20:23]\n\t"
    "ds_read_b64_tr_b16 v[158:159], v239 offset:45824\n\t"
    "v_mfma_f32_16x16x32_bf16 a[52:55], v[116:119], v[52:55], a[52:55]\n\t"
    "v_mfma_f32_16x16x32_bf16 a[84:87], v[116:119], v[56:59], a[84:87]\n\t"
    "v_mfma_f32_16x16x32_bf16 a[24:27], v[120:123], v[48:51], a[24:27]\n\t"
    "v_mfma_f32_16x16x32_bf16 a[56:59], v[120:123], v[52:55], a[56:59]\n\t"
    "s_add_u32 s72, s59, 8192\n\t"
    "buffer_store_dwordx4 v[248:251], v216, s[24:27], s72 offen offset:3072 sc1\n\t"
    "v_mfma_f32_16x16x32_bf16 a[88:91], v[120:123], v[56:59], a[88:91]\n\t"
    "v_mfma_f32_16x16x32_bf16 a[28:31], v[124:127], v[48:51], a[28:31]\n\t"
    "v_mfma_f32_16x16x32_bf16 a[60:63], v[124:127], v[52:55], a[60:63]\n\t"
    "v_mfma_f32_16x16x32_bf16 a[92:95], v[124:127], v[56:59], a[92:95]\n\t"
    "s_waitcnt lgkmcnt(0)\n\t"
    "v_mfma_f32_16x16x32_bf16 a[0:3], v[128:131], v[60:63], a[0:3]\n\t"
    "ds_read_b64_tr_b16 v[96:97], v238 offset:49152\n\t"
    "v_mfma_f32_16x16x32_bf16 a[32:35], v[128:131], v[64:67], a[32:35]\n\t"
    "ds_read_b64_tr_b16 v[98:99], v238 offset:53248\n\t"
    "v_mfma_f32_16x16x32_bf16 a[64:67], v[128:131], v[68:71], a[64:67]\n\t"
    "ds_read_b64_tr_b16 v[100:101], v239 offset:49152\n\t"
    "v_mfma_f32_16x16x32_bf16 a[4:7], v[132:135], v[60:63], a[4:7]\n\t"
    "ds_read_b64_tr_b16 v[102:103], v239 offset:53248\n\t"
    "v_mfma_f32_16x16x32_bf16 a[36:39], v[132:135], v[64:67], a[36:39]\n\t"
    "ds_read_b64_tr_b16 v[104:105], v238 offset:49408\n\t"
    "v_mfma_f32_16x16x32_bf16 a[68:71], v[132:135], v[68:71], a[68:71]\n\t"
    "ds_read_b64_tr_b16 v[106:107], v238 offset:53504\n\t"
    "v_mfma_f32_16x16x32_bf16 a[8:11], v[136:139], v[60:63], a[8:11]\n\t"
    "ds_read_b64_tr_b16 v[108:109], v239 offset:49408\n\t"
    "v_mfma_f32_16x16x32_bf16 a[40:43], v[136:139], v[64:67], a[40:43]\n\t"
    "ds_read_b64_tr_b16 v[110:111], v239 offset:53504\n\t"
    "v_mfma_f32_16x16x32_bf16 a[72:75], v[136:139], v[68:71], a[72:75]\n\t"
    "ds_read_b64_tr_b16 v[112:113], v238 offset:49664\n\t"
    "v_mfma_f32_16x16x32_bf16 a[12:15], v[140:143], v[60:63], a[12:15]\n\t"
    "ds_read_b64_tr_b16 v[114:115], v238 offset:53760\n\t"
    "v_mfma_f32_16x16x32_bf16 a[44:47], v[140:143], v[64:67], a[44:47]\n\t"
    "ds_read_b64_tr_b16 v[116:117], v239 offset:49664\n\t"
    "v_mfma_f32_16x16x32_bf16 a[76:79], v[140:143], v[68:71], a[76:79]\n\t"
    "ds_read_b64_tr_b16 v[118:119], v239 offset:53760\n\t"
    "v_mfma_f32_16x16x32_bf16 a[16:19], v[144:147], v[60:63], a[16:19]\n\t"
    "ds_read_b64_tr_b16 v[120:121], v238 offset:49920\n\t"
    "v_mfma_f32_16x16x32_bf16 a[48:51], v[144:147], v[64:67], a[48:51]\n\t"
    "ds_read_b64_tr_b16 v[122:123], v238 offset:54016\n\t"
    "v_mfma_f32_16x16x32_bf16 a[80:83], v[144:147], v[68:71], a[80:83]\n\t"
    "ds_read_b64_tr_b16 v[124:125], v239 offset:49920\n\t"
    "v_mfma_f32_16x16x32_bf16 a[20:23], v[148:151], v[60:63], a[20:23]\n\t"
    "ds_read_b64_tr_b16 v[126:127], v239 offset:54016\n\t"
    "v_mfma_f32_16x16x32_bf16 a[52:55], v[148:151], v[64:67], a[52:55]\n\t"
    "v_mfma_f32_16x16x32_bf16 a[84:87], v[148:151], v[68:71], a[84:87]\n\t"
    "v_mfma_f32_16x16x32_bf16 a[24:27], v[152:155], v[60:63], a[24:27]\n\t"
    "v_mfma_f32_16x16x32_bf16 a[56:59], v[152:155], v[64:67], a[56:59]\n\t"
    "v_mfma_f32_16x16x32_bf16 a[88:91], v[152:155], v[68:71], a[88:91]\n\t"
    "v_mfma_f32_16x16x32_bf16 a[28:31], v[156:159], v[60:63], a[28:31]\n\t"
    "v_mfma_f32_16x16x32_bf16 a[60:63], v[156:159], v[64:67], a[60:63]\n\t"
    "v_mfma_f32_16x16x32_bf16 a[92:95], v[156:159], v[68:71], a[92:95]\n\t"
    "s_waitcnt vmcnt(8) lgkmcnt(0)\n\t"
    "s_barrier\n\t"
    "v_mfma_f32_16x16x32_bf16 a[0:3], v[96:99], v[72:75], a[0:3]\n\t"
    "ds_read_b64_tr_b16 v[128:129], v238 offset:57344\n\t"
    "v_mfma_f32_16x16x32_bf16 a[32:35], v[96:99], v[76:79], a[32:35]\n\t"
    "ds_read_b64_tr_b16 v[130:131], v238 offset:61440\n\t"
    "v_mfma_f32_16x16x32_bf16 a[64:67], v[96:99], v[80:83], a[64:67]\n\t"
    "ds_read_b64_tr_b16 v[132:133], v239 offset:57344\n\t"
    "v_mfma_f32_16x16x32_bf16 a[4:7], v[100:103], v[72:75], a[4:7]\n\t"
    "ds_read_b64_tr_b16 v[134:135], v239 offset:61440\n\t"
    "v_mfma_f32_16x16x32_bf16 a[36:39], v[100:103], v[76:79], a[36:39]\n\t"
    "ds_read_b64_tr_b16 v[136:137], v238 offset:57600\n\t"
    "v_mfma_f32_16x16x32_bf16 a[68:71], v[100:103], v[80:83], a[68:71]\n\t"
    "ds_read_b64_tr_b16 v[138:139], v238 offset:61696\n\t"
    "v_mfma_f32_16x16x32_bf16 a[8:11], v[104:107], v[72:75], a[8:11]\n\t"
    "ds_read_b64_tr_b16 v[140:141], v239 offset:57600\n\t"
    "v_mfma_f32_16x16x32_bf16 a[40:43], v[104:107], v[76:79], a[40:43]\n\t"
    "ds_read_b64_tr_b16 v[142:143], v239 offset:61696\n\t"
    "v_mfma_f32_16x16x32_bf16 a[72:75], v[104:107], v[80:83], a[72:75]\n\t"
    "ds_read_b64_tr_b16 v[144:145], v238 offset:57856\n\t"
    "v_mfma_f32_16x16x32_bf16 a[12:15], v[108:111], v[72:75], a[12:15]\n\t"
    "ds_read_b64_tr_b16 v[146:147], v238 offset:61952\n\t"
    "v_mfma_f32_16x16x32_bf16 a[44:47], v[108:111], v[76:79], a[44:47]\n\t"
    "ds_read_b64_tr_b16 v[148:149], v239 offset:57856\n\t"
    "v_mfma_f32_16x16x32_bf16 a[76:79], v[108:111], v[80:83], a[76:79]\n\t"
    "ds_read_b64_tr_b16 v[150:151], v239 offset:61952\n\t"
    "v_mfma_f32_16x16x32_bf16 a[16:19], v[112:115], v[72:75], a[16:19]\n\t"
    "ds_read_b64_tr_b16 v[152:153], v238 offset:58112\n\t"
    "v_mfma_f32_16x16x32_bf16 a[48:51], v[112:115], v[76:79], a[48:51]\n\t"
    "ds_read_b64_tr_b16 v[154:155], v238 offset:62208\n\t"
    "v_mfma_f32_16x16x32_bf16 a[80:83], v[112:115], v[80:83], a[80:83]\n\t"
    "ds_read_b64_tr_b16 v[156:157], v239 offset:58112\n\t"
    "v_mfma_f32_16x16x32_bf16 a[20:23], v[116:119], v[72:75], a[20:23]\n\t"
    "ds_read_b64_tr_b16 v[158:159], v239 offset:62208\n\t"
    "v_mfma_f32_16x16x32_bf16 a[52:55], v[116:119], v[76:79], a[52:55]\n\t"
    "v_mfma_f32_16x16x32_bf16 a[84:87], v[116:119], v[80:83], a[84:87]\n\t"
    "v_mfma_f32_16x16x32_bf16 a[24:27], v[120:123], v[72:75], a[24:27]\n\t"
    "v_mfma_f32_16x16x32_bf16 a[56:59], v[120:123], v[76:79], a[56:59]\n\t"
    "v_mfma_f32_16x16x32_bf16 a[88:91], v[120:123], v[80:83], a[88:91]\n\t"
    "v_mfma_f32_16x16x32_bf16 a[28:31], v[124:127], v[72:75], a[28:31]\n\t"
    "s_waitcnt vmcnt(0)\n\t"
    "s_barrier\n\t"
    "buffer_store_dword v208, v243, s[24:27], 0 offen offset:0 sc1\n\t"
    "v_mfma_f32_16x16x32_bf16 a[60:63], v[124:127], v[76:79], a[60:63]\n\t"
    "v_mfma_f32_16x16x32_bf16 a[92:95], v[124:127], v[80:83], a[92:95]\n\t"
    "s_waitcnt lgkmcnt(0)\n\t"
    "v_mfma_f32_16x16x32_bf16 a[16:19], v[144:147], v[84:87], a[16:19]\n\t"
    "v_mfma_f32_16x16x32_bf16 a[20:23], v[148:151], v[84:87], a[20:23]\n\t"
    "v_mfma_f32_16x16x32_bf16 a[24:27], v[152:155], v[84:87], a[24:27]\n\t"
    "v_mfma_f32_16x16x32_bf16 a[28:31], v[156:159], v[84:87], a[28:31]\n\t"
    "v_mfma_f32_16x16x32_bf16 a[48:51], v[144:147], v[88:91], a[48:51]\n\t"
    "v_mfma_f32_16x16x32_bf16 a[52:55], v[148:151], v[88:91], a[52:55]\n\t"
    "v_mfma_f32_16x16x32_bf16 a[56:59], v[152:155], v[88:91], a[56:59]\n\t"
    "s_nop 1\n\t"
    "ds_write_b128 v206, a[16:19] offset:0\n\t"
    "v_mfma_f32_16x16x32_bf16 a[60:63], v[156:159], v[88:91], a[60:63]\n\t"
    "ds_write_b128 v206, a[20:23] offset:1024\n\t"
    "v_mfma_f32_16x16x32_bf16 a[80:83], v[144:147], v[92:95], a[80:83]\n\t"
    "ds_write_b128 v206, a[24:27] offset:2048\n\t"
    "v_mfma_f32_16x16x32_bf16 a[84:87], v[148:151], v[92:95], a[84:87]\n\t"
    "v_mfma_f32_16x16x32_bf16 a[88:91], v[152:155], v[92:95], a[88:91]\n\t"
    "ds_write_b128 v206, a[28:31] offset:3072\n\t"
    "v_mfma_f32_16x16x32_bf16 a[92:95], v[156:159], v[92:95], a[92:95]\n\t"
    "ds_write_b128 v206, a[48:51] offset:4096\n\t"
    "v_mfma_f32_16x16x32_bf16 a[0:3], v[128:131], v[84:87], a[0:3]\n\t"
    "ds_write_b128 v206, a[52:55] offset:5120\n\t"
    "v_mfma_f32_16x16x32_bf16 a[4:7], v[132:135], v[84:87], a[4:7]\n\t"
    "v_mfma_f32_16x16x32_bf16 a[8:11], v[136:139], v[84:87], a[8:11]\n\t"
    "ds_write_b128 v206, a[56:59] offset:6144\n\t"
    "v_mfma_f32_16x16x32_bf16 a[12:15], v[140:143], v[84:87], a[12:15]\n\t"
    "ds_write_b128 v206, a[60:63] offset:7168\n\t"
    "v_mfma_f32_16x16x32_bf16 a[32:35], v[128:131], v[88:91], a[32:35]\n\t"
    "ds_write_b128 v206, a[80:83] offset:8192\n\t"
    "buffer_load_dword v221, v218, s[24:27], 0 offen offset:0 sc1\n\t"
    "v_mfma_f32_16x16x32_bf16 a[36:39], v[132:135], v[88:91], a[36:39]\n\t"
    "v_mfma_f32_16x16x32_bf16 a[40:43], v[136:139], v[88:91], a[40:43]\n\t"
    "ds_write_b128 v206, a[84:87] offset:9216\n\t"
    "v_mfma_f32_16x16x32_bf16 a[44:47], v[140:143], v[88:91], a[44:47]\n\t"
    "ds_write_b128 v206, a[88:91] offset:10240\n\t"
    "v_mfma_f32_16x16x32_bf16 a[64:67], v[128:131], v[92:95], a[64:67]\n\t"
    "ds_write_b128 v206, a[92:95] offset:11264\n\t"
    "v_mfma_f32_16x16x32_bf16 a[68:71], v[132:135], v[92:95], a[68:71]\n\t"
    "v_mfma_f32_16x16x32_bf16 a[72:75], v[136:139], v[92:95], a[72:75]\n\t"
    "v_mfma_f32_16x16x32_bf16 a[76:79], v[140:143], v[92:95], a[76:79]\n\t"
    "s_waitcnt vmcnt(0)\n\t"
    "v_cmp_ne_u32 vcc, v221, v209\n\t"
    "s_nop 1\n\t"
    "s_and_b32 s73, vcc_lo, 0x100\n\t"
    "s_cbranch_scc0 .LFL2OK_%=\n\t"
    "s_mov_b32 s74, 65536\n\t"
    ".LFL2_%=:\n\t"
    "buffer_load_dword v221, v218, s[24:27], 0 offen offset:0 sc1\n\t"
    "s_waitcnt vmcnt(0)\n\t"
    "v_cmp_ne_u32 vcc, v221, v209\n\t"
    "s_nop 1\n\t"
    "s_and_b32 s73, vcc_lo, 0x100\n\t"
    "s_cbranch_scc0 .LFL2OK_%=\n\t"
    "s_sleep 1\n\t"
    "s_sub_u32 s74, s74, 1\n\t"
    "s_cmp_lg_u32 s74, 0\n\t"
    "s_cbranch_scc1 .LFL2_%=\n\t"
    ".LFL2OK_%=:\n\t"
    "s_xor_b32 s59, s59, 524288\n\t"
    "s_add_u32 s73, s59, 0\n\t"
    "buffer_load_dwordx4 v[0:3], v216, s[24:27], s73 offen offset:0 sc1\n\t"
    "buffer_load_dwordx4 v[4:7], v216, s[24:27], s73 offen offset:1024 sc1\n\t"
    "buffer_load_dwordx4 v[8:11], v216, s[24:27], s73 offen offset:2048 sc1\n\t"
    "buffer_load_dwordx4 v[12:15], v216, s[24:27], s73 offen offset:3072 sc1\n\t"
    "s_add_u32 s73, s59, 4096\n\t"
    "buffer_load_dwordx4 v[16:19], v216, s[24:27], s73 offen offset:0 sc1\n\t"
    "buffer_load_dwordx4 v[20:23], v216, s[24:27], s73 offen offset:1024 sc1\n\t"
    "buffer_load_dwordx4 v[24:27], v216, s[24:27], s73 offen offset:2048 sc1\n\t"
    "buffer_load_dwordx4 v[28:31], v216, s[24:27], s73 offen offset:3072 sc1\n\t"
    "s_add_u32 s73, s59, 8192\n\t"
    "buffer_load_dwordx4 v[32:35], v216, s[24:27], s73 offen offset:0 sc1\n\t"
    "buffer_load_dwordx4 v[36:39], v216, s[24:27], s73 offen offset:1024 sc1\n\t"
    "buffer_load_dwordx4 v[40:43], v216, s[24:27], s73 offen offset:2048 sc1\n\t"
    "buffer_load_dwordx4 v[44:47], v216, s[24:27], s73 offen offset:3072 sc1\n\t"
    "s_waitcnt lgkmcnt(0)\n\t"
    "s_barrier\n\t"
    "ds_read_b128 v[48:51], v207 offset:0\n\t"
    "ds_read_b128 v[52:55], v207 offset:1024\n\t"
    "ds_read_b128 v[56:59], v207 offset:2048\n\t"
    "ds_read_b128 v[60:63], v207 offset:3072\n\t"
    "ds_read_b128 v[64:67], v207 offset:4096\n\t"
    "ds_read_b128 v[68:71], v207 offset:5120\n\t"
    "ds_read_b128 v[72:75], v207 offset:6144\n\t"
    "ds_read_b128 v[76:79], v207 offset:7168\n\t"
    "ds_read_b128 v[80:83], v207 offset:8192\n\t"
    "ds_read_b128 v[84:87], v207 offset:9216\n\t"
    "ds_read_b128 v[88:91], v207 offset:10240\n\t"
    "ds_read_b128 v[92:95], v207 offset:11264\n\t"
    "v_accvgpr_read_b32 v96, a0\n\t"
    "v_accvgpr_read_b32 v97, a1\n\t"
    "v_accvgpr_read_b32 v98, a2\n\t"
    "v_accvgpr_read_b32 v99, a3\n\t"
    "s_waitcnt lgkmcnt(11)\n\t"
    "v_pk_add_f32 v[48:49], v[48:49], v[96:97]\n\t"
    "v_pk_add_f32 v[50:51], v[50:51], v[98:99]\n\t"
    "v_accvgpr_read_b32 v100, a4\n\t"
    "v_accvgpr_read_b32 v101, a5\n\t"
    "v_accvgpr_read_b32 v102, a6\n\t"
    "v_accvgpr_read_b32 v103, a7\n\t"
    "s_waitcnt lgkmcnt(10)\n\t"
    "v_pk_add_f32 v[52:53], v[52:53], v[100:101]\n\t"
    "v_pk_add_f32 v[54:55], v[54:55], v[102:103]\n\t"
    "v_accvgpr_read_b32 v104, a8\n\t"
    "v_accvgpr_read_b32 v105, a9\n\t"
    "v_accvgpr_read_b32 v106, a10\n\t"
    "v_accvgpr_read_b32 v107, a11\n\t"
    "s_waitcnt lgkmcnt(9)\n\t"
    "v_pk_add_f32 v[56:57], v[56:57], v[104:105]\n\t"
    "v_pk_add_f32 v[58:59], v[58:59], v[106:107]\n\t"
    "v_accvgpr_read_b32 v108, a12\n\t"
    "v_accvgpr_read_b32 v109, a13\n\t"
    "v_accvgpr_read_b32 v110, a14\n\t"
    "v_accvgpr_read_b32 v111, a15\n\t"
    "s_waitcnt lgkmcnt(8)\n\t"
    "v_pk_add_f32 v[60:61], v[60:61], v[108:109]\n\t"
    "v_pk_add_f32 v[62:63], v[62:63], v[110:111]\n\t"
    "v_accvgpr_read_b32 v96, a32\n\t"
    "v_accvgpr_read_b32 v97, a33\n\t"
    "v_accvgpr_read_b32 v98, a34\n\t"
    "v_accvgpr_read_b32 v99, a35\n\t"
    "s_waitcnt lgkmcnt(7)\n\t"
    "v_pk_add_f32 v[64:65], v[64:65], v[96:97]\n\t"
    "v_pk_add_f32 v[66:67], v[66:67], v[98:99]\n\t"
    "v_accvgpr_read_b32 v100, a36\n\t"
    "v_accvgpr_read_b32 v101, a37\n\t"
    "v_accvgpr_read_b32 v102, a38\n\t"
    "v_accvgpr_read_b32 v103, a39\n\t"
    "s_waitcnt lgkmcnt(6)\n\t"
    "v_pk_add_f32 v[68:69], v[68:69], v[100:101]\n\t"
    "v_pk_add_f32 v[70:71], v[70:71], v[102:103]\n\t"
    "v_accvgpr_read_b32 v104, a40\n\t"
    "v_accvgpr_read_b32 v105, a41\n\t"
    "v_accvgpr_read_b32 v106, a42\n\t"
    "v_accvgpr_read_b32 v107, a43\n\t"
    "s_waitcnt lgkmcnt(5)\n\t"
    "v_pk_add_f32 v[72:73], v[72:73], v[104:105]\n\t"
    "v_pk_add_f32 v[74:75], v[74:75], v[106:107]\n\t"
    "v_accvgpr_read_b32 v108, a44\n\t"
    "v_accvgpr_read_b32 v109, a45\n\t"
    "v_accvgpr_read_b32 v110, a46\n\t"
    "v_accvgpr_read_b32 v111, a47\n\t"
    "s_waitcnt lgkmcnt(4)\n\t"
    "v_pk_add_f32 v[76:77], v[76:77], v[108:109]\n\t"
    "v_pk_add_f32 v[78:79], v[78:79], v[110:111]\n\t"
    "v_accvgpr_read_b32 v96, a64\n\t"
    "v_accvgpr_read_b32 v97, a65\n\t"
    "v_accvgpr_read_b32 v98, a66\n\t"
    "v_accvgpr_read_b32 v99, a67\n\t"
    "s_waitcnt lgkmcnt(3)\n\t"
    "v_pk_add_f32 v[80:81], v[80:81], v[96:97]\n\t"
    "v_pk_add_f32 v[82:83], v[82:83], v[98:99]\n\t"
    "v_accvgpr_read_b32 v100, a68\n\t"
    "v_accvgpr_read_b32 v101, a69\n\t"
    "v_accvgpr_read_b32 v102, a70\n\t"
    "v_accvgpr_read_b32 v103, a71\n\t"
    "s_waitcnt lgkmcnt(2)\n\t"
    "v_pk_add_f32 v[84:85], v[84:85], v[100:101]\n\t"
    "v_pk_add_f32 v[86:87], v[86:87], v[102:103]\n\t"
    "v_accvgpr_read_b32 v104, a72\n\t"
    "v_accvgpr_read_b32 v105, a73\n\t"
    "v_accvgpr_read_b32 v106, a74\n\t"
    "v_accvgpr_read_b32 v107, a75\n\t"
    "s_waitcnt lgkmcnt(1)\n\t"
    "v_pk_add_f32 v[88:89], v[88:89], v[104:105]\n\t"
    "v_pk_add_f32 v[90:91], v[90:91], v[106:107]\n\t"
    "v_accvgpr_read_b32 v108, a76\n\t"
    "v_accvgpr_read_b32 v109, a77\n\t"
    "v_accvgpr_read_b32 v110, a78\n\t"
    "v_accvgpr_read_b32 v111, a79\n\t"
    "s_waitcnt lgkmcnt(0)\n\t"
    "v_pk_add_f32 v[92:93], v[92:93], v[108:109]\n\t"
    "v_pk_add_f32 v[94:95], v[94:95], v[110:111]\n\t"
    "s_waitcnt vmcnt(11)\n\t"
    "v_pk_add_f32 v[48:49], v[48:49], v[0:1]\n\t"
    "v_pk_add_f32 v[50:51], v[50:51], v[2:3]\n\t"
    "s_waitcnt vmcnt(10)\n\t"
    "v_pk_add_f32 v[52:53], v[52:53], v[4:5]\n\t"
    "v_pk_add_f32 v[54:55], v[54:55], v[6:7]\n\t"
    "v_cvt_pk_bf16_f32 v112, v48, v49\n\t"
    "v_cvt_pk_bf16_f32 v113, v50, v51\n\t"
    "v_cvt_pk_bf16_f32 v114, v52, v53\n\t"
    "v_cvt_pk_bf16_f32 v115, v54, v55\n\t"
    "s_nop 0\n\t"
    "v_permlane32_swap_b32 v112, v114\n\t"
    "v_permlane32_swap_b32 v113, v115\n\t"
    "s_nop 0\n\t"
    "v_permlane16_swap_b32 v112, v114\n\t"
    "v_permlane16_swap_b32 v113, v115\n\t"
    "s_mov_b32 s73, 0\n\t"
    "buffer_store_dwordx4 v[112:115], v244, s[20:23], s73 offen offset:0\n\t"
    "s_waitcnt vmcnt(10)\n\t"
    "v_pk_add_f32 v[56:57], v[56:57], v[8:9]\n\t"
    "v_pk_add_f32 v[58:59], v[58:59], v[10:11]\n\t"
    "s_waitcnt vmcnt(9)\n\t"
    "v_pk_add_f32 v[60:61], v[60:61], v[12:13]\n\t"
    "v_pk_add_f32 v[62:63], v[62:63], v[14:15]\n\t"
    "v_cvt_pk_bf16_f32 v116, v56, v57\n\t"
    "v_cvt_pk_bf16_f32 v117, v58, v59\n\t"
    "v_cvt_pk_bf16_f32 v118, v60, v61\n\t"
    "v_cvt_pk_bf16_f32 v119, v62, v63\n\t"
    "s_nop 0\n\t"
    "v_permlane32_swap_b32 v116, v118\n\t"
    "v_permlane32_swap_b32 v117, v119\n\t"
    "s_nop 0\n\t"
    "v_permlane16_swap_b32 v116, v118\n\t"
    "v_permlane16_swap_b32 v117, v119\n\t"
    "s_mov_b32 s73, 0\n\t"
    "buffer_store_dwordx4 v[116:119], v244, s[20:23], s73 offen offset:64\n\t"
    "s_waitcnt vmcnt(9)\n\t"
    "v_pk_add_f32 v[64:65], v[64:65], v[16:17]\n\t"
    "v_pk_add_f32 v[66:67], v[66:67], v[18:19]\n\t"
    "s_waitcnt vmcnt(8)\n\t"
    "v_pk_add_f32 v[68:69], v[68:69], v[20:21]\n\t"
    "v_pk_add_f32 v[70:71], v[70:71], v[22:23]\n\t"
    "v_cvt_pk_bf16_f32 v120, v64, v65\n\t"
    "v_cvt_pk_bf16_f32 v121, v66, v67\n\t"
    "v_cvt_pk_bf16_f32 v122, v68, v69\n\t"
    "v_cvt_pk_bf16_f32 v123, v70, v71\n\t"
    "s_nop 0\n\t"
    "v_permlane32_swap_b32 v120, v122\n\t"
    "v_permlane32_swap_b32 v121, v123\n\t"
    "s_nop 0\n\t"
    "v_permlane16_swap_b32 v120, v122\n\t"
    "v_permlane16_swap_b32 v121, v123\n\t"
    "s_mov_b32 s73, 32768\n\t"
    "buffer_store_dwordx4 v[120:123], v244, s[20:23], s73 offen offset:0\n\t"
    "s_waitcnt vmcnt(8)\n\t"
    "v_pk_add_f32 v[72:73], v[72:73], v[24:25]\n\t"
    "v_pk_add_f32 v[74:75], v[74:75], v[26:27]\n\t"
    "s_waitcnt vmcnt(7)\n\t"
    "v_pk_add_f32 v[76:77], v[76:77], v[28:29]\n\t"
    "v_pk_add_f32 v[78:79], v[78:79], v[30:31]\n\t"
    "v_cvt_pk_bf16_f32 v124, v72, v73\n\t"
    "v_cvt_pk_bf16_f32 v125, v74, v75\n\t"
    "v_cvt_pk_bf16_f32 v126, v76, v77\n\t"
    "v_cvt_pk_bf16_f32 v127, v78, v79\n\t"
    "s_nop 0\n\t"
    "v_permlane32_swap_b32 v124, v126\n\t"
    "v_permlane32_swap_b32 v125, v127\n\t"
    "s_nop 0\n\t"
    "v_permlane16_swap_b32 v124, v126\n\t"
    "v_permlane16_swap_b32 v125, v127\n\t"
    "s_mov_b32 s73, 32768\n\t"
    "buffer_store_dwordx4 v[124:127], v244, s[20:23], s73 offen offset:64\n\t"
    "s_waitcnt vmcnt(7)\n\t"
    "v_pk_add_f32 v[80:81], v[80:81], v[32:33]\n\t"
    "v_pk_add_f32 v[82:83], v[82:83], v[34:35]\n\t"
    "s_waitcnt vmcnt(6)\n\t"
    "v_pk_add_f32 v[84:85], v[84:85], v[36:37]\n\t"
    "v_pk_add_f32 v[86:87], v[86:87], v[38:39]\n\t"
    "v_cvt_pk_bf16_f32 v128, v80, v81\n\t"
    "v_cvt_pk_bf16_f32 v129, v82, v83\n\t"
    "v_cvt_pk_bf16_f32 v130, v84, v85\n\t"
    "v_cvt_pk_bf16_f32 v131, v86, v87\n\t"
    "s_nop 0\n\t"
    "v_permlane32_swap_b32 v128, v130\n\t"
    "v_permlane32_swap_b32 v129, v131\n\t"
    "s_nop 0\n\t"
    "v_permlane16_swap_b32 v128, v130\n\t"
    "v_permlane16_swap_b32 v129, v131\n\t"
    "s_mov_b32 s73, 65536\n\t"
    "buffer_store_dwordx4 v[128:131], v244, s[20:23], s73 offen offset:0\n\t"
    "s_waitcnt vmcnt(6)\n\t"
    "v_pk_add_f32 v[88:89], v[88:89], v[40:41]\n\t"
    "v_pk_add_f32 v[90:91], v[90:91], v[42:43]\n\t"
    "s_waitcnt vmcnt(5)\n\t"
    "v_pk_add_f32 v[92:93], v[92:93], v[44:45]\n\t"
    "v_pk_add_f32 v[94:95], v[94:95], v[46:47]\n\t"
    "v_cvt_pk_bf16_f32 v132, v88, v89\n\t"
    "v_cvt_pk_bf16_f32 v133, v90, v91\n\t"
    "v_cvt_pk_bf16_f32 v134, v92, v93\n\t"
    "v_cvt_pk_bf16_f32 v135, v94, v95\n\t"
    "s_nop 0\n\t"
    "v_permlane32_swap_b32 v132, v134\n\t"
    "v_permlane32_swap_b32 v133, v135\n\t"
    "s_nop 0\n\t"
    "v_permlane16_swap_b32 v132, v134\n\t"
    "v_permlane16_swap_b32 v133, v135\n\t"
    "s_mov_b32 s73, 65536\n\t"
    "buffer_store_dwordx4 v[132:135], v244, s[20:23], s73 offen offset:64\n\t"
    "s_branch .LEND_%=\n\t"
    ".LSTUB_F_0_%=:\n\t"
    "s_mov_b32 s75, 0\n\t"
    "s_branch .LRARE_0_%=\n\t"
    ".LSTUB_L_0_%=:\n\t"
    "s_mov_b32 s75, 1\n\t"
    "s_branch .LRARE_0_%=\n\t"
    ".LRARE_0_%=:\n\t"
    "s_nop 15\n\t"
    "v_max3_f32 v252, v128, v129, v130\n\t"
    "v_max3_f32 v252, v252, v131, v132\n\t"
    "v_max3_f32 v252, v252, v133, v134\n\t"
    "v_max_f32 v252, v252, v135\n\t"
    "v_mov_b32 v253, v252\n\t"
    "s_nop 1\n\t"
    "v_permlane32_swap_b32 v252, v253\n\t"
    "v_max_f32 v252, v252, v253\n\t"
    "v_mov_b32 v253, v252\n\t"
    "s_nop 1\n\t"
    "v_permlane16_swap_b32 v252, v253\n\t"
    "v_max_f32 v252, v252, v253\n\t"
    "v_max_f32 v252, 0, v252\n\t"
    "v_exp_f32 v231, -v252\n\t"
    "v_sub_f32 v128, v128, v252\n\t"
    "v_sub_f32 v129, v129, v252\n\t"
    "v_sub_f32 v130, v130, v252\n\t"
    "v_sub_f32 v131, v131, v252\n\t"
    "v_sub_f32 v132, v132, v252\n\t"
    "v_sub_f32 v133, v133, v252\n\t"
    "v_sub_f32 v134, v134, v252\n\t"
    "v_sub_f32 v135, v135, v252\n\t"
    "v_sub_f32 v152, v152, v252\n\t"
    "v_sub_f32 v153, v153, v252\n\t"
    "v_sub_f32 v154, v154, v252\n\t"
    "v_sub_f32 v155, v155, v252\n\t"
    "v_accvgpr_read_b32 v253, a0\n\t"
    "v_mul_f32 v253, v253, v231\n\t"
    "v_accvgpr_write_b32 a0, v253\n\t"
    "v_accvgpr_read_b32 v254, a1\n\t"
    "v_mul_f32 v254, v254, v231\n\t"
    "v_accvgpr_write_b32 a1, v254\n\t"
    "v_accvgpr_read_b32 v255, a2\n\t"
    "v_mul_f32 v255, v255, v231\n\t"
    "v_accvgpr_write_b32 a2, v255\n\t"
    "v_accvgpr_read_b32 v253, a3\n\t"
    "v_mul_f32 v253, v253, v231\n\t"
    "v_accvgpr_write_b32 a3, v253\n\t"
    "v_accvgpr_read_b32 v254, a4\n\t"
    "v_mul_f32 v254, v254, v231\n\t"
    "v_accvgpr_write_b32 a4, v254\n\t"
    "v_accvgpr_read_b32 v255, a5\n\t"
    "v_mul_f32 v255, v255, v231\n\t"
    "v_accvgpr_write_b32 a5, v255\n\t"
    "v_accvgpr_read_b32 v253, a6\n\t"
    "v_mul_f32 v253, v253, v231\n\t"
    "v_accvgpr_write_b32 a6, v253\n\t"
    "v_accvgpr_read_b32 v254, a7\n\t"
    "v_mul_f32 v254, v254, v231\n\t"
    "v_accvgpr_write_b32 a7, v254\n\t"
    "v_accvgpr_read_b32 v255, a8\n\t"
    "v_mul_f32 v255, v255, v231\n\t"
    "v_accvgpr_write_b32 a8, v255\n\t"
    "v_accvgpr_read_b32 v253, a9\n\t"
    "v_mul_f32 v253, v253, v231\n\t"
    "v_accvgpr_write_b32 a9, v253\n\t"
    "v_accvgpr_read_b32 v254, a10\n\t"
    "v_mul_f32 v254, v254, v231\n\t"
    "v_accvgpr_write_b32 a10, v254\n\t"
    "v_accvgpr_read_b32 v255, a11\n\t"
    "v_mul_f32 v255, v255, v231\n\t"
    "v_accvgpr_write_b32 a11, v255\n\t"
    "v_accvgpr_read_b32 v253, a12\n\t"
    "v_mul_f32 v253, v253, v231\n\t"
    "v_accvgpr_write_b32 a12, v253\n\t"
    "v_accvgpr_read_b32 v254, a13\n\t"
    "v_mul_f32 v254, v254, v231\n\t"
    "v_accvgpr_write_b32 a13, v254\n\t"
    "v_accvgpr_read_b32 v255, a14\n\t"
    "v_mul_f32 v255, v255, v231\n\t"
    "v_accvgpr_write_b32 a14, v255\n\t"
    "v_accvgpr_read_b32 v253, a15\n\t"
    "v_mul_f32 v253, v253, v231\n\t"
    "v_accvgpr_write_b32 a15, v253\n\t"
    "v_accvgpr_read_b32 v254, a16\n\t"
    "v_mul_f32 v254, v254, v231\n\t"
    "v_accvgpr_write_b32 a16, v254\n\t"
    "v_accvgpr_read_b32 v255, a17\n\t"
    "v_mul_f32 v255, v255, v231\n\t"
    "v_accvgpr_write_b32 a17, v255\n\t"
    "v_accvgpr_read_b32 v253, a18\n\t"
    "v_mul_f32 v253, v253, v231\n\t"
    "v_accvgpr_write_b32 a18, v253\n\t"
    "v_accvgpr_read_b32 v254, a19\n\t"
    "v_mul_f32 v254, v254, v231\n\t"
    "v_accvgpr_write_b32 a19, v254\n\t"
    "v_accvgpr_read_b32 v255, a20\n\t"
    "v_mul_f32 v255, v255, v231\n\t"
    "v_accvgpr_write_b32 a20, v255\n\t"
    "v_accvgpr_read_b32 v253, a21\n\t"
    "v_mul_f32 v253, v253, v231\n\t"
    "v_accvgpr_write_b32 a21, v253\n\t"
    "v_accvgpr_read_b32 v254, a22\n\t"
    "v_mul_f32 v254, v254, v231\n\t"
    "v_accvgpr_write_b32 a22, v254\n\t"
    "v_accvgpr_read_b32 v255, a23\n\t"
    "v_mul_f32 v255, v255, v231\n\t"
    "v_accvgpr_write_b32 a23, v255\n\t"
    "v_accvgpr_read_b32 v253, a24\n\t"
    "v_mul_f32 v253, v253, v231\n\t"
    "v_accvgpr_write_b32 a24, v253\n\t"
    "v_accvgpr_read_b32 v254, a25\n\t"
    "v_mul_f32 v254, v254, v231\n\t"
    "v_accvgpr_write_b32 a25, v254\n\t"
    "v_accvgpr_read_b32 v255, a26\n\t"
    "v_mul_f32 v255, v255, v231\n\t"
    "v_accvgpr_write_b32 a26, v255\n\t"
    "v_accvgpr_read_b32 v253, a27\n\t"
    "v_mul_f32 v253, v253, v231\n\t"
    "v_accvgpr_write_b32 a27, v253\n\t"
    "v_accvgpr_read_b32 v254, a28\n\t"
    "v_mul_f32 v254, v254, v231\n\t"
    "v_accvgpr_write_b32 a28, v254\n\t"
    "v_accvgpr_read_b32 v255, a29\n\t"
    "v_mul_f32 v255, v255, v231\n\t"
    "v_accvgpr_write_b32 a29, v255\n\t"
    "v_accvgpr_read_b32 v253, a30\n\t"
    "v_mul_f32 v253, v253, v231\n\t"
    "v_accvgpr_write_b32 a30, v253\n\t"
    "v_accvgpr_read_b32 v254, a31\n\t"
    "v_mul_f32 v254, v254, v231\n\t"
    "v_accvgpr_write_b32 a31, v254\n\t"
    "v_accvgpr_read_b32 v255, a192\n\t"
    "v_mul_f32 v255, v255, v231\n\t"
    "v_accvgpr_write_b32 a192, v255\n\t"
    "v_accvgpr_read_b32 v253, a193\n\t"
    "v_mul_f32 v253, v253, v231\n\t"
    "v_accvgpr_write_b32 a193, v253\n\t"
    "v_accvgpr_read_b32 v254, a194\n\t"
    "v_mul_f32 v254, v254, v231\n\t"
    "v_accvgpr_write_b32 a194, v254\n\t"
    "v_accvgpr_read_b32 v255, a195\n\t"
    "v_mul_f32 v255, v255, v231\n\t"
    "v_accvgpr_write_b32 a195, v255\n\t"
    "v_max3_f32 v252, v136, v137, v138\n\t"
    "v_max3_f32 v252, v252, v139, v140\n\t"
    "v_max3_f32 v252, v252, v141, v142\n\t"
    "v_max_f32 v252, v252, v143\n\t"
    "v_mov_b32 v253, v252\n\t"
    "s_nop 1\n\t"
    "v_permlane32_swap_b32 v252, v253\n\t"
    "v_max_f32 v252, v252, v253\n\t"
    "v_mov_b32 v253, v252\n\t"
    "s_nop 1\n\t"
    "v_permlane16_swap_b32 v252, v253\n\t"
    "v_max_f32 v252, v252, v253\n\t"
    "v_max_f32 v252, 0, v252\n\t"
    "v_exp_f32 v231, -v252\n\t"
    "v_sub_f32 v136, v136, v252\n\t"
    "v_sub_f32 v137, v137, v252\n\t"
    "v_sub_f32 v138, v138, v252\n\t"
    "v_sub_f32 v139, v139, v252\n\t"
    "v_sub_f32 v140, v140, v252\n\t"
    "v_sub_f32 v141, v141, v252\n\t"
    "v_sub_f32 v142, v142, v252\n\t"
    "v_sub_f32 v143, v143, v252\n\t"
    "v_sub_f32 v156, v156, v252\n\t"
    "v_sub_f32 v157, v157, v252\n\t"
    "v_sub_f32 v158, v158, v252\n\t"
    "v_sub_f32 v159, v159, v252\n\t"
    "v_accvgpr_read_b32 v253, a32\n\t"
    "v_mul_f32 v253, v253, v231\n\t"
    "v_accvgpr_write_b32 a32, v253\n\t"
    "v_accvgpr_read_b32 v254, a33\n\t"
    "v_mul_f32 v254, v254, v231\n\t"
    "v_accvgpr_write_b32 a33, v254\n\t"
    "v_accvgpr_read_b32 v255, a34\n\t"
    "v_mul_f32 v255, v255, v231\n\t"
    "v_accvgpr_write_b32 a34, v255\n\t"
    "v_accvgpr_read_b32 v253, a35\n\t"
    "v_mul_f32 v253, v253, v231\n\t"
    "v_accvgpr_write_b32 a35, v253\n\t"
    "v_accvgpr_read_b32 v254, a36\n\t"
    "v_mul_f32 v254, v254, v231\n\t"
    "v_accvgpr_write_b32 a36, v254\n\t"
    "v_accvgpr_read_b32 v255, a37\n\t"
    "v_mul_f32 v255, v255, v231\n\t"
    "v_accvgpr_write_b32 a37, v255\n\t"
    "v_accvgpr_read_b32 v253, a38\n\t"
    "v_mul_f32 v253, v253, v231\n\t"
    "v_accvgpr_write_b32 a38, v253\n\t"
    "v_accvgpr_read_b32 v254, a39\n\t"
    "v_mul_f32 v254, v254, v231\n\t"
    "v_accvgpr_write_b32 a39, v254\n\t"
    "v_accvgpr_read_b32 v255, a40\n\t"
    "v_mul_f32 v255, v255, v231\n\t"
    "v_accvgpr_write_b32 a40, v255\n\t"
    "v_accvgpr_read_b32 v253, a41\n\t"
    "v_mul_f32 v253, v253, v231\n\t"
    "v_accvgpr_write_b32 a41, v253\n\t"
    "v_accvgpr_read_b32 v254, a42\n\t"
    "v_mul_f32 v254, v254, v231\n\t"
    "v_accvgpr_write_b32 a42, v254\n\t"
    "v_accvgpr_read_b32 v255, a43\n\t"
    "v_mul_f32 v255, v255, v231\n\t"
    "v_accvgpr_write_b32 a43, v255\n\t"
    "v_accvgpr_read_b32 v253, a44\n\t"
    "v_mul_f32 v253, v253, v231\n\t"
    "v_accvgpr_write_b32 a44, v253\n\t"
    "v_accvgpr_read_b32 v254, a45\n\t"
    "v_mul_f32 v254, v254, v231\n\t"
    "v_accvgpr_write_b32 a45, v254\n\t"
    "v_accvgpr_read_b32 v255, a46\n\t"
    "v_mul_f32 v255, v255, v231\n\t"
    "v_accvgpr_write_b32 a46, v255\n\t"
    "v_accvgpr_read_b32 v253, a47\n\t"
    "v_mul_f32 v253, v253, v231\n\t"
    "v_accvgpr_write_b32 a47, v253\n\t"
    "v_accvgpr_read_b32 v254, a48\n\t"
    "v_mul_f32 v254, v254, v231\n\t"
    "v_accvgpr_write_b32 a48, v254\n\t"
    "v_accvgpr_read_b32 v255, a49\n\t"
    "v_mul_f32 v255, v255, v231\n\t"
    "v_accvgpr_write_b32 a49, v255\n\t"
    "v_accvgpr_read_b32 v253, a50\n\t"
    "v_mul_f32 v253, v253, v231\n\t"
    "v_accvgpr_write_b32 a50, v253\n\t"
    "v_accvgpr_read_b32 v254, a51\n\t"
    "v_mul_f32 v254, v254, v231\n\t"
    "v_accvgpr_write_b32 a51, v254\n\t"
    "v_accvgpr_read_b32 v255, a52\n\t"
    "v_mul_f32 v255, v255, v231\n\t"
    "v_accvgpr_write_b32 a52, v255\n\t"
    "v_accvgpr_read_b32 v253, a53\n\t"
    "v_mul_f32 v253, v253, v231\n\t"
    "v_accvgpr_write_b32 a53, v253\n\t"
    "v_accvgpr_read_b32 v254, a54\n\t"
    "v_mul_f32 v254, v254, v231\n\t"
    "v_accvgpr_write_b32 a54, v254\n\t"
    "v_accvgpr_read_b32 v255, a55\n\t"
    "v_mul_f32 v255, v255, v231\n\t"
    "v_accvgpr_write_b32 a55, v255\n\t"
    "v_accvgpr_read_b32 v253, a56\n\t"
    "v_mul_f32 v253, v253, v231\n\t"
    "v_accvgpr_write_b32 a56, v253\n\t"
    "v_accvgpr_read_b32 v254, a57\n\t"
    "v_mul_f32 v254, v254, v231\n\t"
    "v_accvgpr_write_b32 a57, v254\n\t"
    "v_accvgpr_read_b32 v255, a58\n\t"
    "v_mul_f32 v255, v255, v231\n\t"
    "v_accvgpr_write_b32 a58, v255\n\t"
    "v_accvgpr_read_b32 v253, a59\n\t"
    "v_mul_f32 v253, v253, v231\n\t"
    "v_accvgpr_write_b32 a59, v253\n\t"
    "v_accvgpr_read_b32 v254, a60\n\t"
    "v_mul_f32 v254, v254, v231\n\t"
    "v_accvgpr_write_b32 a60, v254\n\t"
    "v_accvgpr_read_b32 v255, a61\n\t"
    "v_mul_f32 v255, v255, v231\n\t"
    "v_accvgpr_write_b32 a61, v255\n\t"
    "v_accvgpr_read_b32 v253, a62\n\t"
    "v_mul_f32 v253, v253, v231\n\t"
    "v_accvgpr_write_b32 a62, v253\n\t"
    "v_accvgpr_read_b32 v254, a63\n\t"
    "v_mul_f32 v254, v254, v231\n\t"
    "v_accvgpr_write_b32 a63, v254\n\t"
    "v_accvgpr_read_b32 v255, a196\n\t"
    "v_mul_f32 v255, v255, v231\n\t"
    "v_accvgpr_write_b32 a196, v255\n\t"
    "v_accvgpr_read_b32 v253, a197\n\t"
    "v_mul_f32 v253, v253, v231\n\t"
    "v_accvgpr_write_b32 a197, v253\n\t"
    "v_accvgpr_read_b32 v254, a198\n\t"
    "v_mul_f32 v254, v254, v231\n\t"
    "v_accvgpr_write_b32 a198, v254\n\t"
    "v_accvgpr_read_b32 v255, a199\n\t"
    "v_mul_f32 v255, v255, v231\n\t"
    "v_accvgpr_write_b32 a199, v255\n\t"
    "v_max3_f32 v252, v176, v177, v178\n\t"
    "v_max3_f32 v252, v252, v179, v180\n\t"
    "v_max3_f32 v252, v252, v181, v182\n\t"
    "v_max_f32 v252, v252, v183\n\t"
    "v_mov_b32 v253, v252\n\t"
    "s_nop 1\n\t"
    "v_permlane32_swap_b32 v252, v253\n\t"
    "v_max_f32 v252, v252, v253\n\t"
    "v_mov_b32 v253, v252\n\t"
    "s_nop 1\n\t"
    "v_permlane16_swap_b32 v252, v253\n\t"
    "v_max_f32 v252, v252, v253\n\t"
    "v_max_f32 v252, 0, v252\n\t"
    "v_exp_f32 v231, -v252\n\t"
    "v_sub_f32 v176, v176, v252\n\t"
    "v_sub_f32 v177, v177, v252\n\t"
    "v_sub_f32 v178, v178, v252\n\t"
    "v_sub_f32 v179, v179, v252\n\t"
    "v_sub_f32 v180, v180, v252\n\t"
    "v_sub_f32 v181, v181, v252\n\t"
    "v_sub_f32 v182, v182, v252\n\t"
    "v_sub_f32 v183, v183, v252\n\t"
    "v_sub_f32 v160, v160, v252\n\t"
    "v_sub_f32 v161, v161, v252\n\t"
    "v_sub_f32 v162, v162, v252\n\t"
    "v_sub_f32 v163, v163, v252\n\t"
    "v_accvgpr_read_b32 v253, a64\n\t"
    "v_mul_f32 v253, v253, v231\n\t"
    "v_accvgpr_write_b32 a64, v253\n\t"
    "v_accvgpr_read_b32 v254, a65\n\t"
    "v_mul_f32 v254, v254, v231\n\t"
    "v_accvgpr_write_b32 a65, v254\n\t"
    "v_accvgpr_read_b32 v255, a66\n\t"
    "v_mul_f32 v255, v255, v231\n\t"
    "v_accvgpr_write_b32 a66, v255\n\t"
    "v_accvgpr_read_b32 v253, a67\n\t"
    "v_mul_f32 v253, v253, v231\n\t"
    "v_accvgpr_write_b32 a67, v253\n\t"
    "v_accvgpr_read_b32 v254, a68\n\t"
    "v_mul_f32 v254, v254, v231\n\t"
    "v_accvgpr_write_b32 a68, v254\n\t"
    "v_accvgpr_read_b32 v255, a69\n\t"
    "v_mul_f32 v255, v255, v231\n\t"
    "v_accvgpr_write_b32 a69, v255\n\t"
    "v_accvgpr_read_b32 v253, a70\n\t"
    "v_mul_f32 v253, v253, v231\n\t"
    "v_accvgpr_write_b32 a70, v253\n\t"
    "v_accvgpr_read_b32 v254, a71\n\t"
    "v_mul_f32 v254, v254, v231\n\t"
    "v_accvgpr_write_b32 a71, v254\n\t"
    "v_accvgpr_read_b32 v255, a72\n\t"
    "v_mul_f32 v255, v255, v231\n\t"
    "v_accvgpr_write_b32 a72, v255\n\t"
    "v_accvgpr_read_b32 v253, a73\n\t"
    "v_mul_f32 v253, v253, v231\n\t"
    "v_accvgpr_write_b32 a73, v253\n\t"
    "v_accvgpr_read_b32 v254, a74\n\t"
    "v_mul_f32 v254, v254, v231\n\t"
    "v_accvgpr_write_b32 a74, v254\n\t"
    "v_accvgpr_read_b32 v255, a75\n\t"
    "v_mul_f32 v255, v255, v231\n\t"
    "v_accvgpr_write_b32 a75, v255\n\t"
    "v_accvgpr_read_b32 v253, a76\n\t"
    "v_mul_f32 v253, v253, v231\n\t"
    "v_accvgpr_write_b32 a76, v253\n\t"
    "v_accvgpr_read_b32 v254, a77\n\t"
    "v_mul_f32 v254, v254, v231\n\t"
    "v_accvgpr_write_b32 a77, v254\n\t"
    "v_accvgpr_read_b32 v255, a78\n\t"
    "v_mul_f32 v255, v255, v231\n\t"
    "v_accvgpr_write_b32 a78, v255\n\t"
    "v_accvgpr_read_b32 v253, a79\n\t"
    "v_mul_f32 v253, v253, v231\n\t"
    "v_accvgpr_write_b32 a79, v253\n\t"
    "v_accvgpr_read_b32 v254, a80\n\t"
    "v_mul_f32 v254, v254, v231\n\t"
    "v_accvgpr_write_b32 a80, v254\n\t"
    "v_accvgpr_read_b32 v255, a81\n\t"
    "v_mul_f32 v255, v255, v231\n\t"
    "v_accvgpr_write_b32 a81, v255\n\t"
    "v_accvgpr_read_b32 v253, a82\n\t"
    "v_mul_f32 v253, v253, v231\n\t"
    "v_accvgpr_write_b32 a82, v253\n\t"
    "v_accvgpr_read_b32 v254, a83\n\t"
    "v_mul_f32 v254, v254, v231\n\t"
    "v_accvgpr_write_b32 a83, v254\n\t"
    "v_accvgpr_read_b32 v255, a84\n\t"
    "v_mul_f32 v255, v255, v231\n\t"
    "v_accvgpr_write_b32 a84, v255\n\t"
    "v_accvgpr_read_b32 v253, a85\n\t"
    "v_mul_f32 v253, v253, v231\n\t"
    "v_accvgpr_write_b32 a85, v253\n\t"
    "v_accvgpr_read_b32 v254, a86\n\t"
    "v_mul_f32 v254, v254, v231\n\t"
    "v_accvgpr_write_b32 a86, v254\n\t"
    "v_accvgpr_read_b32 v255, a87\n\t"
    "v_mul_f32 v255, v255, v231\n\t"
    "v_accvgpr_write_b32 a87, v255\n\t"
    "v_accvgpr_read_b32 v253, a88\n\t"
    "v_mul_f32 v253, v253, v231\n\t"
    "v_accvgpr_write_b32 a88, v253\n\t"
    "v_accvgpr_read_b32 v254, a89\n\t"
    "v_mul_f32 v254, v254, v231\n\t"
    "v_accvgpr_write_b32 a89, v254\n\t"
    "v_accvgpr_read_b32 v255, a90\n\t"
    "v_mul_f32 v255, v255, v231\n\t"
    "v_accvgpr_write_b32 a90, v255\n\t"
    "v_accvgpr_read_b32 v253, a91\n\t"
    "v_mul_f32 v253, v253, v231\n\t"
    "v_accvgpr_write_b32 a91, v253\n\t"
    "v_accvgpr_read_b32 v254, a92\n\t"
    "v_mul_f32 v254, v254, v231\n\t"
    "v_accvgpr_write_b32 a92, v254\n\t"
    "v_accvgpr_read_b32 v255, a93\n\t"
    "v_mul_f32 v255, v255, v231\n\t"
    "v_accvgpr_write_b32 a93, v255\n\t"
    "v_accvgpr_read_b32 v253, a94\n\t"
    "v_mul_f32 v253, v253, v231\n\t"
    "v_accvgpr_write_b32 a94, v253\n\t"
    "v_accvgpr_read_b32 v254, a95\n\t"
    "v_mul_f32 v254, v254, v231\n\t"
    "v_accvgpr_write_b32 a95, v254\n\t"
    "v_accvgpr_read_b32 v255, a200\n\t"
    "v_mul_f32 v255, v255, v231\n\t"
    "v_accvgpr_write_b32 a200, v255\n\t"
    "v_accvgpr_read_b32 v253, a201\n\t"
    "v_mul_f32 v253, v253, v231\n\t"
    "v_accvgpr_write_b32 a201, v253\n\t"
    "v_accvgpr_read_b32 v254, a202\n\t"
    "v_mul_f32 v254, v254, v231\n\t"
    "v_accvgpr_write_b32 a202, v254\n\t"
    "v_accvgpr_read_b32 v255, a203\n\t"
    "v_mul_f32 v255, v255, v231\n\t"
    "v_accvgpr_write_b32 a203, v255\n\t"
    "s_nop 3\n\t"
    "s_cmp_eq_u32 s75, 0\n\t"
    "s_cbranch_scc1 .LRET_F_0_%=\n\t"
    "s_branch .LRET_L_0_%=\n\t"
    ".LSTUB_L_1_%=:\n\t"
    "s_mov_b32 s75, 0\n\t"
    "s_branch .LRARE_1_%=\n\t"
    ".LSTUB_Z_1_%=:\n\t"
    "s_mov_b32 s75, 1\n\t"
    "s_branch .LRARE_1_%=\n\t"
    ".LRARE_1_%=:\n\t"
    "s_nop 15\n\t"
    "v_max3_f32 v252, v184, v185, v186\n\t"
    "v_max3_f32 v252, v252, v187, v188\n\t"
    "v_max3_f32 v252, v252, v189, v190\n\t"
    "v_max_f32 v252, v252, v191\n\t"
    "v_mov_b32 v253, v252\n\t"
    "s_nop 1\n\t"
    "v_permlane32_swap_b32 v252, v253\n\t"
    "v_max_f32 v252, v252, v253\n\t"
    "v_mov_b32 v253, v252\n\t"
    "s_nop 1\n\t"
    "v_permlane16_swap_b32 v252, v253\n\t"
    "v_max_f32 v252, v252, v253\n\t"
    "v_max_f32 v252, 0, v252\n\t"
    "v_exp_f32 v231, -v252\n\t"
    "v_sub_f32 v184, v184, v252\n\t"
    "v_sub_f32 v185, v185, v252\n\t"
    "v_sub_f32 v186, v186, v252\n\t"
    "v_sub_f32 v187, v187, v252\n\t"
    "v_sub_f32 v188, v188, v252\n\t"
    "v_sub_f32 v189, v189, v252\n\t"
    "v_sub_f32 v190, v190, v252\n\t"
    "v_sub_f32 v191, v191, v252\n\t"
    "v_sub_f32 v164, v164, v252\n\t"
    "v_sub_f32 v165, v165, v252\n\t"
    "v_sub_f32 v166, v166, v252\n\t"
    "v_sub_f32 v167, v167, v252\n\t"
    "v_accvgpr_read_b32 v253, a96\n\t"
    "v_mul_f32 v253, v253, v231\n\t"
    "v_accvgpr_write_b32 a96, v253\n\t"
    "v_accvgpr_read_b32 v254, a97\n\t"
    "v_mul_f32 v254, v254, v231\n\t"
    "v_accvgpr_write_b32 a97, v254\n\t"
    "v_accvgpr_read_b32 v255, a98\n\t"
    "v_mul_f32 v255, v255, v231\n\t"
    "v_accvgpr_write_b32 a98, v255\n\t"
    "v_accvgpr_read_b32 v253, a99\n\t"
    "v_mul_f32 v253, v253, v231\n\t"
    "v_accvgpr_write_b32 a99, v253\n\t"
    "v_accvgpr_read_b32 v254, a100\n\t"
    "v_mul_f32 v254, v254, v231\n\t"
    "v_accvgpr_write_b32 a100, v254\n\t"
    "v_accvgpr_read_b32 v255, a101\n\t"
    "v_mul_f32 v255, v255, v231\n\t"
    "v_accvgpr_write_b32 a101, v255\n\t"
    "v_accvgpr_read_b32 v253, a102\n\t"
    "v_mul_f32 v253, v253, v231\n\t"
    "v_accvgpr_write_b32 a102, v253\n\t"
    "v_accvgpr_read_b32 v254, a103\n\t"
    "v_mul_f32 v254, v254, v231\n\t"
    "v_accvgpr_write_b32 a103, v254\n\t"
    "v_accvgpr_read_b32 v255, a104\n\t"
    "v_mul_f32 v255, v255, v231\n\t"
    "v_accvgpr_write_b32 a104, v255\n\t"
    "v_accvgpr_read_b32 v253, a105\n\t"
    "v_mul_f32 v253, v253, v231\n\t"
    "v_accvgpr_write_b32 a105, v253\n\t"
    "v_accvgpr_read_b32 v254, a106\n\t"
    "v_mul_f32 v254, v254, v231\n\t"
    "v_accvgpr_write_b32 a106, v254\n\t"
    "v_accvgpr_read_b32 v255, a107\n\t"
    "v_mul_f32 v255, v255, v231\n\t"
    "v_accvgpr_write_b32 a107, v255\n\t"
    "v_accvgpr_read_b32 v253, a108\n\t"
    "v_mul_f32 v253, v253, v231\n\t"
    "v_accvgpr_write_b32 a108, v253\n\t"
    "v_accvgpr_read_b32 v254, a109\n\t"
    "v_mul_f32 v254, v254, v231\n\t"
    "v_accvgpr_write_b32 a109, v254\n\t"
    "v_accvgpr_read_b32 v255, a110\n\t"
    "v_mul_f32 v255, v255, v231\n\t"
    "v_accvgpr_write_b32 a110, v255\n\t"
    "v_accvgpr_read_b32 v253, a111\n\t"
    "v_mul_f32 v253, v253, v231\n\t"
    "v_accvgpr_write_b32 a111, v253\n\t"
    "v_accvgpr_read_b32 v254, a112\n\t"
    "v_mul_f32 v254, v254, v231\n\t"
    "v_accvgpr_write_b32 a112, v254\n\t"
    "v_accvgpr_read_b32 v255, a113\n\t"
    "v_mul_f32 v255, v255, v231\n\t"
    "v_accvgpr_write_b32 a113, v255\n\t"
    "v_accvgpr_read_b32 v253, a114\n\t"
    "v_mul_f32 v253, v253, v231\n\t"
    "v_accvgpr_write_b32 a114, v253\n\t"
    "v_accvgpr_read_b32 v254, a115\n\t"
    "v_mul_f32 v254, v254, v231\n\t"
    "v_accvgpr_write_b32 a115, v254\n\t"
    "v_accvgpr_read_b32 v255, a116\n\t"
    "v_mul_f32 v255, v255, v231\n\t"
    "v_accvgpr_write_b32 a116, v255\n\t"
    "v_accvgpr_read_b32 v253, a117\n\t"
    "v_mul_f32 v253, v253, v231\n\t"
    "v_accvgpr_write_b32 a117, v253\n\t"
    "v_accvgpr_read_b32 v254, a118\n\t"
    "v_mul_f32 v254, v254, v231\n\t"
    "v_accvgpr_write_b32 a118, v254\n\t"
    "v_accvgpr_read_b32 v255, a119\n\t"
    "v_mul_f32 v255, v255, v231\n\t"
    "v_accvgpr_write_b32 a119, v255\n\t"
    "v_accvgpr_read_b32 v253, a120\n\t"
    "v_mul_f32 v253, v253, v231\n\t"
    "v_accvgpr_write_b32 a120, v253\n\t"
    "v_accvgpr_read_b32 v254, a121\n\t"
    "v_mul_f32 v254, v254, v231\n\t"
    "v_accvgpr_write_b32 a121, v254\n\t"
    "v_accvgpr_read_b32 v255, a122\n\t"
    "v_mul_f32 v255, v255, v231\n\t"
    "v_accvgpr_write_b32 a122, v255\n\t"
    "v_accvgpr_read_b32 v253, a123\n\t"
    "v_mul_f32 v253, v253, v231\n\t"
    "v_accvgpr_write_b32 a123, v253\n\t"
    "v_accvgpr_read_b32 v254, a124\n\t"
    "v_mul_f32 v254, v254, v231\n\t"
    "v_accvgpr_write_b32 a124, v254\n\t"
    "v_accvgpr_read_b32 v255, a125\n\t"
    "v_mul_f32 v255, v255, v231\n\t"
    "v_accvgpr_write_b32 a125, v255\n\t"
    "v_accvgpr_read_b32 v253, a126\n\t"
    "v_mul_f32 v253, v253, v231\n\t"
    "v_accvgpr_write_b32 a126, v253\n\t"
    "v_accvgpr_read_b32 v254, a127\n\t"
    "v_mul_f32 v254, v254, v231\n\t"
    "v_accvgpr_write_b32 a127, v254\n\t"
    "v_accvgpr_read_b32 v255, a204\n\t"
    "v_mul_f32 v255, v255, v231\n\t"
    "v_accvgpr_write_b32 a204, v255\n\t"
    "v_accvgpr_read_b32 v253, a205\n\t"
    "v_mul_f32 v253, v253, v231\n\t"
    "v_accvgpr_write_b32 a205, v253\n\t"
    "v_accvgpr_read_b32 v254, a206\n\t"
    "v_mul_f32 v254, v254, v231\n\t"
    "v_accvgpr_write_b32 a206, v254\n\t"
    "v_accvgpr_read_b32 v255, a207\n\t"
    "v_mul_f32 v255, v255, v231\n\t"
    "v_accvgpr_write_b32 a207, v255\n\t"
    "v_max3_f32 v252, v192, v193, v194\n\t"
    "v_max3_f32 v252, v252, v195, v196\n\t"
    "v_max3_f32 v252, v252, v197, v198\n\t"
    "v_max_f32 v252, v252, v199\n\t"
    "v_mov_b32 v253, v252\n\t"
    "s_nop 1\n\t"
    "v_permlane32_swap_b32 v252, v253\n\t"
    "v_max_f32 v252, v252, v253\n\t"
    "v_mov_b32 v253, v252\n\t"
    "s_nop 1\n\t"
    "v_permlane16_swap_b32 v252, v253\n\t"
    "v_max_f32 v252, v252, v253\n\t"
    "v_max_f32 v252, 0, v252\n\t"
    "v_exp_f32 v231, -v252\n\t"
    "v_sub_f32 v192, v192, v252\n\t"
    "v_sub_f32 v193, v193, v252\n\t"
    "v_sub_f32 v194, v194, v252\n\t"
    "v_sub_f32 v195, v195, v252\n\t"
    "v_sub_f32 v196, v196, v252\n\t"
    "v_sub_f32 v197, v197, v252\n\t"
    "v_sub_f32 v198, v198, v252\n\t"
    "v_sub_f32 v199, v199, v252\n\t"
    "v_sub_f32 v168, v168, v252\n\t"
    "v_sub_f32 v169, v169, v252\n\t"
    "v_sub_f32 v170, v170, v252\n\t"
    "v_sub_f32 v171, v171, v252\n\t"
    "v_accvgpr_read_b32 v253, a128\n\t"
    "v_mul_f32 v253, v253, v231\n\t"
    "v_accvgpr_write_b32 a128, v253\n\t"
    "v_accvgpr_read_b32 v254, a129\n\t"
    "v_mul_f32 v254, v254, v231\n\t"
    "v_accvgpr_write_b32 a129, v254\n\t"
    "v_accvgpr_read_b32 v255, a130\n\t"
    "v_mul_f32 v255, v255, v231\n\t"
    "v_accvgpr_write_b32 a130, v255\n\t"
    "v_accvgpr_read_b32 v253, a131\n\t"
    "v_mul_f32 v253, v253, v231\n\t"
    "v_accvgpr_write_b32 a131, v253\n\t"
    "v_accvgpr_read_b32 v254, a132\n\t"
    "v_mul_f32 v254, v254, v231\n\t"
    "v_accvgpr_write_b32 a132, v254\n\t"
    "v_accvgpr_read_b32 v255, a133\n\t"
    "v_mul_f32 v255, v255, v231\n\t"
    "v_accvgpr_write_b32 a133, v255\n\t"
    "v_accvgpr_read_b32 v253, a134\n\t"
    "v_mul_f32 v253, v253, v231\n\t"
    "v_accvgpr_write_b32 a134, v253\n\t"
    "v_accvgpr_read_b32 v254, a135\n\t"
    "v_mul_f32 v254, v254, v231\n\t"
    "v_accvgpr_write_b32 a135, v254\n\t"
    "v_accvgpr_read_b32 v255, a136\n\t"
    "v_mul_f32 v255, v255, v231\n\t"
    "v_accvgpr_write_b32 a136, v255\n\t"
    "v_accvgpr_read_b32 v253, a137\n\t"
    "v_mul_f32 v253, v253, v231\n\t"
    "v_accvgpr_write_b32 a137, v253\n\t"
    "v_accvgpr_read_b32 v254, a138\n\t"
    "v_mul_f32 v254, v254, v231\n\t"
    "v_accvgpr_write_b32 a138, v254\n\t"
    "v_accvgpr_read_b32 v255, a139\n\t"
    "v_mul_f32 v255, v255, v231\n\t"
    "v_accvgpr_write_b32 a139, v255\n\t"
    "v_accvgpr_read_b32 v253, a140\n\t"
    "v_mul_f32 v253, v253, v231\n\t"
    "v_accvgpr_write_b32 a140, v253\n\t"
    "v_accvgpr_read_b32 v254, a141\n\t"
    "v_mul_f32 v254, v254, v231\n\t"
    "v_accvgpr_write_b32 a141, v254\n\t"
    "v_accvgpr_read_b32 v255, a142\n\t"
    "v_mul_f32 v255, v255, v231\n\t"
    "v_accvgpr_write_b32 a142, v255\n\t"
    "v_accvgpr_read_b32 v253, a143\n\t"
    "v_mul_f32 v253, v253, v231\n\t"
    "v_accvgpr_write_b32 a143, v253\n\t"
    "v_accvgpr_read_b32 v254, a144\n\t"
    "v_mul_f32 v254, v254, v231\n\t"
    "v_accvgpr_write_b32 a144, v254\n\t"
    "v_accvgpr_read_b32 v255, a145\n\t"
    "v_mul_f32 v255, v255, v231\n\t"
    "v_accvgpr_write_b32 a145, v255\n\t"
    "v_accvgpr_read_b32 v253, a146\n\t"
    "v_mul_f32 v253, v253, v231\n\t"
    "v_accvgpr_write_b32 a146, v253\n\t"
    "v_accvgpr_read_b32 v254, a147\n\t"
    "v_mul_f32 v254, v254, v231\n\t"
    "v_accvgpr_write_b32 a147, v254\n\t"
    "v_accvgpr_read_b32 v255, a148\n\t"
    "v_mul_f32 v255, v255, v231\n\t"
    "v_accvgpr_write_b32 a148, v255\n\t"
    "v_accvgpr_read_b32 v253, a149\n\t"
    "v_mul_f32 v253, v253, v231\n\t"
    "v_accvgpr_write_b32 a149, v253\n\t"
    "v_accvgpr_read_b32 v254, a150\n\t"
    "v_mul_f32 v254, v254, v231\n\t"
    "v_accvgpr_write_b32 a150, v254\n\t"
    "v_accvgpr_read_b32 v255, a151\n\t"
    "v_mul_f32 v255, v255, v231\n\t"
    "v_accvgpr_write_b32 a151, v255\n\t"
    "v_accvgpr_read_b32 v253, a152\n\t"
    "v_mul_f32 v253, v253, v231\n\t"
    "v_accvgpr_write_b32 a152, v253\n\t"
    "v_accvgpr_read_b32 v254, a153\n\t"
    "v_mul_f32 v254, v254, v231\n\t"
    "v_accvgpr_write_b32 a153, v254\n\t"
    "v_accvgpr_read_b32 v255, a154\n\t"
    "v_mul_f32 v255, v255, v231\n\t"
    "v_accvgpr_write_b32 a154, v255\n\t"
    "v_accvgpr_read_b32 v253, a155\n\t"
    "v_mul_f32 v253, v253, v231\n\t"
    "v_accvgpr_write_b32 a155, v253\n\t"
    "v_accvgpr_read_b32 v254, a156\n\t"
    "v_mul_f32 v254, v254, v231\n\t"
    "v_accvgpr_write_b32 a156, v254\n\t"
    "v_accvgpr_read_b32 v255, a157\n\t"
    "v_mul_f32 v255, v255, v231\n\t"
    "v_accvgpr_write_b32 a157, v255\n\t"
    "v_accvgpr_read_b32 v253, a158\n\t"
    "v_mul_f32 v253, v253, v231\n\t"
    "v_accvgpr_write_b32 a158, v253\n\t"
    "v_accvgpr_read_b32 v254, a159\n\t"
    "v_mul_f32 v254, v254, v231\n\t"
    "v_accvgpr_write_b32 a159, v254\n\t"
    "v_accvgpr_read_b32 v255, a208\n\t"
    "v_mul_f32 v255, v255, v231\n\t"
    "v_accvgpr_write_b32 a208, v255\n\t"
    "v_accvgpr_read_b32 v253, a209\n\t"
    "v_mul_f32 v253, v253, v231\n\t"
    "v_accvgpr_write_b32 a209, v253\n\t"
    "v_accvgpr_read_b32 v254, a210\n\t"
    "v_mul_f32 v254, v254, v231\n\t"
    "v_accvgpr_write_b32 a210, v254\n\t"
    "v_accvgpr_read_b32 v255, a211\n\t"
    "v_mul_f32 v255, v255, v231\n\t"
    "v_accvgpr_write_b32 a211, v255\n\t"
    "v_max3_f32 v252, v200, v201, v202\n\t"
    "v_max3_f32 v252, v252, v203, v248\n\t"
    "v_max3_f32 v252, v252, v249, v250\n\t"
    "v_max_f32 v252, v252, v251\n\t"
    "v_mov_b32 v253, v252\n\t"
    "s_nop 1\n\t"
    "v_permlane32_swap_b32 v252, v253\n\t"
    "v_max_f32 v252, v252, v253\n\t"
    "v_mov_b32 v253, v252\n\t"
    "s_nop 1\n\t"
    "v_permlane16_swap_b32 v252, v253\n\t"
    "v_max_f32 v252, v252, v253\n\t"
    "v_max_f32 v252, 0, v252\n\t"
    "v_exp_f32 v231, -v252\n\t"
    "v_sub_f32 v200, v200, v252\n\t"
    "v_sub_f32 v201, v201, v252\n\t"
    "v_sub_f32 v202, v202, v252\n\t"
    "v_sub_f32 v203, v203, v252\n\t"
    "v_sub_f32 v248, v248, v252\n\t"
    "v_sub_f32 v249, v249, v252\n\t"
    "v_sub_f32 v250, v250, v252\n\t"
    "v_sub_f32 v251, v251, v252\n\t"
    "v_sub_f32 v172, v172, v252\n\t"
    "v_sub_f32 v173, v173, v252\n\t"
    "v_sub_f32 v174, v174, v252\n\t"
    "v_sub_f32 v175, v175, v252\n\t"
    "v_accvgpr_read_b32 v253, a160\n\t"
    "v_mul_f32 v253, v253, v231\n\t"
    "v_accvgpr_write_b32 a160, v253\n\t"
    "v_accvgpr_read_b32 v254, a161\n\t"
    "v_mul_f32 v254, v254, v231\n\t"
    "v_accvgpr_write_b32 a161, v254\n\t"
    "v_accvgpr_read_b32 v255, a162\n\t"
    "v_mul_f32 v255, v255, v231\n\t"
    "v_accvgpr_write_b32 a162, v255\n\t"
    "v_accvgpr_read_b32 v253, a163\n\t"
    "v_mul_f32 v253, v253, v231\n\t"
    "v_accvgpr_write_b32 a163, v253\n\t"
    "v_accvgpr_read_b32 v254, a164\n\t"
    "v_mul_f32 v254, v254, v231\n\t"
    "v_accvgpr_write_b32 a164, v254\n\t"
    "v_accvgpr_read_b32 v255, a165\n\t"
    "v_mul_f32 v255, v255, v231\n\t"
    "v_accvgpr_write_b32 a165, v255\n\t"
    "v_accvgpr_read_b32 v253, a166\n\t"
    "v_mul_f32 v253, v253, v231\n\t"
    "v_accvgpr_write_b32 a166, v253\n\t"
    "v_accvgpr_read_b32 v254, a167\n\t"
    "v_mul_f32 v254, v254, v231\n\t"
    "v_accvgpr_write_b32 a167, v254\n\t"
    "v_accvgpr_read_b32 v255, a168\n\t"
    "v_mul_f32 v255, v255, v231\n\t"
    "v_accvgpr_write_b32 a168, v255\n\t"
    "v_accvgpr_read_b32 v253, a169\n\t"
    "v_mul_f32 v253, v253, v231\n\t"
    "v_accvgpr_write_b32 a169, v253\n\t"
    "v_accvgpr_read_b32 v254, a170\n\t"
    "v_mul_f32 v254, v254, v231\n\t"
    "v_accvgpr_write_b32 a170, v254\n\t"
    "v_accvgpr_read_b32 v255, a171\n\t"
    "v_mul_f32 v255, v255, v231\n\t"
    "v_accvgpr_write_b32 a171, v255\n\t"
    "v_accvgpr_read_b32 v253, a172\n\t"
    "v_mul_f32 v253, v253, v231\n\t"
    "v_accvgpr_write_b32 a172, v253\n\t"
    "v_accvgpr_read_b32 v254, a173\n\t"
    "v_mul_f32 v254, v254, v231\n\t"
    "v_accvgpr_write_b32 a173, v254\n\t"
    "v_accvgpr_read_b32 v255, a174\n\t"
    "v_mul_f32 v255, v255, v231\n\t"
    "v_accvgpr_write_b32 a174, v255\n\t"
    "v_accvgpr_read_b32 v253, a175\n\t"
    "v_mul_f32 v253, v253, v231\n\t"
    "v_accvgpr_write_b32 a175, v253\n\t"
    "v_accvgpr_read_b32 v254, a176\n\t"
    "v_mul_f32 v254, v254, v231\n\t"
    "v_accvgpr_write_b32 a176, v254\n\t"
    "v_accvgpr_read_b32 v255, a177\n\t"
    "v_mul_f32 v255, v255, v231\n\t"
    "v_accvgpr_write_b32 a177, v255\n\t"
    "v_accvgpr_read_b32 v253, a178\n\t"
    "v_mul_f32 v253, v253, v231\n\t"
    "v_accvgpr_write_b32 a178, v253\n\t"
    "v_accvgpr_read_b32 v254, a179\n\t"
    "v_mul_f32 v254, v254, v231\n\t"
    "v_accvgpr_write_b32 a179, v254\n\t"
    "v_accvgpr_read_b32 v255, a180\n\t"
    "v_mul_f32 v255, v255, v231\n\t"
    "v_accvgpr_write_b32 a180, v255\n\t"
    "v_accvgpr_read_b32 v253, a181\n\t"
    "v_mul_f32 v253, v253, v231\n\t"
    "v_accvgpr_write_b32 a181, v253\n\t"
    "v_accvgpr_read_b32 v254, a182\n\t"
    "v_mul_f32 v254, v254, v231\n\t"
    "v_accvgpr_write_b32 a182, v254\n\t"
    "v_accvgpr_read_b32 v255, a183\n\t"
    "v_mul_f32 v255, v255, v231\n\t"
    "v_accvgpr_write_b32 a183, v255\n\t"
    "v_accvgpr_read_b32 v253, a184\n\t"
    "v_mul_f32 v253, v253, v231\n\t"
    "v_accvgpr_write_b32 a184, v253\n\t"
    "v_accvgpr_read_b32 v254, a185\n\t"
    "v_mul_f32 v254, v254, v231\n\t"
    "v_accvgpr_write_b32 a185, v254\n\t"
    "v_accvgpr_read_b32 v255, a186\n\t"
    "v_mul_f32 v255, v255, v231\n\t"
    "v_accvgpr_write_b32 a186, v255\n\t"
    "v_accvgpr_read_b32 v253, a187\n\t"
    "v_mul_f32 v253, v253, v231\n\t"
    "v_accvgpr_write_b32 a187, v253\n\t"
    "v_accvgpr_read_b32 v254, a188\n\t"
    "v_mul_f32 v254, v254, v231\n\t"
    "v_accvgpr_write_b32 a188, v254\n\t"
    "v_accvgpr_read_b32 v255, a189\n\t"
    "v_mul_f32 v255, v255, v231\n\t"
    "v_accvgpr_write_b32 a189, v255\n\t"
    "v_accvgpr_read_b32 v253, a190\n\t"
    "v_mul_f32 v253, v253, v231\n\t"
    "v_accvgpr_write_b32 a190, v253\n\t"
    "v_accvgpr_read_b32 v254, a191\n\t"
    "v_mul_f32 v254, v254, v231\n\t"
    "v_accvgpr_write_b32 a191, v254\n\t"
    "v_accvgpr_read_b32 v255, a212\n\t"
    "v_mul_f32 v255, v255, v231\n\t"
    "v_accvgpr_write_b32 a212, v255\n\t"
    "v_accvgpr_read_b32 v253, a213\n\t"
    "v_mul_f32 v253, v253, v231\n\t"
    "v_accvgpr_write_b32 a213, v253\n\t"
    "v_accvgpr_read_b32 v254, a214\n\t"
    "v_mul_f32 v254, v254, v231\n\t"
    "v_accvgpr_write_b32 a214, v254\n\t"
    "v_accvgpr_read_b32 v255, a215\n\t"
    "v_mul_f32 v255, v255, v231\n\t"
    "v_accvgpr_write_b32 a215, v255\n\t"
    "s_nop 3\n\t"
    "s_cmp_eq_u32 s75, 0\n\t"
    "s_cbranch_scc1 .LRET_L_1_%=\n\t"
    "s_branch .LRET_Z_1_%=\n\t"
    ".LEND_%=:\n\t"
    : "+{s[12:27]}"(s0), "+{s[28:35]}"(s1), "+{s[36:43]}"(sxw), "+{s[44:51]}"(skv), "+{s[52:55]}"(so), "+{s[56:63]}"(sq), "+{s[64:67]}"(sk),
      "+{s[92:95]}"(clk), "+{s[100:101]}"(pwo), "+{v[192:199]}"(vd), "+{v[200:207]}"(va), "+{v[208:223]}"(vb), "+{v[232:247]}"(vc2)
    :
    : "v0","v1","v2","v3","v4","v5","v6","v7","v8","v9","v10","v11","v12","v13","v14","v15","v16","v17","v18","v19","v20","v21","v22","v23","v24","v25","v26","v27","v28","v29","v30","v31","v32","v33","v34","v35","v36","v37","v38","v39","v40","v41","v42","v43","v44","v45","v46","v47","v48","v49","v50","v51","v52","v53","v54","v55","v56","v57","v58","v59","v60","v61","v62","v63","v64","v65","v66","v67","v68","v69","v70","v71","v72","v73","v74","v75","v76","v77","v78","v79","v80","v81","v82","v83","v84","v85","v86","v87","v88","v89","v90","v91","v92","v93","v94","v95","v96","v97","v98","v99","v100","v101","v102","v103","v104","v105","v106","v107","v108","v109","v110","v111","v112","v113","v114","v115","v116","v117","v118","v119","v120","v121","v122","v123","v124","v125","v126","v127","v128","v129","v130","v131","v132","v133","v134","v135","v136","v137","v138","v139","v140","v141","v142","v143","v144","v145","v146","v147","v148","v149","v150","v151","v152","v153","v154","v155","v156","v157","v158","v159","v160","v161","v162","v163","v164","v165","v166","v167","v168","v169","v170","v171","v172","v173","v174","v175","v176","v177","v178","v179","v180","v181","v182","v183","v184","v185","v186","v187","v188","v189","v190","v191","v224","v225","v226","v227","v228","v229","v230","v231","v248","v249","v250","v251","v252","v253","v254","v255","a0","a1","a2","a3","a4","a5","a6","a7","a8","a9","a10","a11","a12","a13","a14","a15","a16","a17","a18","a19","a20","a21","a22","a23","a24","a25","a26","a27","a28","a29","a30","a31","a32","a33","a34","a35","a36","a37","a38","a39","a40","a41","a42","a43","a44","a45","a46","a47","a48","a49","a50","a51","a52","a53","a54","a55","a56","a57","a58","a59","a60","a61","a62","a63","a64","a65","a66","a67","a68","a69","a70","a71","a72","a73","a74","a75","a76","a77","a78","a79","a80","a81","a82","a83","a84","a85","a86","a87","a88","a89","a90","a91","a92","a93","a94","a95","a96","a97","a98","a99","a100","a101","a102","a103","a104","a105","a106","a107","a108","a109","a110","a111","a112","a113","a114","a115","a116","a117","a118","a119","a120","a121","a122","a123","a124","a125","a126","a127","a128","a129","a130","a131","a132","a133","a134","a135","a136","a137","a138","a139","a140","a141","a142","a143","a144","a145","a146","a147","a148","a149","a150","a151","a152","a153","a154","a155","a156","a157","a158","a159","a160","a161","a162","a163","a164","a165","a166","a167","a168","a169","a170","a171","a172","a173","a174","a175","a176","a177","a178","a179","a180","a181","a182","a183","a184","a185","a186","a187","a188","a189","a190","a191","a192","a193","a194","a195","a196","a197","a198","a199","a200","a201","a202","a203","a204","a205","a206","a207","a208","a209","a210","a211","a212","a213","a214","a215","a216","a217","a218","a219","a220","a221","a222","a223","a224","a225","a226","a227","a228","a229","a230","a231","a232","a233","a234","a235","a236","a237","a238","a239","a240","a241","a242","a243","a244","a245","a246","a247","a248","a249","a250","a251","a252","a253","a254","a255","s68","s69","s70","s71","s72","s73","s74","s75","s76","s77","s78","s79","s80","s81","s82","s83","s84","s85","s86","s87","s88","s89","s90","s91","s96","s97","vcc","scc","memory");
}

extern "C" void kernel_launch(void* const* d_in, const int* in_sizes, int n_in, void* d_out, int out_size, void* d_ws, size_t ws_size, hipStream_t stream) {
  (void)in_sizes; (void)n_in; (void)out_size; (void)ws_size;
  (void)hipFuncSetAttribute((const void*)fused_mha_kernel, hipFuncAttributeMaxDynamicSharedMemorySize, 163840);
  fused_mha_kernel<<<256, 256, 163840, stream>>>((const char*)d_in[0], (const char*)d_in[1], (const char*)d_in[2], (const char*)d_in[3],
                                                 (const char*)d_in[4], (char*)d_ws, (char*)d_out );
}
```
